```python
import torch

def _softmax(x, axis=-1):
    return torch.softmax(x, dim=axis)
def _gelu(x):
    return torch.nn.functional.gelu(x, approximate="tanh")
def _silu(x):
    return torch.nn.functional.silu(x)
def _sigmoid(x):
    return torch.sigmoid(x)
def _layernorm(x, axis=-1, eps=1e-5):
    m = x.mean(dim=axis, keepdim=True)
    v = x.var(dim=axis, keepdim=True, unbiased=False)
    return (x - m) / torch.sqrt(v + eps)
def _scatter_add(tgt, idx, src, axis=0):
    return tgt.clone().index_add_(axis, idx, src)
def _segment_sum(src, seg, n):
    out = torch.zeros((n,) + src.shape[1:], dtype=src.dtype)
    return out.index_add_(0, seg, src)
def _pad(x, pw):
    flat = tuple(v for pair in reversed(pw) for v in pair)
    return torch.nn.functional.pad(x, flat)

kernel_name = 'recurrent_scan__gqa_block_cached__glu_gated_bilinear__prompt'

def setup_inputs():

    _w = (lambda a: (lambda t: t.to(torch.bfloat16) if t.is_floating_point() else t)(torch.as_tensor(a)))
    def _A_setup():
        B, Sq, D = 1, 2048, 512
        Hq, Hkv, Sk, Dh = 32, 32, 2048, 128
        assert Hq % Hkv == 0, (Hq, Hkv)
        return {
            "x": _w((lambda *s: torch.randn(*s, dtype=torch.bfloat16) * 0.05)(B, Sq, D)),
            "k_cache": _w((lambda *s: torch.randn(*s, dtype=torch.bfloat16) * 1.6)(B, Hkv, Sk, Dh)),
            "v_cache": _w((lambda *s: torch.randn(*s, dtype=torch.bfloat16) * 1.6)(B, Hkv, Sk, Dh)),
            "wq": _w((lambda *s: torch.randn(*s, dtype=torch.bfloat16) * 1.6)(D, Hq * Dh) * (D ** -0.5)),
            "wo": _w((lambda *s: torch.randn(*s, dtype=torch.bfloat16) * 1.6)(Hq * Dh, D) * ((Hq * Dh) ** -0.5)),
        }
    _A_in = _A_setup()

    def _B_setup():
        B, S, D, F = 1, 2048, 512, 1376
        return {
            "x": _w((lambda *s: torch.randn(*s, dtype=torch.bfloat16) * 0.05)(B, S, D)),
            "w_gate": _w((lambda *s: torch.randn(*s, dtype=torch.bfloat16) * 0.2)(D, F) * (D ** -0.5)),
            "w_up": _w((lambda *s: torch.randn(*s, dtype=torch.bfloat16) * 0.2)(D, F) * (D ** -0.5)),
            "w_down": _w((lambda *s: torch.randn(*s, dtype=torch.bfloat16) * 0.2)(F, D) * (F ** -0.5)),
        }
    _B_in = _B_setup()

    return {
        "A_x": _A_in["x"],
        "A_k_cache": _A_in["k_cache"],
        "A_v_cache": _A_in["v_cache"],
        "A_wq": _A_in["wq"],
        "A_wo": _A_in["wo"],
        "B_x": _B_in["x"],
        "B_w_gate": _B_in["w_gate"],
        "B_w_up": _B_in["w_up"],
        "B_w_down": _B_in["w_down"],
    }

def reference(A_x, A_k_cache, A_v_cache, A_wq, A_wo, B_x, B_w_gate, B_w_up, B_w_down):
    def _A_ref(x, k_cache, v_cache, wq, wo):
        B, Sq, D = x.shape
        Hq, Dh = 32, 128
        Hkv = k_cache.shape[1]
        rep = Hq // Hkv
        q = (lambda x, *d: x.permute(*d))((x @ wq).reshape(B, Sq, Hq, Dh), 0, 2, 1, 3)
        k = (lambda x, n, axis: torch.repeat_interleave(x, n, dim=axis))(k_cache, rep, axis=1)
        v = (lambda x, n, axis: torch.repeat_interleave(x, n, dim=axis))(v_cache, rep, axis=1)
        scores = torch.einsum("bhqd,bhkd->bhqk", q, k) * (Dh ** -0.5)
        p = _softmax(scores, axis=-1)
        o = torch.einsum("bhqk,bhkd->bhqd", p, v)
        return (lambda x, *d: x.permute(*d))(o, 0, 2, 1, 3).reshape(B, Sq, Hq * Dh) @ wo

    def _B_ref(x, w_gate, w_up, w_down):
        g = torch.einsum("bsd,df->bsf", x, w_gate)
        u = torch.einsum("bsd,df->bsf", x, w_up)
        h = g * u
        return torch.einsum("bsf,fd->bsd", h, w_down)

    x = A_x
    xn = _layernorm(x, axis=-1)
    s = _A_ref(xn, A_k_cache, A_v_cache, A_wq, A_wo)
    g = _silu(_B_ref(xn, B_w_gate, B_w_up, B_w_down))
    return x + s * g
```

```cpp
#include <hip/hip_runtime.h>

extern "C" __global__ __launch_bounds__(256, 1) void fused_block(const char* __restrict__ x, const char* __restrict__ k, const char* __restrict__ v,
    const char* __restrict__ wq, const char* __restrict__ wo, const char* __restrict__ wg, const char* __restrict__ wu, const char* __restrict__ wd,
    char* __restrict__ ws, char* __restrict__ out) {
  __shared__ __attribute__((aligned(1024))) char lds[163840];
  unsigned tid = threadIdx.x, bid = blockIdx.x, lb = (unsigned)(size_t)lds;
  unsigned long long ka = (unsigned long long)__builtin_amdgcn_kernarg_segment_ptr();
  asm volatile(
    "s_getpc_b64 s[92:93]\n"
    "L_pc%=:\n"
    "s_load_dwordx2 s[4:5], s[0:1], 0\n"
    "s_load_dwordx2 s[16:17], s[0:1], 24\n"
    "s_load_dwordx2 s[8:9], s[0:1], 8\n"
    "s_load_dwordx2 s[12:13], s[0:1], 16\n"
    "s_load_dwordx2 s[32:33], s[0:1], 32\n"
    "s_load_dwordx2 s[28:29], s[0:1], 56\n"
    "s_load_dwordx2 s[24:25], s[0:1], 64\n"
    "s_load_dwordx2 s[36:37], s[0:1], 72\n"
    "v_lshrrev_b32 v44, 6, %[tid]\n"
    "v_lshlrev_b32 v45, 7, %[tid]\n"
    "s_and_b32 s93, s93, 0xffff\n"
    "s_mov_b32 s94, 8192\n"
    "s_mov_b32 s95, 0x20000\n"
    "buffer_load_dword a221, v45, s[92:95], 0 offen offset:0\n"
    "v_readfirstlane_b32 s64, v44\n"
    "v_and_b32 v1, 63, %[tid]\n"
    "v_mov_b32 v3, %[tid]\n"
    "v_lshlrev_b32 v2, 4, v1\n"
    "s_bfe_u32 s44, s2, 0x30003\n"
    "s_lshr_b32 s84, s2, 6\n"
    "s_and_b32 s80, s84, 3\n"
    "s_lshr_b32 s68, s64, 1\n"
    "s_lshl_b32 s68, s68, 3\n"
    "s_add_u32 s68, s68, 40\n"
    "s_load_dwordx2 s[20:21], s[0:1], s68\n"
    "s_and_b32 s81, s44, 6\n"
    "s_lshl_b32 s68, s81, 5\n"
    "s_lshl_b32 s81, s81, 11\n"
    "s_lshl_b32 s69, s80, 8\n"
    "s_or_b32 s68, s68, s69\n"
    "v_xor_b32 v128, s68, v2\n"
    "s_mov_b32 s82, 0\n"
    "s_bfe_u32 s68, s2, 0x10003\n"
    "s_lshl1_add_u32 s86, s68, s82\n"
    "s_lshl_b32 s82, s86, 14\n"
    "s_or_b32 s82, s82, s81\n"
    "s_lshl_b32 s65, s64, 12\n"
    "s_lshl_b32 s75, s64, 14\n"
    "s_xor_b32 s81, s81, s75\n"
    "s_and_b32 s3, s2, 1\n"
    "s_lshl2_add_u32 s3, s3, s84\n"
    "s_lshl2_add_u32 s40, s3, s64\n"
    "s_lshl_b32 s40, s40, 16\n"
    "v_xor_b32 v129, 16, v128\n"
    "v_xor_b32 v130, 32, v128\n"
    "v_xor_b32 v131, 48, v128\n"
    "s_mov_b32 s6, 0x200000\n"
    "s_mov_b32 s7, 0x20000\n"
    "s_waitcnt lgkmcnt(0)\n"
    "s_and_b32 s5, s5, 0xffff\n"
    "s_xor_b32 m0, s81, 0\n"
    "s_xor_b32 s68, s82, 0xc000\n"
    "s_add_u32 s68, s68, s40\n"
    "buffer_load_dwordx4 v128, s[4:7], s68 offen offset:0 lds\n"
    "v_xor_b32 v132, 64, v128\n"
    "v_xor_b32 v133, 0x50, v128\n"
    "v_xor_b32 v134, 0x60, v128\n"
    "buffer_load_dwordx4 v129, s[4:7], s68 offen offset:1024 lds\n"
    "v_xor_b32 v135, 0x70, v128\n"
    "v_xor_b32 v136, 0x80, v128\n"
    "v_xor_b32 v137, 0x90, v128\n"
    "buffer_load_dwordx4 v130, s[4:7], s68 offen offset:2048 lds\n"
    "v_xor_b32 v138, 0xa0, v128\n"
    "v_xor_b32 v139, 0xb0, v128\n"
    "v_xor_b32 v140, 0xc0, v128\n"
    "buffer_load_dwordx4 v131, s[4:7], s68 offen offset:3072 lds\n"
    "v_xor_b32 v141, 0xd0, v128\n"
    "v_xor_b32 v142, 0xe0, v128\n"
    "v_xor_b32 v143, 0xf0, v128\n"
    "s_xor_b32 m0, s81, 0x1000\n"
    "s_xor_b32 s68, s82, 0xd000\n"
    "s_add_u32 s68, s68, s40\n"
    "buffer_load_dwordx4 v132, s[4:7], s68 offen offset:0 lds\n"
    "buffer_load_dwordx4 v133, s[4:7], s68 offen offset:1024 lds\n"
    "buffer_load_dwordx4 v134, s[4:7], s68 offen offset:2048 lds\n"
    "buffer_load_dwordx4 v135, s[4:7], s68 offen offset:3072 lds\n"
    "s_xor_b32 m0, s81, 0x2000\n"
    "s_xor_b32 s68, s82, 0xe000\n"
    "s_add_u32 s68, s68, s40\n"
    "buffer_load_dwordx4 v136, s[4:7], s68 offen offset:0 lds\n"
    "buffer_load_dwordx4 v137, s[4:7], s68 offen offset:1024 lds\n"
    "buffer_load_dwordx4 v138, s[4:7], s68 offen offset:2048 lds\n"
    "buffer_load_dwordx4 v139, s[4:7], s68 offen offset:3072 lds\n"
    "s_xor_b32 s58, s86, 0\n"
    "s_xor_b32 s59, s86, 1\n"
    "s_xor_b32 s62, s86, 2\n"
    "s_xor_b32 s63, s86, 3\n"
    "v_lshrrev_b32 v40, 4, v1\n"
    "v_and_b32 v41, 15, v1\n"
    "v_add_u32 v44, 0, v40\n"
    "v_xor_b32 v44, v44, v41\n"
    "v_lshlrev_b32 v44, 4, v44\n"
    "v_lshl_add_u32 v22, v41, 10, v44\n"
    "v_add_u32 v22, s75, v22\n"
    "v_add_u32 v44, 4, v40\n"
    "v_xor_b32 v44, v44, v41\n"
    "v_lshlrev_b32 v44, 4, v44\n"
    "v_lshl_add_u32 v23, v41, 10, v44\n"
    "v_add_u32 v23, s75, v23\n"
    "v_add_u32 v44, 8, v40\n"
    "v_xor_b32 v44, v44, v41\n"
    "v_lshlrev_b32 v44, 4, v44\n"
    "v_lshl_add_u32 v24, v41, 10, v44\n"
    "v_add_u32 v24, s75, v24\n"
    "v_add_u32 v44, 12, v40\n"
    "v_xor_b32 v44, v44, v41\n"
    "v_lshlrev_b32 v44, 4, v44\n"
    "v_lshl_add_u32 v25, v41, 10, v44\n"
    "v_add_u32 v25, s75, v25\n"
    "s_mov_b32 s70, 0x3f80\n"
    "s_mov_b32 s71, 0x3f800000\n"
    "s_mov_b32 s72, 0x3f803f80\n"
    "s_bfe_u32 s45, s2, 0x20001\n"
    "s_lshl3_add_u32 s0, s45, s44\n"
    "s_lshr_b32 s66, s0, 1\n"
    "s_mul_i32 s66, s66, 0xc0\n"
    "s_and_b32 s67, s0, 1\n"
    "s_lshl_b32 s87, s67, 6\n"
    "s_add_u32 s66, s66, s87\n"
    "s_sub_u32 s87, 0x158000, s66\n"
    "s_cmp_lt_u32 s0, 29\n"
    "s_cselect_b32 s87, s87, 0\n"
    "s_ashr_i32 s99, s66, 31\n"
    "s_add_u32 s20, s20, s66\n"
    "s_addc_u32 s21, s21, s99\n"
    "s_and_b32 s21, s21, 0xffff\n"
    "s_mov_b32 s22, s87\n"
    "s_mov_b32 s23, 0x20000\n"
    "v_bfe_u32 v42, v1, 2, 2\n"
    "v_and_b32 v43, 3, v1\n"
    "v_bfe_u32 v47, v1, 2, 3\n"
    "v_lshrrev_b32 v48, 5, v1\n"
    "v_xor_b32 v45, s67, v48\n"
    "v_mul_u32_u24 v44, 0xac0, v47\n"
    "v_lshl_add_u32 v44, v45, 6, v44\n"
    "v_lshl_add_u32 v30, v43, 4, v44\n"
    "v_xor_b32 v45, 2, v43\n"
    "v_lshl_add_u32 v45, v45, 4, v44\n"
    "v_add_u32 v31, 0x5200, v45\n"
    "v_add_u32 v32, 0xa400, v30\n"
    "v_add_u32 v33, 0xf600, v45\n"
    "s_and_b32 s66, s64, 1\n"
    "s_mul_i32 s66, s66, 0x2b000\n"
    "s_xor_b32 s76, s80, 0\n"
    "s_mul_i32 s76, s76, 0x56000\n"
    "s_add_u32 s76, s76, s66\n"
    "s_xor_b32 s77, s80, 1\n"
    "s_mul_i32 s77, s77, 0x56000\n"
    "s_add_u32 s77, s77, s66\n"
    "s_xor_b32 s78, s80, 2\n"
    "s_mul_i32 s78, s78, 0x56000\n"
    "s_add_u32 s78, s78, s66\n"
    "s_xor_b32 s79, s80, 3\n"
    "s_mul_i32 s79, s79, 0x56000\n"
    "s_add_u32 s79, s79, s66\n"
    "s_lshl_b32 s88, s64, 13\n"
    "v_lshlrev_b32 v44, 10, v40\n"
    "v_lshl_add_u32 v44, v42, 6, v44\n"
    "v_lshl_add_u32 v45, v43, 3, v44\n"
    "v_and_b32 v46, 1, v40\n"
    "v_lshl_add_u32 v45, v46, 5, v45\n"
    "v_add_u32 v36, 0x10000, v45\n"
    "v_xor_b32 v8, 32, v36\n"
    "v_add_u32 v9, 0x20000, v45\n"
    "v_xor_b32 v21, 32, v9\n"
    "s_lshl2_add_u32 s85, s84, s64\n"
    "s_lshl3_add_u32 s85, s85, s44\n"
    "s_lshl_b32 s85, s85, 10\n"
    "s_mov_b32 s94, L_end%=-L_pc%=\n"
    "v_and_b32 v44, 7, v1\n"
    "v_lshlrev_b32 v44, 7, v44\n"
    "buffer_load_dword a220, v44, s[92:95], s85 offen offset:0\n"
    "s_mov_b32 s86, 0xf0000f\n"
    "s_mov_b32 s87, 0xf0000f00\n"
    "s_mov_b32 s90, 0xaaaaaaaa\n"
    "s_mov_b32 s91, 0xaaaaaaaa\n"
    "s_mov_b32 s96, 0xcccccccc\n"
    "s_mov_b32 s97, 0xcccccccc\n"
    "v_mov_b32 v50, 0x3f80\n"
    "v_mov_b32 v51, 0x3f800000\n"
    "v_cndmask_b32 v50, v50, v51, s[90:91]\n"
    "v_cndmask_b32 v50, 0, v50, s[86:87]\n"
    "v_cndmask_b32 v51, v50, 0, s[96:97]\n"
    "v_cndmask_b32 v52, 0, v50, s[96:97]\n"
    "v_mov_b32 v50, 0x3f803f80\n"
    "v_accvgpr_write_b32 a192, 0\n"
    "v_accvgpr_write_b32 a193, 0\n"
    "v_accvgpr_write_b32 a196, 0\n"
    "v_accvgpr_write_b32 a197, 0\n"
    "v_accvgpr_write_b32 a194, v51\n"
    "v_accvgpr_write_b32 a195, v52\n"
    "v_accvgpr_write_b32 a198, v50\n"
    "v_accvgpr_write_b32 a199, v50\n"
    "v_accvgpr_write_b32 a200, v50\n"
    "v_accvgpr_write_b32 a201, v50\n"
    "s_xor_b32 m0, s81, 0x3000\n"
    "s_xor_b32 s68, s82, 0xf000\n"
    "s_add_u32 s68, s68, s40\n"
    "buffer_load_dwordx4 v140, s[4:7], s68 offen offset:0 lds\n"
    "buffer_load_dwordx4 v141, s[4:7], s68 offen offset:1024 lds\n"
    "buffer_load_dwordx4 v142, s[4:7], s68 offen offset:2048 lds\n"
    "buffer_load_dwordx4 v143, s[4:7], s68 offen offset:3072 lds\n"
    "s_xor_b32 m0, s81, 0x10000\n"
    "s_xor_b32 s68, s82, 0\n"
    "s_add_u32 s68, s68, s40\n"
    "buffer_load_dwordx4 v128, s[4:7], s68 offen offset:0 lds\n"
    "buffer_load_dwordx4 v129, s[4:7], s68 offen offset:1024 lds\n"
    "buffer_load_dwordx4 v130, s[4:7], s68 offen offset:2048 lds\n"
    "buffer_load_dwordx4 v131, s[4:7], s68 offen offset:3072 lds\n"
    "s_xor_b32 m0, s81, 0x11000\n"
    "s_xor_b32 s68, s82, 0x1000\n"
    "s_add_u32 s68, s68, s40\n"
    "buffer_load_dwordx4 v132, s[4:7], s68 offen offset:0 lds\n"
    "buffer_load_dwordx4 v133, s[4:7], s68 offen offset:1024 lds\n"
    "buffer_load_dwordx4 v134, s[4:7], s68 offen offset:2048 lds\n"
    "buffer_load_dwordx4 v135, s[4:7], s68 offen offset:3072 lds\n"
    "s_xor_b32 m0, s81, 0x12000\n"
    "s_xor_b32 s68, s82, 0x2000\n"
    "s_add_u32 s68, s68, s40\n"
    "buffer_load_dwordx4 v136, s[4:7], s68 offen offset:0 lds\n"
    "buffer_load_dwordx4 v137, s[4:7], s68 offen offset:1024 lds\n"
    "s_waitcnt vmcnt(10)\n"
    "ds_read_b128 a[64:67], v22 offset:0\n"
    "ds_read_b128 a[68:71], v23 offset:0\n"
    "ds_read_b128 a[72:75], v24 offset:0\n"
    "ds_read_b128 a[76:79], v25 offset:0\n"
    "ds_read_b128 a[80:83], v22 offset:256\n"
    "ds_read_b128 a[84:87], v23 offset:256\n"
    "ds_read_b128 a[88:91], v24 offset:256\n"
    "ds_read_b128 a[92:95], v25 offset:256\n"
    "ds_read_b128 a[96:99], v22 offset:512\n"
    "ds_read_b128 a[100:103], v23 offset:512\n"
    "ds_read_b128 a[104:107], v24 offset:512\n"
    "ds_read_b128 a[108:111], v25 offset:512\n"
    "ds_read_b128 a[112:115], v22 offset:768\n"
    "ds_read_b128 a[116:119], v23 offset:768\n"
    "ds_read_b128 a[120:123], v24 offset:768\n"
    "ds_read_b128 a[124:127], v25 offset:768\n"
    "v_xor_b32 v22, 0x10000, v22\n"
    "v_xor_b32 v23, 0x10000, v23\n"
    "v_xor_b32 v24, 0x10000, v24\n"
    "v_xor_b32 v25, 0x10000, v25\n"
    "buffer_load_dwordx4 v138, s[4:7], s68 offen offset:2048 lds\n"
    "buffer_load_dwordx4 v139, s[4:7], s68 offen offset:3072 lds\n"
    "s_waitcnt lgkmcnt(0)\n"
    "v_mfma_f32_16x16x32_bf16 v[4:7], a[198:201], a[64:67], 0\n"
    "s_xor_b32 m0, s81, 0x13000\n"
    "s_xor_b32 s68, s82, 0x3000\n"
    "s_add_u32 s68, s68, s40\n"
    "buffer_load_dwordx4 v140, s[4:7], s68 offen offset:0 lds\n"
    "v_mfma_f32_16x16x32_bf16 v[4:7], a[198:201], a[68:71], v[4:7]\n"
    "v_mfma_f32_16x16x32_bf16 v[4:7], a[198:201], a[72:75], v[4:7]\n"
    "buffer_load_dwordx4 v141, s[4:7], s68 offen offset:1024 lds\n"
    "v_mfma_f32_16x16x32_bf16 v[4:7], a[198:201], a[76:79], v[4:7]\n"
    "v_mfma_f32_16x16x32_bf16 v[4:7], a[198:201], a[80:83], v[4:7]\n"
    "buffer_load_dwordx4 v142, s[4:7], s68 offen offset:2048 lds\n"
    "v_mfma_f32_16x16x32_bf16 v[4:7], a[198:201], a[84:87], v[4:7]\n"
    "v_mfma_f32_16x16x32_bf16 v[4:7], a[198:201], a[88:91], v[4:7]\n"
    "buffer_load_dwordx4 v143, s[4:7], s68 offen offset:3072 lds\n"
    "v_mfma_f32_16x16x32_bf16 v[4:7], a[198:201], a[92:95], v[4:7]\n"
    "v_mfma_f32_16x16x32_bf16 v[4:7], a[198:201], a[96:99], v[4:7]\n"
    "s_xor_b32 m0, s81, 0\n"
    "s_xor_b32 s68, s82, 0x4000\n"
    "s_add_u32 s68, s68, s40\n"
    "buffer_load_dwordx4 v128, s[4:7], s68 offen offset:0 lds\n"
    "v_mfma_f32_16x16x32_bf16 v[4:7], a[198:201], a[100:103], v[4:7]\n"
    "v_mfma_f32_16x16x32_bf16 v[4:7], a[198:201], a[104:107], v[4:7]\n"
    "buffer_load_dwordx4 v129, s[4:7], s68 offen offset:1024 lds\n"
    "v_mfma_f32_16x16x32_bf16 v[4:7], a[198:201], a[108:111], v[4:7]\n"
    "v_mfma_f32_16x16x32_bf16 v[4:7], a[198:201], a[112:115], v[4:7]\n"
    "v_mfma_f32_16x16x32_bf16 v[4:7], a[198:201], a[116:119], v[4:7]\n"
    "v_mfma_f32_16x16x32_bf16 v[4:7], a[198:201], a[120:123], v[4:7]\n"
    "v_mfma_f32_16x16x32_bf16 v[4:7], a[198:201], a[124:127], v[4:7]\n"
    "s_nop 7\n"
    "v_mul_f32 v20, 0x3b000000, v4\n"
    "v_cvt_pk_bf16_f32 v34, v20, v20\n"
    "v_lshlrev_b32 v34, 16, v34\n"
    "v_sub_f32 v20, v20, v34\n"
    "v_sub_f32 v4, 0, v34\n"
    "v_sub_f32 v5, 0, v34\n"
    "v_sub_f32 v6, 0, v34\n"
    "v_sub_f32 v7, 0, v34\n"
    "s_nop 1\n"
    "v_mfma_f32_16x16x32_bf16 v[40:43], a[194:197], a[64:67], v[4:7]\n"
    "v_mfma_f32_16x16x32_bf16 v[44:47], a[192:195], a[64:67], v[4:7]\n"
    "buffer_load_dwordx4 v130, s[4:7], s68 offen offset:2048 lds\n"
    "v_mfma_f32_16x16x32_bf16 v[48:51], a[194:197], a[68:71], v[4:7]\n"
    "v_mfma_f32_16x16x32_bf16 v[52:55], a[192:195], a[68:71], v[4:7]\n"
    "v_mfma_f32_16x16x32_bf16 v[56:59], a[194:197], a[72:75], v[4:7]\n"
    "buffer_load_dwordx4 v131, s[4:7], s68 offen offset:3072 lds\n"
    "v_mfma_f32_16x16x32_bf16 v[60:63], a[192:195], a[72:75], v[4:7]\n"
    "s_nop 0\n"
    "v_cvt_pk_bf16_f32 v192, v40, v41\n"
    "v_cvt_pk_bf16_f32 v193, v42, v43\n"
    "v_cvt_pk_bf16_f32 v194, v44, v45\n"
    "v_cvt_pk_bf16_f32 v195, v46, v47\n"
    "v_mfma_f32_16x16x32_bf16 v[40:43], a[194:197], a[76:79], v[4:7]\n"
    "s_waitcnt vmcnt(4)\n"
    "ds_read_b128 a[128:131], v22 offset:0\n"
    "ds_read_b128 a[132:135], v23 offset:0\n"
    "v_mfma_f32_16x16x32_bf16 v[44:47], a[192:195], a[76:79], v[4:7]\n"
    "ds_read_b128 a[136:139], v24 offset:0\n"
    "ds_read_b128 a[140:143], v25 offset:0\n"
    "s_xor_b32 m0, s81, 0x1000\n"
    "s_xor_b32 s68, s82, 0x5000\n"
    "s_add_u32 s68, s68, s40\n"
    "buffer_load_dwordx4 v132, s[4:7], s68 offen offset:0 lds\n"
    "v_mfma_f32_16x16x32_bf16 v[14:17], v[192:195], v[192:195], 0\n"
    "ds_read_b128 a[144:147], v22 offset:256\n"
    "ds_read_b128 a[148:151], v23 offset:256\n"
    "v_cvt_pk_bf16_f32 v196, v48, v49\n"
    "v_cvt_pk_bf16_f32 v197, v50, v51\n"
    "v_cvt_pk_bf16_f32 v198, v52, v53\n"
    "v_cvt_pk_bf16_f32 v199, v54, v55\n"
    "v_mfma_f32_16x16x32_bf16 v[48:51], a[194:197], a[80:83], v[4:7]\n"
    "ds_read_b128 a[152:155], v24 offset:256\n"
    "ds_read_b128 a[156:159], v25 offset:256\n"
    "v_mfma_f32_16x16x32_bf16 v[52:55], a[192:195], a[80:83], v[4:7]\n"
    "ds_read_b128 a[160:163], v22 offset:512\n"
    "ds_read_b128 a[164:167], v23 offset:512\n"
    "buffer_load_dwordx4 v133, s[4:7], s68 offen offset:1024 lds\n"
    "v_cvt_pk_bf16_f32 v200, v56, v57\n"
    "v_mfma_f32_16x16x32_bf16 v[14:17], v[196:199], v[196:199], v[14:17]\n"
    "ds_read_b128 a[168:171], v24 offset:512\n"
    "ds_read_b128 a[172:175], v25 offset:512\n"
    "v_cvt_pk_bf16_f32 v201, v58, v59\n"
    "v_cvt_pk_bf16_f32 v202, v60, v61\n"
    "v_cvt_pk_bf16_f32 v203, v62, v63\n"
    "v_mfma_f32_16x16x32_bf16 v[56:59], a[194:197], a[84:87], v[4:7]\n"
    "ds_read_b128 a[176:179], v22 offset:768\n"
    "ds_read_b128 a[180:183], v23 offset:768\n"
    "v_cvt_pk_bf16_f32 v204, v40, v41\n"
    "v_mfma_f32_16x16x32_bf16 v[60:63], a[192:195], a[84:87], v[4:7]\n"
    "ds_read_b128 a[184:187], v24 offset:768\n"
    "ds_read_b128 a[188:191], v25 offset:768\n"
    "buffer_load_dwordx4 v134, s[4:7], s68 offen offset:2048 lds\n"
    "v_cvt_pk_bf16_f32 v205, v42, v43\n"
    "v_mfma_f32_16x16x32_bf16 v[14:17], v[200:203], v[200:203], v[14:17]\n"
    "v_xor_b32 v22, 0x10000, v22\n"
    "v_cvt_pk_bf16_f32 v206, v44, v45\n"
    "v_cvt_pk_bf16_f32 v207, v46, v47\n"
    "v_mfma_f32_16x16x32_bf16 v[40:43], a[194:197], a[88:91], v[4:7]\n"
    "v_xor_b32 v23, 0x10000, v23\n"
    "v_cvt_pk_bf16_f32 v208, v48, v49\n"
    "v_mfma_f32_16x16x32_bf16 v[44:47], a[192:195], a[88:91], v[4:7]\n"
    "v_xor_b32 v24, 0x10000, v24\n"
    "buffer_load_dwordx4 v135, s[4:7], s68 offen offset:3072 lds\n"
    "v_cvt_pk_bf16_f32 v209, v50, v51\n"
    "v_mfma_f32_16x16x32_bf16 v[14:17], v[204:207], v[204:207], v[14:17]\n"
    "v_xor_b32 v25, 0x10000, v25\n"
    "v_cvt_pk_bf16_f32 v210, v52, v53\n"
    "v_cvt_pk_bf16_f32 v211, v54, v55\n"
    "v_mfma_f32_16x16x32_bf16 v[48:51], a[194:197], a[92:95], v[4:7]\n"
    "s_waitcnt lgkmcnt(0)\n"
    "v_cvt_pk_bf16_f32 v212, v56, v57\n"
    "v_cvt_pk_bf16_f32 v213, v58, v59\n"
    "v_mfma_f32_16x16x32_bf16 v[52:55], a[192:195], a[92:95], v[4:7]\n"
    "s_xor_b32 m0, s81, 0x2000\n"
    "s_xor_b32 s68, s82, 0x6000\n"
    "s_add_u32 s68, s68, s40\n"
    "buffer_load_dwordx4 v136, s[4:7], s68 offen offset:0 lds\n"
    "v_cvt_pk_bf16_f32 v214, v60, v61\n"
    "v_mfma_f32_16x16x32_bf16 v[10:13], a[198:201], a[128:131], 0\n"
    "v_cvt_pk_bf16_f32 v215, v62, v63\n"
    "v_cvt_pk_bf16_f32 v216, v40, v41\n"
    "v_mfma_f32_16x16x32_bf16 v[10:13], a[198:201], a[132:135], v[10:13]\n"
    "v_cvt_pk_bf16_f32 v217, v42, v43\n"
    "v_cvt_pk_bf16_f32 v218, v44, v45\n"
    "v_mfma_f32_16x16x32_bf16 v[56:59], a[194:197], a[96:99], v[4:7]\n"
    "buffer_load_dwordx4 v137, s[4:7], s68 offen offset:1024 lds\n"
    "v_cvt_pk_bf16_f32 v219, v46, v47\n"
    "v_mfma_f32_16x16x32_bf16 v[60:63], a[192:195], a[96:99], v[4:7]\n"
    "v_cvt_pk_bf16_f32 v220, v48, v49\n"
    "v_cvt_pk_bf16_f32 v221, v50, v51\n"
    "v_mfma_f32_16x16x32_bf16 v[10:13], a[198:201], a[136:139], v[10:13]\n"
    "v_cvt_pk_bf16_f32 v222, v52, v53\n"
    "v_cvt_pk_bf16_f32 v223, v54, v55\n"
    "v_mfma_f32_16x16x32_bf16 v[10:13], a[198:201], a[140:143], v[10:13]\n"
    "buffer_load_dwordx4 v138, s[4:7], s68 offen offset:2048 lds\n"
    "v_mfma_f32_16x16x32_bf16 v[40:43], a[194:197], a[100:103], v[4:7]\n"
    "v_cvt_pk_bf16_f32 v224, v56, v57\n"
    "v_cvt_pk_bf16_f32 v225, v58, v59\n"
    "v_mfma_f32_16x16x32_bf16 v[44:47], a[192:195], a[100:103], v[4:7]\n"
    "v_cvt_pk_bf16_f32 v226, v60, v61\n"
    "v_cvt_pk_bf16_f32 v227, v62, v63\n"
    "v_mfma_f32_16x16x32_bf16 v[10:13], a[198:201], a[144:147], v[10:13]\n"
    "buffer_load_dwordx4 v139, s[4:7], s68 offen offset:3072 lds\n"
    "v_mfma_f32_16x16x32_bf16 v[10:13], a[198:201], a[148:151], v[10:13]\n"
    "v_accvgpr_write_b32 a0, v192\n"
    "v_accvgpr_write_b32 a1, v193\n"
    "v_mfma_f32_16x16x32_bf16 v[48:51], a[194:197], a[104:107], v[4:7]\n"
    "v_cvt_pk_bf16_f32 v228, v40, v41\n"
    "v_cvt_pk_bf16_f32 v229, v42, v43\n"
    "v_mfma_f32_16x16x32_bf16 v[52:55], a[192:195], a[104:107], v[4:7]\n"
    "s_xor_b32 m0, s81, 0x3000\n"
    "s_xor_b32 s68, s82, 0x7000\n"
    "s_add_u32 s68, s68, s40\n"
    "buffer_load_dwordx4 v140, s[4:7], s68 offen offset:0 lds\n"
    "v_mfma_f32_16x16x32_bf16 v[10:13], a[198:201], a[152:155], v[10:13]\n"
    "v_cvt_pk_bf16_f32 v230, v44, v45\n"
    "v_cvt_pk_bf16_f32 v231, v46, v47\n"
    "v_mfma_f32_16x16x32_bf16 v[10:13], a[198:201], a[156:159], v[10:13]\n"
    "v_accvgpr_write_b32 a2, v194\n"
    "v_accvgpr_write_b32 a3, v195\n"
    "v_mfma_f32_16x16x32_bf16 v[56:59], a[194:197], a[108:111], v[4:7]\n"
    "buffer_load_dwordx4 v141, s[4:7], s68 offen offset:1024 lds\n"
    "v_mfma_f32_16x16x32_bf16 v[60:63], a[192:195], a[108:111], v[4:7]\n"
    "v_cvt_pk_bf16_f32 v232, v48, v49\n"
    "v_cvt_pk_bf16_f32 v233, v50, v51\n"
    "v_mfma_f32_16x16x32_bf16 v[10:13], a[198:201], a[160:163], v[10:13]\n"
    "v_cvt_pk_bf16_f32 v234, v52, v53\n"
    "v_cvt_pk_bf16_f32 v235, v54, v55\n"
    "v_mfma_f32_16x16x32_bf16 v[10:13], a[198:201], a[164:167], v[10:13]\n"
    "buffer_load_dwordx4 v142, s[4:7], s68 offen offset:2048 lds\n"
    "v_mfma_f32_16x16x32_bf16 v[40:43], a[194:197], a[112:115], v[4:7]\n"
    "v_cvt_pk_bf16_f32 v236, v56, v57\n"
    "v_cvt_pk_bf16_f32 v237, v58, v59\n"
    "v_mfma_f32_16x16x32_bf16 v[44:47], a[192:195], a[112:115], v[4:7]\n"
    "v_cvt_pk_bf16_f32 v238, v60, v61\n"
    "v_cvt_pk_bf16_f32 v239, v62, v63\n"
    "v_mfma_f32_16x16x32_bf16 v[10:13], a[198:201], a[168:171], v[10:13]\n"
    "buffer_load_dwordx4 v143, s[4:7], s68 offen offset:3072 lds\n"
    "v_mfma_f32_16x16x32_bf16 v[10:13], a[198:201], a[172:175], v[10:13]\n"
    "v_accvgpr_write_b32 a4, v196\n"
    "v_accvgpr_write_b32 a5, v197\n"
    "v_mfma_f32_16x16x32_bf16 v[48:51], a[194:197], a[116:119], v[4:7]\n"
    "v_cvt_pk_bf16_f32 v240, v40, v41\n"
    "v_cvt_pk_bf16_f32 v241, v42, v43\n"
    "v_mfma_f32_16x16x32_bf16 v[52:55], a[192:195], a[116:119], v[4:7]\n"
    "s_xor_b32 m0, s81, 0x10000\n"
    "s_xor_b32 s68, s82, 0x8000\n"
    "s_add_u32 s68, s68, s40\n"
    "buffer_load_dwordx4 v128, s[4:7], s68 offen offset:0 lds\n"
    "v_mfma_f32_16x16x32_bf16 v[10:13], a[198:201], a[176:179], v[10:13]\n"
    "v_cvt_pk_bf16_f32 v242, v44, v45\n"
    "v_cvt_pk_bf16_f32 v243, v46, v47\n"
    "v_mfma_f32_16x16x32_bf16 v[10:13], a[198:201], a[180:183], v[10:13]\n"
    "v_accvgpr_write_b32 a6, v198\n"
    "v_accvgpr_write_b32 a7, v199\n"
    "v_mfma_f32_16x16x32_bf16 v[56:59], a[194:197], a[120:123], v[4:7]\n"
    "buffer_load_dwordx4 v129, s[4:7], s68 offen offset:1024 lds\n"
    "v_mfma_f32_16x16x32_bf16 v[60:63], a[192:195], a[120:123], v[4:7]\n"
    "v_cvt_pk_bf16_f32 v244, v48, v49\n"
    "v_cvt_pk_bf16_f32 v245, v50, v51\n"
    "v_mfma_f32_16x16x32_bf16 v[10:13], a[198:201], a[184:187], v[10:13]\n"
    "v_cvt_pk_bf16_f32 v246, v52, v53\n"
    "v_cvt_pk_bf16_f32 v247, v54, v55\n"
    "v_mfma_f32_16x16x32_bf16 v[10:13], a[198:201], a[188:191], v[10:13]\n"
    "buffer_load_dwordx4 v130, s[4:7], s68 offen offset:2048 lds\n"
    "v_mfma_f32_16x16x32_bf16 v[40:43], a[194:197], a[124:127], v[4:7]\n"
    "v_cvt_pk_bf16_f32 v248, v56, v57\n"
    "v_cvt_pk_bf16_f32 v249, v58, v59\n"
    "v_mfma_f32_16x16x32_bf16 v[44:47], a[192:195], a[124:127], v[4:7]\n"
    "v_cvt_pk_bf16_f32 v250, v60, v61\n"
    "v_cvt_pk_bf16_f32 v251, v62, v63\n"
    "v_mfma_f32_16x16x32_bf16 v[14:17], v[208:211], v[208:211], v[14:17]\n"
    "buffer_load_dwordx4 v131, s[4:7], s68 offen offset:3072 lds\n"
    "v_mfma_f32_16x16x32_bf16 v[14:17], v[212:215], v[212:215], v[14:17]\n"
    "v_mul_f32 v37, 0x3b000000, v10\n"
    "v_cvt_pk_bf16_f32 v34, v37, v37\n"
    "v_lshlrev_b32 v34, 16, v34\n"
    "v_sub_f32 v37, v37, v34\n"
    "v_sub_f32 v10, 0, v34\n"
    "v_sub_f32 v11, 0, v34\n"
    "v_sub_f32 v12, 0, v34\n"
    "v_sub_f32 v13, 0, v34\n"
    "s_nop 1\n"
    "v_mfma_f32_16x16x32_bf16 v[48:51], a[194:197], a[128:131], v[10:13]\n"
    "v_cvt_pk_bf16_f32 v252, v40, v41\n"
    "v_cvt_pk_bf16_f32 v253, v42, v43\n"
    "v_mfma_f32_16x16x32_bf16 v[52:55], a[192:195], a[128:131], v[10:13]\n"
    "s_xor_b32 m0, s81, 0x11000\n"
    "s_xor_b32 s68, s82, 0x9000\n"
    "s_add_u32 s68, s68, s40\n"
    "buffer_load_dwordx4 v132, s[4:7], s68 offen offset:0 lds\n"
    "v_mfma_f32_16x16x32_bf16 v[14:17], v[216:219], v[216:219], v[14:17]\n"
    "v_cvt_pk_bf16_f32 v254, v44, v45\n"
    "v_cvt_pk_bf16_f32 v255, v46, v47\n"
    "v_mfma_f32_16x16x32_bf16 v[14:17], v[220:223], v[220:223], v[14:17]\n"
    "v_accvgpr_write_b32 a8, v200\n"
    "v_accvgpr_write_b32 a9, v201\n"
    "v_mfma_f32_16x16x32_bf16 v[56:59], a[194:197], a[132:135], v[10:13]\n"
    "buffer_load_dwordx4 v133, s[4:7], s68 offen offset:1024 lds\n"
    "v_mfma_f32_16x16x32_bf16 v[60:63], a[192:195], a[132:135], v[10:13]\n"
    "v_cvt_pk_bf16_f32 v64, v48, v49\n"
    "v_cvt_pk_bf16_f32 v65, v50, v51\n"
    "v_mfma_f32_16x16x32_bf16 v[14:17], v[224:227], v[224:227], v[14:17]\n"
    "s_waitcnt vmcnt(6)\n"
    "ds_read_b128 a[64:67], v22 offset:0\n"
    "ds_read_b128 a[68:71], v23 offset:0\n"
    "v_cvt_pk_bf16_f32 v66, v52, v53\n"
    "v_mfma_f32_16x16x32_bf16 v[14:17], v[228:231], v[228:231], v[14:17]\n"
    "buffer_load_dwordx4 v134, s[4:7], s68 offen offset:2048 lds\n"
    "ds_read_b128 a[72:75], v24 offset:0\n"
    "ds_read_b128 a[76:79], v25 offset:0\n"
    "v_cvt_pk_bf16_f32 v67, v54, v55\n"
    "v_mfma_f32_16x16x32_bf16 v[40:43], a[194:197], a[136:139], v[10:13]\n"
    "ds_read_b128 a[80:83], v22 offset:256\n"
    "ds_read_b128 a[84:87], v23 offset:256\n"
    "v_mfma_f32_16x16x32_bf16 v[44:47], a[192:195], a[136:139], v[10:13]\n"
    "ds_read_b128 a[88:91], v24 offset:256\n"
    "ds_read_b128 a[92:95], v25 offset:256\n"
    "v_accvgpr_write_b32 a10, v202\n"
    "v_mfma_f32_16x16x32_bf16 v[14:17], v[232:235], v[232:235], v[14:17]\n"
    "buffer_load_dwordx4 v135, s[4:7], s68 offen offset:3072 lds\n"
    "ds_read_b128 a[96:99], v22 offset:512\n"
    "ds_read_b128 a[100:103], v23 offset:512\n"
    "v_cvt_pk_bf16_f32 v68, v56, v57\n"
    "v_mfma_f32_16x16x32_bf16 v[14:17], v[236:239], v[236:239], v[14:17]\n"
    "ds_read_b128 a[104:107], v24 offset:512\n"
    "ds_read_b128 a[108:111], v25 offset:512\n"
    "v_cvt_pk_bf16_f32 v69, v58, v59\n"
    "v_mfma_f32_16x16x32_bf16 v[48:51], a[194:197], a[140:143], v[10:13]\n"
    "ds_read_b128 a[112:115], v22 offset:768\n"
    "ds_read_b128 a[116:119], v23 offset:768\n"
    "v_cvt_pk_bf16_f32 v70, v60, v61\n"
    "v_mfma_f32_16x16x32_bf16 v[52:55], a[192:195], a[140:143], v[10:13]\n"
    "s_xor_b32 m0, s81, 0x12000\n"
    "s_xor_b32 s68, s82, 0xa000\n"
    "s_add_u32 s68, s68, s40\n"
    "buffer_load_dwordx4 v136, s[4:7], s68 offen offset:0 lds\n"
    "ds_read_b128 a[120:123], v24 offset:768\n"
    "ds_read_b128 a[124:127], v25 offset:768\n"
    "v_cvt_pk_bf16_f32 v71, v62, v63\n"
    "v_mfma_f32_16x16x32_bf16 v[14:17], v[240:243], v[240:243], v[14:17]\n"
    "v_xor_b32 v22, 0x10000, v22\n"
    "v_cvt_pk_bf16_f32 v72, v40, v41\n"
    "v_mfma_f32_16x16x32_bf16 v[14:17], v[244:247], v[244:247], v[14:17]\n"
    "v_xor_b32 v23, 0x10000, v23\n"
    "v_cvt_pk_bf16_f32 v73, v42, v43\n"
    "v_accvgpr_write_b32 a11, v203\n"
    "v_mfma_f32_16x16x32_bf16 v[56:59], a[194:197], a[144:147], v[10:13]\n"
    "buffer_load_dwordx4 v137, s[4:7], s68 offen offset:1024 lds\n"
    "v_xor_b32 v24, 0x10000, v24\n"
    "v_cvt_pk_bf16_f32 v74, v44, v45\n"
    "v_mfma_f32_16x16x32_bf16 v[60:63], a[192:195], a[144:147], v[10:13]\n"
    "v_xor_b32 v25, 0x10000, v25\n"
    "v_cvt_pk_bf16_f32 v75, v46, v47\n"
    "v_accvgpr_write_b32 a12, v204\n"
    "v_mfma_f32_16x16x32_bf16 v[14:17], v[248:251], v[248:251], v[14:17]\n"
    "s_waitcnt lgkmcnt(0)\n"
    "v_cvt_pk_bf16_f32 v76, v48, v49\n"
    "v_cvt_pk_bf16_f32 v77, v50, v51\n"
    "v_mfma_f32_16x16x32_bf16 v[4:7], a[198:201], a[64:67], 0\n"
    "buffer_load_dwordx4 v138, s[4:7], s68 offen offset:2048 lds\n"
    "v_cvt_pk_bf16_f32 v78, v52, v53\n"
    "v_mfma_f32_16x16x32_bf16 v[40:43], a[194:197], a[148:151], v[10:13]\n"
    "v_cvt_pk_bf16_f32 v79, v54, v55\n"
    "v_cvt_pk_bf16_f32 v80, v56, v57\n"
    "v_mfma_f32_16x16x32_bf16 v[44:47], a[192:195], a[148:151], v[10:13]\n"
    "v_cvt_pk_bf16_f32 v81, v58, v59\n"
    "v_cvt_pk_bf16_f32 v82, v60, v61\n"
    "v_accvgpr_write_b32 a13, v205\n"
    "v_mfma_f32_16x16x32_bf16 v[4:7], a[198:201], a[68:71], v[4:7]\n"
    "buffer_load_dwordx4 v139, s[4:7], s68 offen offset:3072 lds\n"
    "v_cvt_pk_bf16_f32 v83, v62, v63\n"
    "v_mfma_f32_16x16x32_bf16 v[4:7], a[198:201], a[72:75], v[4:7]\n"
    "v_accvgpr_write_b32 a14, v206\n"
    "v_accvgpr_write_b32 a15, v207\n"
    "v_accvgpr_write_b32 a16, v208\n"
    "v_mfma_f32_16x16x32_bf16 v[48:51], a[194:197], a[152:155], v[10:13]\n"
    "v_cvt_pk_bf16_f32 v84, v40, v41\n"
    "v_cvt_pk_bf16_f32 v85, v42, v43\n"
    "v_mfma_f32_16x16x32_bf16 v[52:55], a[192:195], a[152:155], v[10:13]\n"
    "s_xor_b32 m0, s81, 0x13000\n"
    "s_xor_b32 s68, s82, 0xb000\n"
    "s_add_u32 s68, s68, s40\n"
    "buffer_load_dwordx4 v140, s[4:7], s68 offen offset:0 lds\n"
    "v_accvgpr_write_b32 a17, v209\n"
    "v_mfma_f32_16x16x32_bf16 v[4:7], a[198:201], a[76:79], v[4:7]\n"
    "v_cvt_pk_bf16_f32 v86, v44, v45\n"
    "v_cvt_pk_bf16_f32 v87, v46, v47\n"
    "v_mfma_f32_16x16x32_bf16 v[4:7], a[198:201], a[80:83], v[4:7]\n"
    "v_accvgpr_write_b32 a18, v210\n"
    "v_accvgpr_write_b32 a19, v211\n"
    "v_accvgpr_write_b32 a20, v212\n"
    "v_mfma_f32_16x16x32_bf16 v[56:59], a[194:197], a[156:159], v[10:13]\n"
    "buffer_load_dwordx4 v141, s[4:7], s68 offen offset:1024 lds\n"
    "v_mfma_f32_16x16x32_bf16 v[60:63], a[192:195], a[156:159], v[10:13]\n"
    "v_cvt_pk_bf16_f32 v88, v48, v49\n"
    "v_cvt_pk_bf16_f32 v89, v50, v51\n"
    "v_accvgpr_write_b32 a21, v213\n"
    "v_mfma_f32_16x16x32_bf16 v[4:7], a[198:201], a[84:87], v[4:7]\n"
    "v_cvt_pk_bf16_f32 v90, v52, v53\n"
    "v_cvt_pk_bf16_f32 v91, v54, v55\n"
    "v_mfma_f32_16x16x32_bf16 v[4:7], a[198:201], a[88:91], v[4:7]\n"
    "buffer_load_dwordx4 v142, s[4:7], s68 offen offset:2048 lds\n"
    "v_accvgpr_write_b32 a22, v214\n"
    "v_mfma_f32_16x16x32_bf16 v[40:43], a[194:197], a[160:163], v[10:13]\n"
    "v_cvt_pk_bf16_f32 v92, v56, v57\n"
    "v_cvt_pk_bf16_f32 v93, v58, v59\n"
    "v_mfma_f32_16x16x32_bf16 v[44:47], a[192:195], a[160:163], v[10:13]\n"
    "v_cvt_pk_bf16_f32 v94, v60, v61\n"
    "v_cvt_pk_bf16_f32 v95, v62, v63\n"
    "v_accvgpr_write_b32 a23, v215\n"
    "v_mfma_f32_16x16x32_bf16 v[4:7], a[198:201], a[92:95], v[4:7]\n"
    "buffer_load_dwordx4 v143, s[4:7], s68 offen offset:3072 lds\n"
    "v_mfma_f32_16x16x32_bf16 v[4:7], a[198:201], a[96:99], v[4:7]\n"
    "v_accvgpr_write_b32 a24, v216\n"
    "v_accvgpr_write_b32 a25, v217\n"
    "v_accvgpr_write_b32 a26, v218\n"
    "v_mfma_f32_16x16x32_bf16 v[48:51], a[194:197], a[164:167], v[10:13]\n"
    "v_cvt_pk_bf16_f32 v96, v40, v41\n"
    "v_cvt_pk_bf16_f32 v97, v42, v43\n"
    "v_mfma_f32_16x16x32_bf16 v[52:55], a[192:195], a[164:167], v[10:13]\n"
    "v_cvt_pk_bf16_f32 v98, v44, v45\n"
    "v_cvt_pk_bf16_f32 v99, v46, v47\n"
    "v_accvgpr_write_b32 a27, v219\n"
    "v_mfma_f32_16x16x32_bf16 v[4:7], a[198:201], a[100:103], v[4:7]\n"
    "v_accvgpr_write_b32 a28, v220\n"
    "v_accvgpr_write_b32 a29, v221\n"
    "v_mfma_f32_16x16x32_bf16 v[4:7], a[198:201], a[104:107], v[4:7]\n"
    "v_accvgpr_write_b32 a30, v222\n"
    "v_accvgpr_write_b32 a31, v223\n"
    "v_accvgpr_write_b32 a32, v224\n"
    "v_mfma_f32_16x16x32_bf16 v[56:59], a[194:197], a[168:171], v[10:13]\n"
    "s_add_u32 m0, s88, 0x20000\n"
    "s_add_u32 s69, s76, 0\n"
    "buffer_load_dwordx4 v30, s[20:23], s69 offen offset:0 lds\n"
    "v_mfma_f32_16x16x32_bf16 v[60:63], a[192:195], a[168:171], v[10:13]\n"
    "v_cvt_pk_bf16_f32 v100, v48, v49\n"
    "v_cvt_pk_bf16_f32 v101, v50, v51\n"
    "v_accvgpr_write_b32 a33, v225\n"
    "v_mfma_f32_16x16x32_bf16 v[4:7], a[198:201], a[108:111], v[4:7]\n"
    "v_cvt_pk_bf16_f32 v102, v52, v53\n"
    "v_cvt_pk_bf16_f32 v103, v54, v55\n"
    "v_mfma_f32_16x16x32_bf16 v[4:7], a[198:201], a[112:115], v[4:7]\n"
    "v_accvgpr_write_b32 a34, v226\n"
    "v_accvgpr_write_b32 a35, v227\n"
    "v_accvgpr_write_b32 a36, v228\n"
    "v_mfma_f32_16x16x32_bf16 v[40:43], a[194:197], a[172:175], v[10:13]\n"
    "v_cvt_pk_bf16_f32 v104, v56, v57\n"
    "v_cvt_pk_bf16_f32 v105, v58, v59\n"
    "v_mfma_f32_16x16x32_bf16 v[44:47], a[192:195], a[172:175], v[10:13]\n"
    "v_cvt_pk_bf16_f32 v106, v60, v61\n"
    "v_cvt_pk_bf16_f32 v107, v62, v63\n"
    "v_accvgpr_write_b32 a37, v229\n"
    "v_mfma_f32_16x16x32_bf16 v[4:7], a[198:201], a[116:119], v[4:7]\n"
    "buffer_load_dwordx4 v31, s[20:23], s69 offen offset:1024 lds\n"
    "v_mfma_f32_16x16x32_bf16 v[4:7], a[198:201], a[120:123], v[4:7]\n"
    "v_accvgpr_write_b32 a38, v230\n"
    "v_accvgpr_write_b32 a39, v231\n"
    "v_accvgpr_write_b32 a40, v232\n"
    "v_mfma_f32_16x16x32_bf16 v[48:51], a[194:197], a[176:179], v[10:13]\n"
    "v_cvt_pk_bf16_f32 v108, v40, v41\n"
    "v_cvt_pk_bf16_f32 v109, v42, v43\n"
    "v_mfma_f32_16x16x32_bf16 v[52:55], a[192:195], a[176:179], v[10:13]\n"
    "v_cvt_pk_bf16_f32 v110, v44, v45\n"
    "v_cvt_pk_bf16_f32 v111, v46, v47\n"
    "v_accvgpr_write_b32 a41, v233\n"
    "v_mfma_f32_16x16x32_bf16 v[4:7], a[198:201], a[124:127], v[4:7]\n"
    "v_accvgpr_write_b32 a42, v234\n"
    "v_accvgpr_write_b32 a43, v235\n"
    "v_mfma_f32_16x16x32_bf16 v[14:17], v[252:255], v[252:255], v[14:17]\n"
    "v_accvgpr_write_b32 a44, v236\n"
    "v_accvgpr_write_b32 a45, v237\n"
    "v_accvgpr_write_b32 a46, v238\n"
    "v_mfma_f32_16x16x32_bf16 v[56:59], a[194:197], a[180:183], v[10:13]\n"
    "buffer_load_dwordx4 v32, s[20:23], s69 offen offset:2048 lds\n"
    "v_mfma_f32_16x16x32_bf16 v[60:63], a[192:195], a[180:183], v[10:13]\n"
    "v_cvt_pk_bf16_f32 v112, v48, v49\n"
    "v_cvt_pk_bf16_f32 v113, v50, v51\n"
    "v_accvgpr_write_b32 a47, v239\n"
    "v_mfma_f32_16x16x32_bf16 v[26:29], v[64:67], v[64:67], 0\n"
    "v_cvt_pk_bf16_f32 v114, v52, v53\n"
    "v_cvt_pk_bf16_f32 v115, v54, v55\n"
    "v_mfma_f32_16x16x32_bf16 v[26:29], v[68:71], v[68:71], v[26:29]\n"
    "v_mul_f32 v38, 0x3b000000, v4\n"
    "v_cvt_pk_bf16_f32 v34, v38, v38\n"
    "v_accvgpr_write_b32 a48, v240\n"
    "v_mfma_f32_16x16x32_bf16 v[40:43], a[194:197], a[184:187], v[10:13]\n"
    "v_cvt_pk_bf16_f32 v116, v56, v57\n"
    "v_cvt_pk_bf16_f32 v117, v58, v59\n"
    "v_mfma_f32_16x16x32_bf16 v[44:47], a[192:195], a[184:187], v[10:13]\n"
    "v_cvt_pk_bf16_f32 v118, v60, v61\n"
    "v_cvt_pk_bf16_f32 v119, v62, v63\n"
    "v_accvgpr_write_b32 a49, v241\n"
    "v_mfma_f32_16x16x32_bf16 v[26:29], v[72:75], v[72:75], v[26:29]\n"
    "buffer_load_dwordx4 v33, s[20:23], s69 offen offset:3072 lds\n"
    "v_mfma_f32_16x16x32_bf16 v[26:29], v[76:79], v[76:79], v[26:29]\n"
    "v_lshlrev_b32 v34, 16, v34\n"
    "v_sub_f32 v38, v38, v34\n"
    "v_accvgpr_write_b32 a50, v242\n"
    "v_mfma_f32_16x16x32_bf16 v[48:51], a[194:197], a[188:191], v[10:13]\n"
    "v_cvt_pk_bf16_f32 v120, v40, v41\n"
    "v_cvt_pk_bf16_f32 v121, v42, v43\n"
    "v_mfma_f32_16x16x32_bf16 v[52:55], a[192:195], a[188:191], v[10:13]\n"
    "v_cvt_pk_bf16_f32 v122, v44, v45\n"
    "v_cvt_pk_bf16_f32 v123, v46, v47\n"
    "v_accvgpr_write_b32 a51, v243\n"
    "v_mfma_f32_16x16x32_bf16 v[26:29], v[80:83], v[80:83], v[26:29]\n"
    "v_sub_f32 v4, 0, v34\n"
    "v_sub_f32 v5, 0, v34\n"
    "v_mfma_f32_16x16x32_bf16 v[26:29], v[84:87], v[84:87], v[26:29]\n"
    "v_sub_f32 v6, 0, v34\n"
    "v_sub_f32 v7, 0, v34\n"
    "v_accvgpr_write_b32 a52, v244\n"
    "v_cndmask_b32 v14, v14, v15, s[90:91]\n"
    "v_cndmask_b32 v35, v16, v17, s[90:91]\n"
    "v_mul_f32 v15, v20, v20\n"
    "v_cndmask_b32 v14, v14, v35, s[96:97]\n"
    "v_cndmask_b32 v14, 0, v14, s[86:87]\n"
    "v_mov_b32 v35, v14\n"
    "s_nop 1\n"
    "v_permlane16_swap_b32 v14, v35\n"
    "v_add_f32 v14, v14, v35\n"
    "v_mov_b32 v35, v14\n"
    "s_nop 1\n"
    "v_permlane32_swap_b32 v14, v35\n"
    "v_add_f32 v14, v14, v35\n"
    "v_mul_f32 v14, 0x3b000000, v14\n"
    "v_sub_f32 v14, v14, v15\n"
    "v_max_f32 v14, 0, v14\n"
    "v_cvt_pk_bf16_f32 v14, v14, v14\n"
    "v_lshlrev_b32 v14, 16, v14\n"
    "v_add_f32 v14, 0x3727c5ac, v14\n"
    "v_cvt_pk_bf16_f32 v14, v14, v14\n"
    "v_lshlrev_b32 v14, 16, v14\n"
    "v_sqrt_f32 v14, v14\n"
    "s_nop 0\n"
    "v_cvt_pk_bf16_f32 v14, v14, v14\n"
    "v_lshlrev_b32 v14, 16, v14\n"
    "v_rcp_f32 v20, v14\n"
    "v_mfma_f32_16x16x32_bf16 v[56:59], a[194:197], a[64:67], v[4:7]\n"
    "s_add_u32 m0, s88, 0x21000\n"
    "s_add_u32 s69, s76, 0x15800\n"
    "buffer_load_dwordx4 v30, s[20:23], s69 offen offset:0 lds\n"
    "v_mfma_f32_16x16x32_bf16 v[60:63], a[192:195], a[64:67], v[4:7]\n"
    "v_cvt_pk_bf16_f32 v124, v48, v49\n"
    "v_cvt_pk_bf16_f32 v125, v50, v51\n"
    "v_accvgpr_write_b32 a53, v245\n"
    "v_mfma_f32_16x16x32_bf16 v[26:29], v[88:91], v[88:91], v[26:29]\n"
    "v_cvt_pk_bf16_f32 v126, v52, v53\n"
    "v_cvt_pk_bf16_f32 v127, v54, v55\n"
    "v_mfma_f32_16x16x32_bf16 v[26:29], v[92:95], v[92:95], v[26:29]\n"
    "v_accvgpr_write_b32 a54, v246\n"
    "v_accvgpr_write_b32 a55, v247\n"
    "v_accvgpr_write_b32 a56, v248\n"
    "v_mfma_f32_16x16x32_bf16 v[40:43], a[194:197], a[68:71], v[4:7]\n"
    "v_cvt_pk_bf16_f32 v128, v56, v57\n"
    "v_cvt_pk_bf16_f32 v129, v58, v59\n"
    "v_mfma_f32_16x16x32_bf16 v[44:47], a[192:195], a[68:71], v[4:7]\n"
    "v_cvt_pk_bf16_f32 v130, v60, v61\n"
    "v_cvt_pk_bf16_f32 v131, v62, v63\n"
    "v_accvgpr_write_b32 a57, v249\n"
    "v_mfma_f32_16x16x32_bf16 v[26:29], v[96:99], v[96:99], v[26:29]\n"
    "buffer_load_dwordx4 v31, s[20:23], s69 offen offset:1024 lds\n"
    "s_waitcnt vmcnt(6)\n"
    "ds_read_b128 a[128:131], v22 offset:0\n"
    "ds_read_b128 a[132:135], v23 offset:0\n"
    "v_mfma_f32_16x16x32_bf16 v[26:29], v[100:103], v[100:103], v[26:29]\n"
    "ds_read_b128 a[136:139], v24 offset:0\n"
    "ds_read_b128 a[140:143], v25 offset:0\n"
    "v_accvgpr_write_b32 a58, v250\n"
    "v_mfma_f32_16x16x32_bf16 v[48:51], a[194:197], a[72:75], v[4:7]\n"
    "ds_read_b128 a[144:147], v22 offset:256\n"
    "ds_read_b128 a[148:151], v23 offset:256\n"
    "v_mfma_f32_16x16x32_bf16 v[52:55], a[192:195], a[72:75], v[4:7]\n"
    "ds_read_b128 a[152:155], v24 offset:256\n"
    "ds_read_b128 a[156:159], v25 offset:256\n"
    "v_accvgpr_write_b32 a59, v251\n"
    "v_mfma_f32_16x16x32_bf16 v[26:29], v[104:107], v[104:107], v[26:29]\n"
    "ds_read_b128 a[160:163], v22 offset:512\n"
    "ds_read_b128 a[164:167], v23 offset:512\n"
    "v_cvt_pk_bf16_f32 v132, v40, v41\n"
    "v_mfma_f32_16x16x32_bf16 v[26:29], v[108:111], v[108:111], v[26:29]\n"
    "ds_read_b128 a[168:171], v24 offset:512\n"
    "ds_read_b128 a[172:175], v25 offset:512\n"
    "v_cvt_pk_bf16_f32 v133, v42, v43\n"
    "v_mfma_f32_16x16x32_bf16 v[56:59], a[194:197], a[76:79], v[4:7]\n"
    "buffer_load_dwordx4 v32, s[20:23], s69 offen offset:2048 lds\n"
    "ds_read_b128 a[176:179], v22 offset:768\n"
    "ds_read_b128 a[180:183], v23 offset:768\n"
    "v_cvt_pk_bf16_f32 v134, v44, v45\n"
    "v_mfma_f32_16x16x32_bf16 v[60:63], a[192:195], a[76:79], v[4:7]\n"
    "ds_read_b128 a[184:187], v24 offset:768\n"
    "ds_read_b128 a[188:191], v25 offset:768\n"
    "v_cvt_pk_bf16_f32 v135, v46, v47\n"
    "v_mfma_f32_16x16x32_bf16 v[26:29], v[112:115], v[112:115], v[26:29]\n"
    "v_cvt_pk_bf16_f32 v136, v48, v49\n"
    "v_cvt_pk_bf16_f32 v137, v50, v51\n"
    "v_mfma_f32_16x16x32_bf16 v[26:29], v[116:119], v[116:119], v[26:29]\n"
    "v_cvt_pk_bf16_f32 v138, v52, v53\n"
    "v_cvt_pk_bf16_f32 v139, v54, v55\n"
    "v_accvgpr_write_b32 a60, v252\n"
    "v_mfma_f32_16x16x32_bf16 v[40:43], a[194:197], a[80:83], v[4:7]\n"
    "v_cvt_pk_bf16_f32 v140, v56, v57\n"
    "v_cvt_pk_bf16_f32 v141, v58, v59\n"
    "v_mfma_f32_16x16x32_bf16 v[44:47], a[192:195], a[80:83], v[4:7]\n"
    "v_cvt_pk_bf16_f32 v142, v60, v61\n"
    "v_cvt_pk_bf16_f32 v143, v62, v63\n"
    "v_accvgpr_write_b32 a61, v253\n"
    "v_mfma_f32_16x16x32_bf16 v[26:29], v[120:123], v[120:123], v[26:29]\n"
    "buffer_load_dwordx4 v33, s[20:23], s69 offen offset:3072 lds\n"
    "s_waitcnt lgkmcnt(0)\n"
    "s_barrier\n"
    "v_mfma_f32_16x16x32_bf16 v[10:13], a[198:201], a[128:131], 0\n"
    "v_accvgpr_write_b32 a62, v254\n"
    "v_accvgpr_write_b32 a63, v255\n"
    "v_mfma_f32_16x16x32_bf16 v[48:51], a[194:197], a[84:87], v[4:7]\n"
    "v_cvt_pk_bf16_f32 v144, v40, v41\n"
    "v_cvt_pk_bf16_f32 v145, v42, v43\n"
    "v_mfma_f32_16x16x32_bf16 v[52:55], a[192:195], a[84:87], v[4:7]\n"
    "v_cvt_pk_bf16_f32 v146, v44, v45\n"
    "v_cvt_pk_bf16_f32 v147, v46, v47\n"
    "v_mfma_f32_16x16x32_bf16 v[10:13], a[198:201], a[132:135], v[10:13]\n"
    "v_mfma_f32_16x16x32_bf16 v[10:13], a[198:201], a[136:139], v[10:13]\n"
    "v_mfma_f32_16x16x32_bf16 v[56:59], a[194:197], a[88:91], v[4:7]\n"
    "v_cvt_pk_bf16_f32 v148, v48, v49\n"
    "v_cvt_pk_bf16_f32 v149, v50, v51\n"
    "v_mfma_f32_16x16x32_bf16 v[60:63], a[192:195], a[88:91], v[4:7]\n"
    "v_cvt_pk_bf16_f32 v150, v52, v53\n"
    "v_cvt_pk_bf16_f32 v151, v54, v55\n"
    "v_mfma_f32_16x16x32_bf16 v[10:13], a[198:201], a[140:143], v[10:13]\n"
    "v_mfma_f32_16x16x32_bf16 v[10:13], a[198:201], a[144:147], v[10:13]\n"
    "v_mfma_f32_16x16x32_bf16 v[40:43], a[194:197], a[92:95], v[4:7]\n"
    "s_add_u32 m0, s88, 0x10000\n"
    "s_add_u32 s69, s77, 0\n"
    "buffer_load_dwordx4 v30, s[20:23], s69 offen offset:0 lds\n"
    "v_mfma_f32_16x16x32_bf16 v[44:47], a[192:195], a[92:95], v[4:7]\n"
    "v_cvt_pk_bf16_f32 v152, v56, v57\n"
    "v_cvt_pk_bf16_f32 v153, v58, v59\n"
    "v_mfma_f32_16x16x32_bf16 v[10:13], a[198:201], a[148:151], v[10:13]\n"
    "v_cvt_pk_bf16_f32 v154, v60, v61\n"
    "v_cvt_pk_bf16_f32 v155, v62, v63\n"
    "v_mfma_f32_16x16x32_bf16 v[10:13], a[198:201], a[152:155], v[10:13]\n"
    "v_mfma_f32_16x16x32_bf16 v[48:51], a[194:197], a[96:99], v[4:7]\n"
    "v_cvt_pk_bf16_f32 v156, v40, v41\n"
    "v_cvt_pk_bf16_f32 v157, v42, v43\n"
    "v_mfma_f32_16x16x32_bf16 v[52:55], a[192:195], a[96:99], v[4:7]\n"
    "v_cvt_pk_bf16_f32 v158, v44, v45\n"
    "v_cvt_pk_bf16_f32 v159, v46, v47\n"
    "v_mfma_f32_16x16x32_bf16 v[10:13], a[198:201], a[156:159], v[10:13]\n"
    "buffer_load_dwordx4 v31, s[20:23], s69 offen offset:1024 lds\n"
    "v_mfma_f32_16x16x32_bf16 v[10:13], a[198:201], a[160:163], v[10:13]\n"
    "v_mfma_f32_16x16x32_bf16 v[56:59], a[194:197], a[100:103], v[4:7]\n"
    "v_cvt_pk_bf16_f32 v160, v48, v49\n"
    "v_cvt_pk_bf16_f32 v161, v50, v51\n"
    "v_mfma_f32_16x16x32_bf16 v[60:63], a[192:195], a[100:103], v[4:7]\n"
    "v_cvt_pk_bf16_f32 v162, v52, v53\n"
    "v_cvt_pk_bf16_f32 v163, v54, v55\n"
    "v_mfma_f32_16x16x32_bf16 v[10:13], a[198:201], a[164:167], v[10:13]\n"
    "v_mfma_f32_16x16x32_bf16 v[10:13], a[198:201], a[168:171], v[10:13]\n"
    "v_mfma_f32_16x16x32_bf16 v[40:43], a[194:197], a[104:107], v[4:7]\n"
    "buffer_load_dwordx4 v32, s[20:23], s69 offen offset:2048 lds\n"
    "v_mfma_f32_16x16x32_bf16 v[44:47], a[192:195], a[104:107], v[4:7]\n"
    "v_cvt_pk_bf16_f32 v164, v56, v57\n"
    "v_cvt_pk_bf16_f32 v165, v58, v59\n"
    "v_mfma_f32_16x16x32_bf16 v[10:13], a[198:201], a[172:175], v[10:13]\n"
    "v_cvt_pk_bf16_f32 v166, v60, v61\n"
    "v_cvt_pk_bf16_f32 v167, v62, v63\n"
    "v_mfma_f32_16x16x32_bf16 v[10:13], a[198:201], a[176:179], v[10:13]\n"
    "v_mfma_f32_16x16x32_bf16 v[48:51], a[194:197], a[108:111], v[4:7]\n"
    "v_cvt_pk_bf16_f32 v168, v40, v41\n"
    "v_cvt_pk_bf16_f32 v169, v42, v43\n"
    "v_mfma_f32_16x16x32_bf16 v[52:55], a[192:195], a[108:111], v[4:7]\n"
    "v_cvt_pk_bf16_f32 v170, v44, v45\n"
    "v_cvt_pk_bf16_f32 v171, v46, v47\n"
    "v_mfma_f32_16x16x32_bf16 v[10:13], a[198:201], a[180:183], v[10:13]\n"
    "buffer_load_dwordx4 v33, s[20:23], s69 offen offset:3072 lds\n"
    "v_mfma_f32_16x16x32_bf16 v[10:13], a[198:201], a[184:187], v[10:13]\n"
    "v_mfma_f32_16x16x32_bf16 v[56:59], a[194:197], a[112:115], v[4:7]\n"
    "v_cvt_pk_bf16_f32 v172, v48, v49\n"
    "v_cvt_pk_bf16_f32 v173, v50, v51\n"
    "v_mfma_f32_16x16x32_bf16 v[60:63], a[192:195], a[112:115], v[4:7]\n"
    "v_cvt_pk_bf16_f32 v174, v52, v53\n"
    "v_cvt_pk_bf16_f32 v175, v54, v55\n"
    "v_mfma_f32_16x16x32_bf16 v[10:13], a[198:201], a[188:191], v[10:13]\n"
    "v_mfma_f32_16x16x32_bf16 v[26:29], v[124:127], v[124:127], v[26:29]\n"
    "v_mfma_f32_16x16x32_bf16 v[40:43], a[194:197], a[116:119], v[4:7]\n"
    "s_add_u32 m0, s88, 0x11000\n"
    "s_add_u32 s69, s77, 0x15800\n"
    "buffer_load_dwordx4 v30, s[20:23], s69 offen offset:0 lds\n"
    "v_mfma_f32_16x16x32_bf16 v[44:47], a[192:195], a[116:119], v[4:7]\n"
    "v_cvt_pk_bf16_f32 v176, v56, v57\n"
    "v_cvt_pk_bf16_f32 v177, v58, v59\n"
    "v_mfma_f32_16x16x32_bf16 v[14:17], v[128:131], v[128:131], 0\n"
    "v_cvt_pk_bf16_f32 v178, v60, v61\n"
    "v_cvt_pk_bf16_f32 v179, v62, v63\n"
    "v_mfma_f32_16x16x32_bf16 v[14:17], v[132:135], v[132:135], v[14:17]\n"
    "v_mul_f32 v39, 0x3b000000, v10\n"
    "v_cvt_pk_bf16_f32 v34, v39, v39\n"
    "v_mfma_f32_16x16x32_bf16 v[48:51], a[194:197], a[120:123], v[4:7]\n"
    "v_cvt_pk_bf16_f32 v180, v40, v41\n"
    "v_cvt_pk_bf16_f32 v181, v42, v43\n"
    "v_mfma_f32_16x16x32_bf16 v[52:55], a[192:195], a[120:123], v[4:7]\n"
    "v_cvt_pk_bf16_f32 v182, v44, v45\n"
    "v_cvt_pk_bf16_f32 v183, v46, v47\n"
    "v_mfma_f32_16x16x32_bf16 v[14:17], v[136:139], v[136:139], v[14:17]\n"
    "buffer_load_dwordx4 v31, s[20:23], s69 offen offset:1024 lds\n"
    "v_mfma_f32_16x16x32_bf16 v[14:17], v[140:143], v[140:143], v[14:17]\n"
    "v_lshlrev_b32 v34, 16, v34\n"
    "v_sub_f32 v39, v39, v34\n"
    "v_mfma_f32_16x16x32_bf16 v[56:59], a[194:197], a[124:127], v[4:7]\n"
    "v_cvt_pk_bf16_f32 v184, v48, v49\n"
    "v_cvt_pk_bf16_f32 v185, v50, v51\n"
    "v_mfma_f32_16x16x32_bf16 v[60:63], a[192:195], a[124:127], v[4:7]\n"
    "v_cvt_pk_bf16_f32 v186, v52, v53\n"
    "v_cvt_pk_bf16_f32 v187, v54, v55\n"
    "v_mfma_f32_16x16x32_bf16 v[14:17], v[144:147], v[144:147], v[14:17]\n"
    "v_sub_f32 v10, 0, v34\n"
    "v_sub_f32 v11, 0, v34\n"
    "v_mfma_f32_16x16x32_bf16 v[14:17], v[148:151], v[148:151], v[14:17]\n"
    "v_sub_f32 v12, 0, v34\n"
    "v_sub_f32 v13, 0, v34\n"
    "v_cndmask_b32 v26, v26, v27, s[90:91]\n"
    "v_cndmask_b32 v35, v28, v29, s[90:91]\n"
    "v_mul_f32 v27, v37, v37\n"
    "v_cndmask_b32 v26, v26, v35, s[96:97]\n"
    "v_cndmask_b32 v26, 0, v26, s[86:87]\n"
    "v_mov_b32 v35, v26\n"
    "s_nop 1\n"
    "v_permlane16_swap_b32 v26, v35\n"
    "v_add_f32 v26, v26, v35\n"
    "v_mov_b32 v35, v26\n"
    "s_nop 1\n"
    "v_permlane32_swap_b32 v26, v35\n"
    "v_add_f32 v26, v26, v35\n"
    "v_mul_f32 v26, 0x3b000000, v26\n"
    "v_sub_f32 v26, v26, v27\n"
    "v_max_f32 v26, 0, v26\n"
    "v_cvt_pk_bf16_f32 v26, v26, v26\n"
    "v_lshlrev_b32 v26, 16, v26\n"
    "v_add_f32 v26, 0x3727c5ac, v26\n"
    "v_cvt_pk_bf16_f32 v26, v26, v26\n"
    "v_lshlrev_b32 v26, 16, v26\n"
    "v_sqrt_f32 v26, v26\n"
    "s_nop 0\n"
    "v_cvt_pk_bf16_f32 v26, v26, v26\n"
    "v_lshlrev_b32 v26, 16, v26\n"
    "v_rcp_f32 v37, v26\n"
    "v_mfma_f32_16x16x32_bf16 v[40:43], a[194:197], a[128:131], v[10:13]\n"
    "buffer_load_dwordx4 v32, s[20:23], s69 offen offset:2048 lds\n"
    "v_mfma_f32_16x16x32_bf16 v[44:47], a[192:195], a[128:131], v[10:13]\n"
    "v_cvt_pk_bf16_f32 v188, v56, v57\n"
    "v_cvt_pk_bf16_f32 v189, v58, v59\n"
    "v_mfma_f32_16x16x32_bf16 v[14:17], v[152:155], v[152:155], v[14:17]\n"
    "v_cvt_pk_bf16_f32 v190, v60, v61\n"
    "v_cvt_pk_bf16_f32 v191, v62, v63\n"
    "v_mfma_f32_16x16x32_bf16 v[14:17], v[156:159], v[156:159], v[14:17]\n"
    "v_mfma_f32_16x16x32_bf16 v[48:51], a[194:197], a[132:135], v[10:13]\n"
    "v_cvt_pk_bf16_f32 v192, v40, v41\n"
    "v_cvt_pk_bf16_f32 v193, v42, v43\n"
    "v_mfma_f32_16x16x32_bf16 v[52:55], a[192:195], a[132:135], v[10:13]\n"
    "v_cvt_pk_bf16_f32 v194, v44, v45\n"
    "v_cvt_pk_bf16_f32 v195, v46, v47\n"
    "v_mfma_f32_16x16x32_bf16 v[14:17], v[160:163], v[160:163], v[14:17]\n"
    "buffer_load_dwordx4 v33, s[20:23], s69 offen offset:3072 lds\n"
    "v_mfma_f32_16x16x32_bf16 v[14:17], v[164:167], v[164:167], v[14:17]\n"
    "v_mfma_f32_16x16x32_bf16 v[56:59], a[194:197], a[136:139], v[10:13]\n"
    "v_cvt_pk_bf16_f32 v196, v48, v49\n"
    "v_cvt_pk_bf16_f32 v197, v50, v51\n"
    "v_mfma_f32_16x16x32_bf16 v[60:63], a[192:195], a[136:139], v[10:13]\n"
    "v_cvt_pk_bf16_f32 v198, v52, v53\n"
    "v_cvt_pk_bf16_f32 v199, v54, v55\n"
    "v_mfma_f32_16x16x32_bf16 v[14:17], v[168:171], v[168:171], v[14:17]\n"
    "v_mfma_f32_16x16x32_bf16 v[14:17], v[172:175], v[172:175], v[14:17]\n"
    "v_mfma_f32_16x16x32_bf16 v[40:43], a[194:197], a[140:143], v[10:13]\n"
    "v_cvt_pk_bf16_f32 v200, v56, v57\n"
    "v_cvt_pk_bf16_f32 v201, v58, v59\n"
    "v_mfma_f32_16x16x32_bf16 v[44:47], a[192:195], a[140:143], v[10:13]\n"
    "v_cvt_pk_bf16_f32 v202, v60, v61\n"
    "v_cvt_pk_bf16_f32 v203, v62, v63\n"
    "v_mfma_f32_16x16x32_bf16 v[14:17], v[176:179], v[176:179], v[14:17]\n"
    "v_mfma_f32_16x16x32_bf16 v[14:17], v[180:183], v[180:183], v[14:17]\n"
    "v_mfma_f32_16x16x32_bf16 v[48:51], a[194:197], a[144:147], v[10:13]\n"
    "v_cvt_pk_bf16_f32 v204, v40, v41\n"
    "v_cvt_pk_bf16_f32 v205, v42, v43\n"
    "v_mfma_f32_16x16x32_bf16 v[52:55], a[192:195], a[144:147], v[10:13]\n"
    "v_cvt_pk_bf16_f32 v206, v44, v45\n"
    "v_cvt_pk_bf16_f32 v207, v46, v47\n"
    "v_mfma_f32_16x16x32_bf16 v[14:17], v[184:187], v[184:187], v[14:17]\n"
    "v_mfma_f32_16x16x32_bf16 v[14:17], v[188:191], v[188:191], v[14:17]\n"
    "v_mfma_f32_16x16x32_bf16 v[56:59], a[194:197], a[148:151], v[10:13]\n"
    "v_cvt_pk_bf16_f32 v208, v48, v49\n"
    "v_cvt_pk_bf16_f32 v209, v50, v51\n"
    "v_mfma_f32_16x16x32_bf16 v[60:63], a[192:195], a[148:151], v[10:13]\n"
    "v_cvt_pk_bf16_f32 v210, v52, v53\n"
    "v_cvt_pk_bf16_f32 v211, v54, v55\n"
    "v_mfma_f32_16x16x32_bf16 v[26:29], v[192:195], v[192:195], 0\n"
    "v_mfma_f32_16x16x32_bf16 v[26:29], v[196:199], v[196:199], v[26:29]\n"
    "v_cndmask_b32 v14, v14, v15, s[90:91]\n"
    "v_cndmask_b32 v35, v16, v17, s[90:91]\n"
    "v_mfma_f32_16x16x32_bf16 v[40:43], a[194:197], a[152:155], v[10:13]\n"
    "v_cvt_pk_bf16_f32 v212, v56, v57\n"
    "v_cvt_pk_bf16_f32 v213, v58, v59\n"
    "v_mfma_f32_16x16x32_bf16 v[44:47], a[192:195], a[152:155], v[10:13]\n"
    "v_cvt_pk_bf16_f32 v214, v60, v61\n"
    "v_cvt_pk_bf16_f32 v215, v62, v63\n"
    "v_mfma_f32_16x16x32_bf16 v[26:29], v[200:203], v[200:203], v[26:29]\n"
    "v_mul_f32 v15, v38, v38\n"
    "v_cndmask_b32 v14, v14, v35, s[96:97]\n"
    "v_mfma_f32_16x16x32_bf16 v[26:29], v[204:207], v[204:207], v[26:29]\n"
    "v_cndmask_b32 v14, 0, v14, s[86:87]\n"
    "v_mov_b32 v35, v14\n"
    "v_mfma_f32_16x16x32_bf16 v[48:51], a[194:197], a[156:159], v[10:13]\n"
    "v_cvt_pk_bf16_f32 v216, v40, v41\n"
    "v_cvt_pk_bf16_f32 v217, v42, v43\n"
    "v_mfma_f32_16x16x32_bf16 v[52:55], a[192:195], a[156:159], v[10:13]\n"
    "v_cvt_pk_bf16_f32 v218, v44, v45\n"
    "v_cvt_pk_bf16_f32 v219, v46, v47\n"
    "v_mfma_f32_16x16x32_bf16 v[26:29], v[208:211], v[208:211], v[26:29]\n"
    "v_permlane16_swap_b32 v14, v35\n"
    "v_add_f32 v14, v14, v35\n"
    "v_mfma_f32_16x16x32_bf16 v[56:59], a[194:197], a[160:163], v[10:13]\n"
    "v_mov_b32 v35, v14\n"
    "s_nop 1\n"
    "v_permlane32_swap_b32 v14, v35\n"
    "v_mfma_f32_16x16x32_bf16 v[60:63], a[192:195], a[160:163], v[10:13]\n"
    "v_cvt_pk_bf16_f32 v220, v48, v49\n"
    "v_cvt_pk_bf16_f32 v221, v50, v51\n"
    "v_mfma_f32_16x16x32_bf16 v[26:29], v[212:215], v[212:215], v[26:29]\n"
    "v_cvt_pk_bf16_f32 v222, v52, v53\n"
    "v_cvt_pk_bf16_f32 v223, v54, v55\n"
    "v_mfma_f32_16x16x32_bf16 v[40:43], a[194:197], a[164:167], v[10:13]\n"
    "v_add_f32 v14, v14, v35\n"
    "v_mul_f32 v14, 0x3b000000, v14\n"
    "v_mfma_f32_16x16x32_bf16 v[44:47], a[192:195], a[164:167], v[10:13]\n"
    "v_cvt_pk_bf16_f32 v224, v56, v57\n"
    "v_cvt_pk_bf16_f32 v225, v58, v59\n"
    "v_mfma_f32_16x16x32_bf16 v[26:29], v[216:219], v[216:219], v[26:29]\n"
    "s_waitcnt vmcnt(8)\n"
    "s_barrier\n"
    "v_cvt_pk_bf16_f32 v226, v60, v61\n"
    "v_cvt_pk_bf16_f32 v227, v62, v63\n"
    "v_mfma_f32_16x16x32_bf16 v[48:51], a[194:197], a[168:171], v[10:13]\n"
    "ds_read_b64_tr_b16 a[224:225], v9 offset:0\n"
    "v_sub_f32 v14, v14, v15\n"
    "v_mfma_f32_16x16x32_bf16 v[52:55], a[192:195], a[168:171], v[10:13]\n"
    "ds_read_b64_tr_b16 a[226:227], v9 offset:256\n"
    "v_cvt_pk_bf16_f32 v228, v40, v41\n"
    "v_mfma_f32_16x16x32_bf16 v[26:29], v[220:223], v[220:223], v[26:29]\n"
    "ds_read_b64_tr_b16 a[228:229], v9 offset:4096\n"
    "v_cvt_pk_bf16_f32 v229, v42, v43\n"
    "v_mfma_f32_16x16x32_bf16 v[56:59], a[194:197], a[172:175], v[10:13]\n"
    "ds_read_b64_tr_b16 a[230:231], v9 offset:4352\n"
    "v_cvt_pk_bf16_f32 v230, v44, v45\n"
    "v_mfma_f32_16x16x32_bf16 v[60:63], a[192:195], a[172:175], v[10:13]\n"
    "ds_read_b64_tr_b16 a[232:233], v9 offset:8192\n"
    "v_cvt_pk_bf16_f32 v231, v46, v47\n"
    "v_mfma_f32_16x16x32_bf16 v[26:29], v[224:227], v[224:227], v[26:29]\n"
    "ds_read_b64_tr_b16 a[234:235], v9 offset:8448\n"
    "v_cvt_pk_bf16_f32 v232, v48, v49\n"
    "v_mfma_f32_16x16x32_bf16 v[40:43], a[194:197], a[176:179], v[10:13]\n"
    "ds_read_b64_tr_b16 a[236:237], v9 offset:12288\n"
    "v_cvt_pk_bf16_f32 v233, v50, v51\n"
    "v_mfma_f32_16x16x32_bf16 v[44:47], a[192:195], a[176:179], v[10:13]\n"
    "ds_read_b64_tr_b16 a[238:239], v9 offset:12544\n"
    "v_cvt_pk_bf16_f32 v234, v52, v53\n"
    "v_mfma_f32_16x16x32_bf16 v[26:29], v[228:231], v[228:231], v[26:29]\n"
    "v_cvt_pk_bf16_f32 v235, v54, v55\n"
    "v_cvt_pk_bf16_f32 v236, v56, v57\n"
    "v_mfma_f32_16x16x32_bf16 v[48:51], a[194:197], a[180:183], v[10:13]\n"
    "v_cvt_pk_bf16_f32 v237, v58, v59\n"
    "v_cvt_pk_bf16_f32 v238, v60, v61\n"
    "v_mfma_f32_16x16x32_bf16 v[52:55], a[192:195], a[180:183], v[10:13]\n"
    "v_cvt_pk_bf16_f32 v239, v62, v63\n"
    "v_cvt_pk_bf16_f32 v240, v40, v41\n"
    "v_mfma_f32_16x16x32_bf16 v[26:29], v[232:235], v[232:235], v[26:29]\n"
    "v_cvt_pk_bf16_f32 v241, v42, v43\n"
    "v_cvt_pk_bf16_f32 v242, v44, v45\n"
    "v_mfma_f32_16x16x32_bf16 v[56:59], a[194:197], a[184:187], v[10:13]\n"
    "v_cvt_pk_bf16_f32 v243, v46, v47\n"
    "v_max_f32 v14, 0, v14\n"
    "v_mfma_f32_16x16x32_bf16 v[60:63], a[192:195], a[184:187], v[10:13]\n"
    "v_cvt_pk_bf16_f32 v244, v48, v49\n"
    "v_cvt_pk_bf16_f32 v245, v50, v51\n"
    "v_mfma_f32_16x16x32_bf16 v[26:29], v[236:239], v[236:239], v[26:29]\n"
    "v_cvt_pk_bf16_f32 v246, v52, v53\n"
    "v_cvt_pk_bf16_f32 v247, v54, v55\n"
    "v_mfma_f32_16x16x32_bf16 v[40:43], a[194:197], a[188:191], v[10:13]\n"
    "v_cvt_pk_bf16_f32 v14, v14, v14\n"
    "v_lshlrev_b32 v14, 16, v14\n"
    "v_mfma_f32_16x16x32_bf16 v[44:47], a[192:195], a[188:191], v[10:13]\n"
    "v_cvt_pk_bf16_f32 v248, v56, v57\n"
    "v_cvt_pk_bf16_f32 v249, v58, v59\n"
    "v_mfma_f32_16x16x32_bf16 v[26:29], v[240:243], v[240:243], v[26:29]\n"
    "v_cvt_pk_bf16_f32 v250, v60, v61\n"
    "v_cvt_pk_bf16_f32 v251, v62, v63\n"
    "v_cvt_pk_bf16_f32 v252, v40, v41\n"
    "v_cvt_pk_bf16_f32 v253, v42, v43\n"
    "s_nop 0\n"
    "v_cvt_pk_bf16_f32 v254, v44, v45\n"
    "v_cvt_pk_bf16_f32 v255, v46, v47\n"
    "v_mfma_f32_16x16x32_bf16 v[26:29], v[244:247], v[244:247], v[26:29]\n"
    "v_mfma_f32_16x16x32_bf16 v[26:29], v[248:251], v[248:251], v[26:29]\n"
    "v_mfma_f32_16x16x32_bf16 v[26:29], v[252:255], v[252:255], v[26:29]\n"
    "v_add_f32 v14, 0x3727c5ac, v14\n"
    "v_cvt_pk_bf16_f32 v14, v14, v14\n"
    "v_lshlrev_b32 v14, 16, v14\n"
    "v_sqrt_f32 v14, v14\n"
    "s_nop 0\n"
    "v_cvt_pk_bf16_f32 v14, v14, v14\n"
    "v_lshlrev_b32 v14, 16, v14\n"
    "v_rcp_f32 v38, v14\n"
    "v_cndmask_b32 v26, v26, v27, s[90:91]\n"
    "v_cndmask_b32 v35, v28, v29, s[90:91]\n"
    "v_mul_f32 v27, v39, v39\n"
    "v_cndmask_b32 v26, v26, v35, s[96:97]\n"
    "v_cndmask_b32 v26, 0, v26, s[86:87]\n"
    "v_mov_b32 v35, v26\n"
    "s_nop 1\n"
    "v_permlane16_swap_b32 v26, v35\n"
    "v_add_f32 v26, v26, v35\n"
    "v_mov_b32 v35, v26\n"
    "s_nop 1\n"
    "v_permlane32_swap_b32 v26, v35\n"
    "v_add_f32 v26, v26, v35\n"
    "v_mul_f32 v26, 0x3b000000, v26\n"
    "v_sub_f32 v26, v26, v27\n"
    "v_max_f32 v26, 0, v26\n"
    "v_cvt_pk_bf16_f32 v26, v26, v26\n"
    "v_lshlrev_b32 v26, 16, v26\n"
    "v_add_f32 v26, 0x3727c5ac, v26\n"
    "v_cvt_pk_bf16_f32 v26, v26, v26\n"
    "v_lshlrev_b32 v26, 16, v26\n"
    "v_sqrt_f32 v26, v26\n"
    "s_nop 0\n"
    "v_cvt_pk_bf16_f32 v26, v26, v26\n"
    "v_lshlrev_b32 v26, 16, v26\n"
    "v_rcp_f32 v39, v26\n"
    "s_waitcnt lgkmcnt(0)\n"
    "v_mfma_f32_16x16x32_bf16 a[64:67], a[224:227], v[64:67], 0\n"
    "s_lshl_b32 s80, s80, 20\n"
    "ds_read_b64_tr_b16 a[240:241], v9 offset:16384\n"
    "v_mfma_f32_16x16x32_bf16 a[64:67], a[228:231], v[68:71], a[64:67]\n"
    "s_lshl_b32 s66, s0, 8\n"
    "ds_read_b64_tr_b16 a[242:243], v9 offset:16640\n"
    "v_mfma_f32_16x16x32_bf16 a[64:67], a[232:235], v[72:75], a[64:67]\n"
    "s_sub_u32 s67, 0x400000, s66\n"
    "v_mfma_f32_16x16x32_bf16 a[64:67], a[236:239], v[76:79], a[64:67]\n"
    "s_ashr_i32 s99, s66, 31\n"
    "ds_read_b64_tr_b16 a[244:245], v9 offset:20480\n"
    "v_mfma_f32_16x16x32_bf16 a[68:71], a[236:239], v[140:143], 0\n"
    "s_add_u32 s16, s16, s66\n"
    "s_addc_u32 s17, s17, s99\n"
    "ds_read_b64_tr_b16 a[246:247], v9 offset:20736\n"
    "v_mfma_f32_16x16x32_bf16 a[68:71], a[232:235], v[136:139], a[68:71]\n"
    "s_and_b32 s17, s17, 0xffff\n"
    "v_mfma_f32_16x16x32_bf16 a[68:71], a[228:231], v[132:135], a[68:71]\n"
    "s_mov_b32 s18, s67\n"
    "ds_read_b64_tr_b16 a[248:249], v9 offset:24576\n"
    "v_mfma_f32_16x16x32_bf16 a[68:71], a[224:227], v[128:131], a[68:71]\n"
    "s_mov_b32 s19, 0x20000\n"
    "ds_read_b64_tr_b16 a[250:251], v9 offset:24832\n"
    "v_mfma_f32_16x16x32_bf16 a[72:75], a[224:227], v[192:195], 0\n"
    "s_and_b32 s25, s25, 0xffff\n"
    "v_bfe_u32 v47, v1, 2, 3\n"
    "v_lshrrev_b32 v48, 5, v1\n"
    "v_mfma_f32_16x16x32_bf16 a[72:75], a[228:231], v[196:199], a[72:75]\n"
    "s_add_u32 m0, s88, 0x18000\n"
    "ds_read_b64_tr_b16 a[252:253], v9 offset:28672\n"
    "v_and_b32 v43, 3, v1\n"
    "v_mfma_f32_16x16x32_bf16 a[72:75], a[232:235], v[200:203], a[72:75]\n"
    "s_add_u32 s69, s78, 0\n"
    "buffer_load_dwordx4 v30, s[20:23], s69 offen offset:0 lds\n"
    "v_mfma_f32_16x16x32_bf16 a[72:75], a[236:239], v[204:207], a[72:75]\n"
    "s_mov_b32 s26, 0x2260000\n"
    "ds_read_b64_tr_b16 a[254:255], v9 offset:28928\n"
    "v_lshlrev_b32 v44, 13, v47\n"
    "v_mfma_f32_16x16x32_bf16 a[76:79], a[236:239], a[12:15], 0\n"
    "s_mov_b32 s27, 0x20000\n"
    "buffer_load_dwordx4 v31, s[20:23], s69 offen offset:1024 lds\n"
    "v_mfma_f32_16x16x32_bf16 a[76:79], a[232:235], a[8:11], a[76:79]\n"
    "s_and_b32 s37, s37, 0xffff\n"
    "v_lshl_add_u32 v44, v48, 6, v44\n"
    "v_lshl_add_u32 v44, v43, 4, v44\n"
    "v_mfma_f32_16x16x32_bf16 a[76:79], a[228:231], a[4:7], a[76:79]\n"
    "s_mov_b32 s38, 0x200000\n"
    "buffer_load_dwordx4 v32, s[20:23], s69 offen offset:2048 lds\n"
    "v_mfma_f32_16x16x32_bf16 a[76:79], a[224:227], a[0:3], a[76:79]\n"
    "s_waitcnt lgkmcnt(0)\n"
    "v_mov_b32 v45, s75\n"
    "v_lshl_add_u32 v44, v45, 4, v44\n"
    "v_mfma_f32_16x16x32_bf16 a[92:95], a[240:243], a[0:3], 0\n"
    "s_mov_b32 s39, 0x20000\n"
    "ds_read_b64_tr_b16 a[224:225], v21 offset:0\n"
    "v_add_u32 v26, 0, v44\n"
    "v_mfma_f32_16x16x32_bf16 a[92:95], a[244:247], a[4:7], a[92:95]\n"
    "s_lshl_b32 s66, s0, 19\n"
    "ds_read_b64_tr_b16 a[226:227], v21 offset:256\n"
    "v_add_u32 v27, 0xfc00, v44\n"
    "v_mfma_f32_16x16x32_bf16 a[92:95], a[248:251], a[8:11], a[92:95]\n"
    "s_ashr_i32 s99, s66, 31\n"
    "v_add_u32 v28, 0x1f800, v44\n"
    "v_add_u32 v29, 0x2f400, v44\n"
    "v_mfma_f32_16x16x32_bf16 a[92:95], a[252:255], a[12:15], a[92:95]\n"
    "s_add_u32 s8, s8, s66\n"
    "s_addc_u32 s9, s9, s99\n"
    "ds_read_b64_tr_b16 a[228:229], v21 offset:4096\n"
    "v_lshrrev_b32 v40, 4, v1\n"
    "v_mfma_f32_16x16x32_bf16 a[88:91], a[252:255], v[204:207], 0\n"
    "s_and_b32 s9, s9, 0xffff\n"
    "ds_read_b64_tr_b16 a[230:231], v21 offset:4352\n"
    "v_bfe_u32 v42, v1, 2, 2\n"
    "v_mfma_f32_16x16x32_bf16 a[88:91], a[248:251], v[200:203], a[88:91]\n"
    "s_mov_b32 s10, 0x80000\n"
    "v_lshlrev_b32 v44, 10, v40\n"
    "v_lshl_add_u32 v44, v42, 6, v44\n"
    "v_mfma_f32_16x16x32_bf16 a[88:91], a[244:247], v[196:199], a[88:91]\n"
    "s_mov_b32 s11, 0x20000\n"
    "ds_read_b64_tr_b16 a[232:233], v21 offset:8192\n"
    "v_lshl_add_u32 v44, v43, 4, v44\n"
    "v_mfma_f32_16x16x32_bf16 a[88:91], a[240:243], v[192:195], a[88:91]\n"
    "s_sub_u32 s66, s66, 0x400\n"
    "ds_read_b64_tr_b16 a[234:235], v21 offset:8448\n"
    "v_add_u32 v34, 0x10000, v44\n"
    "v_mfma_f32_16x16x32_bf16 a[84:87], a[240:243], v[128:131], 0\n"
    "s_ashr_i32 s99, s66, 31\n"
    "v_add_u32 v35, 0x20000, v44\n"
    "v_lshrrev_b32 v49, 4, v1\n"
    "v_mfma_f32_16x16x32_bf16 a[84:87], a[244:247], v[132:135], a[84:87]\n"
    "s_add_u32 s12, s12, s66\n"
    "s_addc_u32 s13, s13, s99\n"
    "ds_read_b64_tr_b16 a[236:237], v21 offset:12288\n"
    "v_and_b32 v50, 15, v1\n"
    "v_mfma_f32_16x16x32_bf16 a[84:87], a[248:251], v[136:139], a[84:87]\n"
    "s_and_b32 s13, s13, 0xffff\n"
    "buffer_load_dwordx4 v33, s[20:23], s69 offen offset:3072 lds\n"
    "v_mfma_f32_16x16x32_bf16 a[84:87], a[252:255], v[140:143], a[84:87]\n"
    "s_add_u32 m0, s88, 0x19000\n"
    "ds_read_b64_tr_b16 a[238:239], v21 offset:12544\n"
    "v_and_b32 v51, 3, v1\n"
    "v_mfma_f32_16x16x32_bf16 a[80:83], a[252:255], v[76:79], 0\n"
    "s_add_u32 s69, s78, 0x15800\n"
    "buffer_load_dwordx4 v30, s[20:23], s69 offen offset:0 lds\n"
    "v_mfma_f32_16x16x32_bf16 a[80:83], a[248:251], v[72:75], a[80:83]\n"
    "s_mov_b32 s14, 0x80400\n"
    "v_add_u32 v52, 0, v49\n"
    "v_xor_b32 v52, v52, v50\n"
    "v_mfma_f32_16x16x32_bf16 a[80:83], a[244:247], v[68:71], a[80:83]\n"
    "s_mov_b32 s15, 0x20000\n"
    "buffer_load_dwordx4 v31, s[20:23], s69 offen offset:1024 lds\n"
    "v_mfma_f32_16x16x32_bf16 a[80:83], a[240:243], v[64:67], a[80:83]\n"
    "s_waitcnt lgkmcnt(0)\n"
    "v_lshlrev_b32 v52, 4, v52\n"
    "v_lshl_add_u32 v4, v50, 8, v52\n"
    "v_mfma_f32_16x16x32_bf16 a[96:99], a[224:227], v[64:67], 0\n"
    "s_mul_i32 s58, s58, 0x2c000\n"
    "ds_read_b64_tr_b16 a[240:241], v21 offset:16384\n"
    "v_lshl_add_u32 v52, v49, 8, v52\n"
    "v_mfma_f32_16x16x32_bf16 a[96:99], a[228:231], v[68:71], a[96:99]\n"
    "s_mul_i32 s59, s59, 0x2c000\n"
    "ds_read_b64_tr_b16 a[242:243], v21 offset:16640\n"
    "v_add_u32 v10, s65, v52\n"
    "v_mfma_f32_16x16x32_bf16 a[96:99], a[232:235], v[72:75], a[96:99]\n"
    "s_mul_i32 s62, s62, 0x2c000\n"
    "v_add_u32 v52, 4, v49\n"
    "v_xor_b32 v52, v52, v50\n"
    "v_mfma_f32_16x16x32_bf16 a[96:99], a[236:239], v[76:79], a[96:99]\n"
    "s_mul_i32 s63, s63, 0x2c000\n"
    "ds_read_b64_tr_b16 a[244:245], v21 offset:20480\n"
    "v_lshlrev_b32 v52, 4, v52\n"
    "v_mfma_f32_16x16x32_bf16 a[100:103], a[236:239], v[140:143], 0\n"
    "s_lshr_b32 s66, s40, 14\n"
    "ds_read_b64_tr_b16 a[246:247], v21 offset:20736\n"
    "v_lshl_add_u32 v5, v50, 8, v52\n"
    "v_mfma_f32_16x16x32_bf16 a[100:103], a[232:235], v[136:139], a[100:103]\n"
    "s_mul_i32 s66, s66, 0x2c000\n"
    "v_lshl_add_u32 v52, v49, 8, v52\n"
    "v_add_u32 v11, s65, v52\n"
    "v_mfma_f32_16x16x32_bf16 a[100:103], a[228:231], v[132:135], a[100:103]\n"
    "s_lshl_b32 s67, s0, 12\n"
    "ds_read_b64_tr_b16 a[248:249], v21 offset:24576\n"
    "v_add_u32 v52, 8, v49\n"
    "v_mfma_f32_16x16x32_bf16 a[100:103], a[224:227], v[128:131], a[100:103]\n"
    "s_add_u32 s41, s66, s67\n"
    "ds_read_b64_tr_b16 a[250:251], v21 offset:24832\n"
    "v_xor_b32 v52, v52, v50\n"
    "v_mfma_f32_16x16x32_bf16 a[104:107], a[224:227], v[192:195], 0\n"
    "s_lshr_b32 s67, s0, 1\n"
    "v_lshlrev_b32 v52, 4, v52\n"
    "v_lshl_add_u32 v6, v50, 8, v52\n"
    "v_mfma_f32_16x16x32_bf16 a[104:107], a[228:231], v[196:199], a[104:107]\n"
    "s_mul_i32 s67, s67, 0xc00\n"
    "ds_read_b64_tr_b16 a[252:253], v21 offset:28672\n"
    "v_lshl_add_u32 v52, v49, 8, v52\n"
    "v_mfma_f32_16x16x32_bf16 a[104:107], a[232:235], v[200:203], a[104:107]\n"
    "s_add_u32 s66, s66, s67\n"
    "buffer_load_dwordx4 v32, s[20:23], s69 offen offset:2048 lds\n"
    "v_mfma_f32_16x16x32_bf16 a[104:107], a[236:239], v[204:207], a[104:107]\n"
    "s_add_u32 s43, s66, 0x20400\n"
    "ds_read_b64_tr_b16 a[254:255], v21 offset:28928\n"
    "v_add_u32 v12, s65, v52\n"
    "v_mfma_f32_16x16x32_bf16 a[108:111], a[236:239], a[12:15], 0\n"
    "s_and_b32 s67, s0, 1\n"
    "buffer_load_dwordx4 v33, s[20:23], s69 offen offset:3072 lds\n"
    "v_mfma_f32_16x16x32_bf16 a[108:111], a[232:235], a[8:11], a[108:111]\n"
    "s_lshl_b32 s67, s67, 11\n"
    "v_add_u32 v52, 12, v49\n"
    "v_xor_b32 v52, v52, v50\n"
    "v_mfma_f32_16x16x32_bf16 a[108:111], a[228:231], a[4:7], a[108:111]\n"
    "s_add_u32 s66, s66, s67\n"
    "v_lshlrev_b32 v52, 4, v52\n"
    "v_lshl_add_u32 v7, v50, 8, v52\n"
    "v_mfma_f32_16x16x32_bf16 a[108:111], a[224:227], a[0:3], a[108:111]\n"
    "s_waitcnt lgkmcnt(0)\n"
    "v_lshl_add_u32 v52, v49, 8, v52\n"
    "v_add_u32 v13, s65, v52\n"
    "v_mfma_f32_16x16x32_bf16 a[124:127], a[240:243], a[0:3], 0\n"
    "s_add_u32 s42, s66, 0x20000\n"
    "ds_read_b64_tr_b16 a[224:225], v9 offset:512\n"
    "ds_read_b64_tr_b16 a[226:227], v9 offset:768\n"
    "v_mfma_f32_16x16x32_bf16 a[124:127], a[244:247], a[4:7], a[124:127]\n"
    "s_mov_b32 s73, 0x5f800000\n"
    "ds_read_b64_tr_b16 a[228:229], v9 offset:4608\n"
    "v_bfe_u32 v54, v1, 2, 3\n"
    "v_mfma_f32_16x16x32_bf16 a[124:127], a[248:251], a[8:11], a[124:127]\n"
    "s_mov_b32 s74, 0\n"
    "ds_read_b64_tr_b16 a[230:231], v9 offset:4864\n"
    "ds_read_b64_tr_b16 a[232:233], v9 offset:8704\n"
    "v_mfma_f32_16x16x32_bf16 a[124:127], a[252:255], a[12:15], a[124:127]\n"
    "s_mov_b32 s96, 0x3e0293ee\n"
    "ds_read_b64_tr_b16 a[234:235], v9 offset:8960\n"
    "v_lshrrev_b32 v55, 5, v1\n"
    "v_mfma_f32_16x16x32_bf16 a[120:123], a[252:255], v[204:207], 0\n"
    "s_mov_b32 s90, 0x7f4a7c15\n"
    "ds_read_b64_tr_b16 a[236:237], v9 offset:12800\n"
    "ds_read_b64_tr_b16 a[238:239], v9 offset:13056\n"
    "v_mfma_f32_16x16x32_bf16 a[120:123], a[248:251], v[200:203], a[120:123]\n"
    "s_mov_b32 s91, 0x9e3779b9\n"
    "ds_read_b64_tr_b16 a[192:193], v9 offset:16896\n"
    "v_lshrrev_b32 v53, 2, v54\n"
    "v_mfma_f32_16x16x32_bf16 a[120:123], a[244:247], v[196:199], a[120:123]\n"
    "s_lshl_b32 s89, s44, 17\n"
    "ds_read_b64_tr_b16 a[194:195], v9 offset:17152\n"
    "ds_read_b64_tr_b16 a[196:197], v9 offset:20992\n"
    "v_mfma_f32_16x16x32_bf16 a[120:123], a[240:243], v[192:195], a[120:123]\n"
    "s_lshl_b32 s46, s44, 12\n"
    "ds_read_b64_tr_b16 a[198:199], v9 offset:21248\n"
    "v_lshlrev_b32 v53, 1, v53\n"
    "v_mfma_f32_16x16x32_bf16 a[116:119], a[240:243], v[128:131], 0\n"
    "s_and_b32 s97, s44, 3\n"
    "ds_read_b64_tr_b16 a[200:201], v9 offset:25088\n"
    "v_xor_b32 v53, v53, v51\n"
    "v_mfma_f32_16x16x32_bf16 a[116:119], a[244:247], v[132:135], a[116:119]\n"
    "s_lshl_b32 s97, s97, 8\n"
    "ds_read_b64_tr_b16 a[202:203], v9 offset:25344\n"
    "ds_read_b64_tr_b16 a[204:205], v9 offset:29184\n"
    "v_mfma_f32_16x16x32_bf16 a[116:119], a[248:251], v[136:139], a[116:119]\n"
    "s_sub_u32 s98, s97, 0x400\n"
    "v_lshlrev_b32 v53, 4, v53\n"
    "v_lshl_add_u32 v52, v55, 6, v53\n"
    "v_mfma_f32_16x16x32_bf16 a[116:119], a[252:255], v[140:143], a[116:119]\n"
    "s_mul_i32 s66, s45, 0x60000\n"
    "ds_read_b64_tr_b16 a[206:207], v9 offset:29440\n"
    "v_lshl_add_u32 v52, v54, 8, v52\n"
    "v_mfma_f32_16x16x32_bf16 a[112:115], a[252:255], v[76:79], 0\n"
    "s_add_u32 s66, s66, s98\n"
    "v_add_u32 v52, s65, v52\n"
    "v_add_u32 v14, 0x400, v52\n"
    "v_mfma_f32_16x16x32_bf16 a[112:115], a[248:251], v[72:75], a[112:115]\n"
    "s_cmp_lt_u32 s45, 3\n"
    "s_cselect_b32 s67, 0x28000, 0\n"
    "v_add_u32 v15, 0x80, v52\n"
    "v_lshrrev_b32 v46, 2, v47\n"
    "v_mfma_f32_16x16x32_bf16 a[112:115], a[244:247], v[68:71], a[112:115]\n"
    "s_add_u32 s67, s67, 0x38100\n"
    "v_lshlrev_b32 v46, 1, v46\n"
    "v_xor_b32 v46, v46, v43\n"
    "v_mfma_f32_16x16x32_bf16 a[112:115], a[240:243], v[64:67], a[112:115]\n"
    "s_waitcnt lgkmcnt(0)\n"
    "v_lshlrev_b32 v46, 4, v46\n"
    "v_lshl_add_u32 v44, v47, 10, v46\n"
    "v_mfma_f32_16x16x32_bf16 a[128:131], a[224:227], v[64:67], 0\n"
    "s_ashr_i32 s99, s66, 31\n"
    "ds_read_b64_tr_b16 a[240:241], v21 offset:512\n"
    "ds_read_b64_tr_b16 a[242:243], v21 offset:768\n"
    "v_mfma_f32_16x16x32_bf16 a[128:131], a[228:231], v[68:71], a[128:131]\n"
    "s_add_u32 s28, s28, s66\n"
    "s_addc_u32 s29, s29, s99\n"
    "ds_read_b64_tr_b16 a[244:245], v21 offset:4608\n"
    "v_add_u32 v44, s75, v44\n"
    "v_mfma_f32_16x16x32_bf16 a[128:131], a[232:235], v[72:75], a[128:131]\n"
    "s_and_b32 s29, s29, 0xffff\n"
    "ds_read_b64_tr_b16 a[246:247], v21 offset:4864\n"
    "ds_read_b64_tr_b16 a[248:249], v21 offset:8704\n"
    "v_mfma_f32_16x16x32_bf16 a[128:131], a[236:239], v[76:79], a[128:131]\n"
    "s_mov_b32 s30, s67\n"
    "ds_read_b64_tr_b16 a[250:251], v21 offset:8960\n"
    "v_add_u32 v46, s45, v48\n"
    "v_mfma_f32_16x16x32_bf16 a[132:135], a[236:239], v[140:143], 0\n"
    "s_mov_b32 s31, 0x20000\n"
    "ds_read_b64_tr_b16 a[252:253], v21 offset:12800\n"
    "ds_read_b64_tr_b16 a[254:255], v21 offset:13056\n"
    "v_mfma_f32_16x16x32_bf16 a[132:135], a[232:235], v[136:139], a[132:135]\n"
    "s_lshl_b32 s66, s45, 20\n"
    "ds_read_b64_tr_b16 a[208:209], v21 offset:16896\n"
    "v_add_u32 v45, 0, v46\n"
    "v_mfma_f32_16x16x32_bf16 a[132:135], a[228:231], v[132:135], a[132:135]\n"
    "s_add_u32 s66, s66, s98\n"
    "ds_read_b64_tr_b16 a[210:211], v21 offset:17152\n"
    "ds_read_b64_tr_b16 a[212:213], v21 offset:20992\n"
    "v_mfma_f32_16x16x32_bf16 a[132:135], a[224:227], v[128:131], a[132:135]\n"
    "s_ashr_i32 s99, s66, 31\n"
    "ds_read_b64_tr_b16 a[214:215], v21 offset:21248\n"
    "v_and_b32 v45, 3, v45\n"
    "v_mfma_f32_16x16x32_bf16 a[140:143], a[192:195], v[128:131], 0\n"
    "s_add_u32 s32, s32, s66\n"
    "s_addc_u32 s33, s33, s99\n"
    "ds_read_b64_tr_b16 a[216:217], v21 offset:25088\n"
    "v_lshl_add_u32 v45, v45, 6, v44\n"
    "v_mfma_f32_16x16x32_bf16 a[140:143], a[196:199], v[132:135], a[140:143]\n"
    "s_and_b32 s33, s33, 0xffff\n"
    "ds_read_b64_tr_b16 a[218:219], v21 offset:25344\n"
    "ds_read_b64_tr_b16 a[220:221], v21 offset:29184\n"
    "v_mfma_f32_16x16x32_bf16 a[140:143], a[200:203], v[136:139], a[140:143]\n"
    "s_mov_b32 s34, 0x100100\n"
    "v_add_u32 v16, 0x400, v45\n"
    "v_add_u32 v45, 2, v46\n"
    "v_mfma_f32_16x16x32_bf16 a[140:143], a[204:207], v[140:143], a[140:143]\n"
    "s_mov_b32 s35, 0x20000\n"
    "ds_read_b64_tr_b16 a[222:223], v21 offset:29440\n"
    "v_and_b32 v45, 3, v45\n"
    "v_mfma_f32_16x16x32_bf16 a[136:139], a[204:207], v[76:79], 0\n"
    "s_lshr_b32 s66, s44, 2\n"
    "v_lshl_add_u32 v45, v45, 6, v44\n"
    "v_add_u32 v17, 0, v45\n"
    "v_mfma_f32_16x16x32_bf16 a[136:139], a[200:203], v[72:75], a[136:139]\n"
    "s_lshl1_add_u32 s67, s84, s66\n"
    "v_add_u32 v45, 0, v46\n"
    "v_and_b32 v45, 3, v45\n"
    "v_mfma_f32_16x16x32_bf16 a[136:139], a[196:199], v[68:71], a[136:139]\n"
    "s_lshl2_add_u32 s67, s67, s64\n"
    "v_lshl_add_u32 v45, v45, 6, v44\n"
    "v_add_u32 v18, 0x1c00, v45\n"
    "v_mfma_f32_16x16x32_bf16 a[136:139], a[192:195], v[64:67], a[136:139]\n"
    "s_waitcnt lgkmcnt(0)\n"
    "v_add_u32 v45, 2, v46\n"
    "v_and_b32 v45, 3, v45\n"
    "v_mfma_f32_16x16x32_bf16 a[144:147], a[240:243], v[64:67], 0\n"
    "s_waitcnt vmcnt(8)\n"
    "v_lshl_add_u32 v45, v45, 6, v44\n"
    "v_add_u32 v19, 0x1800, v45\n"
    "v_mfma_f32_16x16x32_bf16 a[144:147], a[244:247], v[68:71], a[144:147]\n"
    "s_barrier\n"
    "v_mfma_f32_16x16x32_bf16 a[144:147], a[248:251], v[72:75], a[144:147]\n"
    "s_lshl_b32 s57, s67, 15\n"
    "ds_read_b64_tr_b16 a[224:225], v36 offset:0\n"
    "v_mfma_f32_16x16x32_bf16 a[144:147], a[252:255], v[76:79], a[144:147]\n"
    "s_lshl1_add_u32 s66, s3, s66\n"
    "ds_read_b64_tr_b16 a[226:227], v36 offset:256\n"
    "v_mfma_f32_16x16x32_bf16 a[148:151], a[252:255], v[140:143], 0\n"
    "s_lshl2_add_u32 s66, s66, s64\n"
    "ds_read_b64_tr_b16 a[228:229], v36 offset:4096\n"
    "v_mfma_f32_16x16x32_bf16 a[148:151], a[248:251], v[136:139], a[148:151]\n"
    "s_lshl_b32 s56, s66, 15\n"
    "v_mfma_f32_16x16x32_bf16 a[148:151], a[244:247], v[132:135], a[148:151]\n"
    "s_add_u32 s56, s56, s97\n"
    "ds_read_b64_tr_b16 a[230:231], v36 offset:4352\n"
    "v_mfma_f32_16x16x32_bf16 a[148:151], a[240:243], v[128:131], a[148:151]\n"
    "s_lshl_b32 s67, s45, 6\n"
    "ds_read_b64_tr_b16 a[232:233], v36 offset:8192\n"
    "v_mfma_f32_16x16x32_bf16 a[156:159], a[208:211], v[128:131], 0\n"
    "s_add_u32 s56, s56, s67\n"
    "ds_read_b64_tr_b16 a[234:235], v36 offset:8448\n"
    "v_mfma_f32_16x16x32_bf16 a[156:159], a[212:215], v[132:135], a[156:159]\n"
    "s_add_u32 m0, s88, 0x20000\n"
    "ds_read_b64_tr_b16 a[236:237], v36 offset:12288\n"
    "v_mfma_f32_16x16x32_bf16 a[156:159], a[216:219], v[136:139], a[156:159]\n"
    "s_add_u32 s69, s79, 0\n"
    "buffer_load_dwordx4 v30, s[20:23], s69 offen offset:0 lds\n"
    "v_mfma_f32_16x16x32_bf16 a[156:159], a[220:223], v[140:143], a[156:159]\n"
    "s_mul_i32 s66, s66, 0x58000\n"
    "ds_read_b64_tr_b16 a[238:239], v36 offset:12544\n"
    "v_mfma_f32_16x16x32_bf16 a[152:155], a[220:223], v[76:79], 0\n"
    "s_lshl_b32 s67, s45, 15\n"
    "buffer_load_dwordx4 v31, s[20:23], s69 offen offset:1024 lds\n"
    "v_mfma_f32_16x16x32_bf16 a[152:155], a[216:219], v[72:75], a[152:155]\n"
    "s_add_u32 s48, s66, s67\n"
    "v_mfma_f32_16x16x32_bf16 a[152:155], a[212:215], v[68:71], a[152:155]\n"
    "s_mul_i32 s67, s45, 0x3000\n"
    "buffer_load_dwordx4 v32, s[20:23], s69 offen offset:2048 lds\n"
    "v_mfma_f32_16x16x32_bf16 a[152:155], a[208:211], v[64:67], a[152:155]\n"
    "s_waitcnt lgkmcnt(0)\n"
    "v_mfma_f32_16x16x32_bf16 a[64:67], a[224:227], v[80:83], a[64:67]\n"
    "s_add_u32 s66, s66, s67\n"
    "ds_read_b64_tr_b16 a[240:241], v36 offset:16384\n"
    "v_mfma_f32_16x16x32_bf16 a[64:67], a[228:231], v[84:87], a[64:67]\n"
    "s_add_u32 s47, s66, 0x20000\n"
    "ds_read_b64_tr_b16 a[242:243], v36 offset:16640\n"
    "v_mfma_f32_16x16x32_bf16 a[64:67], a[232:235], v[88:91], a[64:67]\n"
    "s_lshl_b32 s66, s3, 5\n"
    "v_mfma_f32_16x16x32_bf16 a[64:67], a[236:239], v[92:95], a[64:67]\n"
    "s_add_u32 s67, s66, s0\n"
    "ds_read_b64_tr_b16 a[244:245], v36 offset:20480\n"
    "v_mfma_f32_16x16x32_bf16 a[68:71], a[236:239], v[156:159], a[68:71]\n"
    "s_mul_i32 s55, s67, 0xc000\n"
    "ds_read_b64_tr_b16 a[246:247], v36 offset:20736\n"
    "v_mfma_f32_16x16x32_bf16 a[68:71], a[232:235], v[152:155], a[68:71]\n"
    "s_add_u32 s55, s55, 0x1600000\n"
    "v_mfma_f32_16x16x32_bf16 a[68:71], a[228:231], v[148:151], a[68:71]\n"
    "s_lshl_b32 s67, s67, 9\n"
    "ds_read_b64_tr_b16 a[248:249], v36 offset:24576\n"
    "v_mfma_f32_16x16x32_bf16 a[68:71], a[224:227], v[144:147], a[68:71]\n"
    "s_add_u32 s51, s67, 0x2200000\n"
    "ds_read_b64_tr_b16 a[250:251], v36 offset:24832\n"
    "v_mfma_f32_16x16x32_bf16 a[72:75], a[224:227], v[208:211], a[72:75]\n"
    "s_mul_i32 s67, s44, 0x1f8\n"
    "v_mfma_f32_16x16x32_bf16 a[72:75], a[228:231], v[212:215], a[72:75]\n"
    "s_sub_u32 s49, s51, s67\n"
    "ds_read_b64_tr_b16 a[252:253], v36 offset:28672\n"
    "v_mfma_f32_16x16x32_bf16 a[72:75], a[232:235], v[216:219], a[72:75]\n"
    "s_mul_i32 s67, s45, 0xff8\n"
    "buffer_load_dwordx4 v33, s[20:23], s69 offen offset:3072 lds\n"
    "v_mfma_f32_16x16x32_bf16 a[72:75], a[236:239], v[220:223], a[72:75]\n"
    "s_add_u32 m0, s88, 0x21000\n"
    "ds_read_b64_tr_b16 a[254:255], v36 offset:28928\n"
    "v_mfma_f32_16x16x32_bf16 a[76:79], a[236:239], a[28:31], a[76:79]\n"
    "s_add_u32 s69, s79, 0x15800\n"
    "buffer_load_dwordx4 v30, s[20:23], s69 offen offset:0 lds\n"
    "v_mfma_f32_16x16x32_bf16 a[76:79], a[232:235], a[24:27], a[76:79]\n"
    "s_sub_u32 s50, s51, s67\n"
    "v_mfma_f32_16x16x32_bf16 a[76:79], a[228:231], a[20:23], a[76:79]\n"
    "s_add_u32 s66, s66, s44\n"
    "buffer_load_dwordx4 v31, s[20:23], s69 offen offset:1024 lds\n"
    "v_mfma_f32_16x16x32_bf16 a[76:79], a[224:227], a[16:19], a[76:79]\n"
    "s_waitcnt lgkmcnt(0)\n"
    "v_mfma_f32_16x16x32_bf16 a[92:95], a[240:243], a[16:19], a[92:95]\n"
    "s_add_u32 s52, s45, 1\n"
    "ds_read_b64_tr_b16 a[224:225], v8 offset:0\n"
    "v_mfma_f32_16x16x32_bf16 a[92:95], a[244:247], a[20:23], a[92:95]\n"
    "s_and_b32 s52, s52, 3\n"
    "ds_read_b64_tr_b16 a[226:227], v8 offset:256\n"
    "v_mfma_f32_16x16x32_bf16 a[92:95], a[248:251], a[24:27], a[92:95]\n"
    "s_lshl3_add_u32 s52, s52, s66\n"
    "v_mfma_f32_16x16x32_bf16 a[92:95], a[252:255], a[28:31], a[92:95]\n"
    "s_mul_i32 s52, s52, 0xc000\n"
    "ds_read_b64_tr_b16 a[228:229], v8 offset:4096\n"
    "v_mfma_f32_16x16x32_bf16 a[88:91], a[252:255], v[220:223], a[88:91]\n"
    "s_add_u32 s52, s52, 0x1600000\n"
    "ds_read_b64_tr_b16 a[230:231], v8 offset:4352\n"
    "v_mfma_f32_16x16x32_bf16 a[88:91], a[248:251], v[216:219], a[88:91]\n"
    "s_add_u32 s53, s45, 2\n"
    "v_mfma_f32_16x16x32_bf16 a[88:91], a[244:247], v[212:215], a[88:91]\n"
    "s_and_b32 s53, s53, 3\n"
    "ds_read_b64_tr_b16 a[232:233], v8 offset:8192\n"
    "v_mfma_f32_16x16x32_bf16 a[88:91], a[240:243], v[208:211], a[88:91]\n"
    "s_lshl3_add_u32 s53, s53, s66\n"
    "ds_read_b64_tr_b16 a[234:235], v8 offset:8448\n"
    "v_mfma_f32_16x16x32_bf16 a[84:87], a[240:243], v[144:147], a[84:87]\n"
    "s_mul_i32 s53, s53, 0xc000\n"
    "v_mfma_f32_16x16x32_bf16 a[84:87], a[244:247], v[148:151], a[84:87]\n"
    "s_add_u32 s53, s53, 0x1602000\n"
    "ds_read_b64_tr_b16 a[236:237], v8 offset:12288\n"
    "v_mfma_f32_16x16x32_bf16 a[84:87], a[248:251], v[152:155], a[84:87]\n"
    "s_add_u32 s54, s45, 3\n"
    "buffer_load_dwordx4 v32, s[20:23], s69 offen offset:2048 lds\n"
    "v_mfma_f32_16x16x32_bf16 a[84:87], a[252:255], v[156:159], a[84:87]\n"
    "s_and_b32 s54, s54, 3\n"
    "ds_read_b64_tr_b16 a[238:239], v8 offset:12544\n"
    "v_mfma_f32_16x16x32_bf16 a[80:83], a[252:255], v[92:95], a[80:83]\n"
    "s_lshl3_add_u32 s54, s54, s66\n"
    "buffer_load_dwordx4 v33, s[20:23], s69 offen offset:3072 lds\n"
    "v_mfma_f32_16x16x32_bf16 a[80:83], a[248:251], v[88:91], a[80:83]\n"
    "s_mul_i32 s54, s54, 0xc000\n"
    "v_mfma_f32_16x16x32_bf16 a[80:83], a[244:247], v[84:87], a[80:83]\n"
    "s_add_u32 s54, s54, 0x1604000\n"
    "v_mfma_f32_16x16x32_bf16 a[80:83], a[240:243], v[80:83], a[80:83]\n"
    "s_waitcnt lgkmcnt(0)\n"
    "v_mfma_f32_16x16x32_bf16 a[96:99], a[224:227], v[80:83], a[96:99]\n"
    "ds_read_b64_tr_b16 a[240:241], v8 offset:16384\n"
    "v_mfma_f32_16x16x32_bf16 a[96:99], a[228:231], v[84:87], a[96:99]\n"
    "ds_read_b64_tr_b16 a[242:243], v8 offset:16640\n"
    "v_mfma_f32_16x16x32_bf16 a[96:99], a[232:235], v[88:91], a[96:99]\n"
    "v_mfma_f32_16x16x32_bf16 a[96:99], a[236:239], v[92:95], a[96:99]\n"
    "ds_read_b64_tr_b16 a[244:245], v8 offset:20480\n"
    "v_mfma_f32_16x16x32_bf16 a[100:103], a[236:239], v[156:159], a[100:103]\n"
    "ds_read_b64_tr_b16 a[246:247], v8 offset:20736\n"
    "v_mfma_f32_16x16x32_bf16 a[100:103], a[232:235], v[152:155], a[100:103]\n"
    "v_mfma_f32_16x16x32_bf16 a[100:103], a[228:231], v[148:151], a[100:103]\n"
    "ds_read_b64_tr_b16 a[248:249], v8 offset:24576\n"
    "v_mfma_f32_16x16x32_bf16 a[100:103], a[224:227], v[144:147], a[100:103]\n"
    "ds_read_b64_tr_b16 a[250:251], v8 offset:24832\n"
    "v_mfma_f32_16x16x32_bf16 a[104:107], a[224:227], v[208:211], a[104:107]\n"
    "v_mfma_f32_16x16x32_bf16 a[104:107], a[228:231], v[212:215], a[104:107]\n"
    "ds_read_b64_tr_b16 a[252:253], v8 offset:28672\n"
    "v_mfma_f32_16x16x32_bf16 a[104:107], a[232:235], v[216:219], a[104:107]\n"
    "v_mfma_f32_16x16x32_bf16 a[104:107], a[236:239], v[220:223], a[104:107]\n"
    "ds_read_b64_tr_b16 a[254:255], v8 offset:28928\n"
    "v_mfma_f32_16x16x32_bf16 a[108:111], a[236:239], a[28:31], a[108:111]\n"
    "v_mfma_f32_16x16x32_bf16 a[108:111], a[232:235], a[24:27], a[108:111]\n"
    "v_mfma_f32_16x16x32_bf16 a[108:111], a[228:231], a[20:23], a[108:111]\n"
    "v_mfma_f32_16x16x32_bf16 a[108:111], a[224:227], a[16:19], a[108:111]\n"
    "s_waitcnt lgkmcnt(0)\n"
    "v_mfma_f32_16x16x32_bf16 a[124:127], a[240:243], a[16:19], a[124:127]\n"
    "ds_read_b64_tr_b16 a[224:225], v36 offset:512\n"
    "ds_read_b64_tr_b16 a[226:227], v36 offset:768\n"
    "v_mfma_f32_16x16x32_bf16 a[124:127], a[244:247], a[20:23], a[124:127]\n"
    "ds_read_b64_tr_b16 a[228:229], v36 offset:4608\n"
    "v_mov_b32 v41, v3\n"
    "v_mfma_f32_16x16x32_bf16 a[124:127], a[248:251], a[24:27], a[124:127]\n"
    "ds_read_b64_tr_b16 a[230:231], v36 offset:4864\n"
    "ds_read_b64_tr_b16 a[232:233], v36 offset:8704\n"
    "v_mfma_f32_16x16x32_bf16 a[124:127], a[252:255], a[28:31], a[124:127]\n"
    "ds_read_b64_tr_b16 a[234:235], v36 offset:8960\n"
    "v_cmp_gt_u32 vcc, 8, v3\n"
    "v_mfma_f32_16x16x32_bf16 a[120:123], a[252:255], v[220:223], a[120:123]\n"
    "ds_read_b64_tr_b16 a[236:237], v36 offset:12800\n"
    "ds_read_b64_tr_b16 a[238:239], v36 offset:13056\n"
    "v_mfma_f32_16x16x32_bf16 a[120:123], a[248:251], v[216:219], a[120:123]\n"
    "ds_read_b64_tr_b16 a[192:193], v36 offset:16896\n"
    "v_lshlrev_b32 v44, 9, v3\n"
    "v_mfma_f32_16x16x32_bf16 a[120:123], a[244:247], v[212:215], a[120:123]\n"
    "ds_read_b64_tr_b16 a[194:195], v36 offset:17152\n"
    "ds_read_b64_tr_b16 a[196:197], v36 offset:20992\n"
    "v_mfma_f32_16x16x32_bf16 a[120:123], a[240:243], v[208:211], a[120:123]\n"
    "ds_read_b64_tr_b16 a[198:199], v36 offset:21248\n"
    "v_mov_b32 v45, 0x7ff00000\n"
    "v_mfma_f32_16x16x32_bf16 a[116:119], a[240:243], v[144:147], a[116:119]\n"
    "ds_read_b64_tr_b16 a[200:201], v36 offset:25088\n"
    "v_cndmask_b32 v3, v45, v44, vcc\n"
    "v_mfma_f32_16x16x32_bf16 a[116:119], a[244:247], v[148:151], a[116:119]\n"
    "ds_read_b64_tr_b16 a[202:203], v36 offset:25344\n"
    "ds_read_b64_tr_b16 a[204:205], v36 offset:29184\n"
    "v_mfma_f32_16x16x32_bf16 a[116:119], a[248:251], v[152:155], a[116:119]\n"
    "v_subrev_u32 v44, 8, v1\n"
    "v_lshlrev_b32 v44, 12, v44\n"
    "v_mfma_f32_16x16x32_bf16 a[116:119], a[252:255], v[156:159], a[116:119]\n"
    "ds_read_b64_tr_b16 a[206:207], v36 offset:29440\n"
    "v_add_u32 v44, s50, v44\n"
    "v_mfma_f32_16x16x32_bf16 a[112:115], a[252:255], v[92:95], a[112:115]\n"
    "v_add_u32 v44, 0x140, v44\n"
    "v_lshlrev_b32 v46, 9, v1\n"
    "v_mfma_f32_16x16x32_bf16 a[112:115], a[248:251], v[88:91], a[112:115]\n"
    "v_add_u32 v46, s49, v46\n"
    "v_add_u32 v46, 0x100, v46\n"
    "v_mfma_f32_16x16x32_bf16 a[112:115], a[244:247], v[84:87], a[112:115]\n"
    "v_cndmask_b32 v44, v44, v46, vcc\n"
    "v_cmp_gt_u32 vcc, 12, v41\n"
    "v_mfma_f32_16x16x32_bf16 a[112:115], a[240:243], v[80:83], a[112:115]\n"
    "s_waitcnt lgkmcnt(0)\n"
    "v_min_u32 v46, 11, v1\n"
    "v_lshlrev_b32 v23, 3, v46\n"
    "v_mfma_f32_16x16x32_bf16 a[128:131], a[224:227], v[80:83], a[128:131]\n"
    "ds_read_b64_tr_b16 a[240:241], v8 offset:512\n"
    "ds_read_b64_tr_b16 a[242:243], v8 offset:768\n"
    "v_mfma_f32_16x16x32_bf16 a[128:131], a[228:231], v[84:87], a[128:131]\n"
    "ds_read_b64_tr_b16 a[244:245], v8 offset:4608\n"
    "v_cndmask_b32 v22, v45, v44, vcc\n"
    "v_mfma_f32_16x16x32_bf16 a[128:131], a[232:235], v[88:91], a[128:131]\n"
    "ds_read_b64_tr_b16 a[246:247], v8 offset:4864\n"
    "ds_read_b64_tr_b16 a[248:249], v8 offset:8704\n"
    "v_mfma_f32_16x16x32_bf16 a[128:131], a[236:239], v[92:95], a[128:131]\n"
    "ds_read_b64_tr_b16 a[250:251], v8 offset:8960\n"
    "v_cmp_gt_u32 vcc, 32, v41\n"
    "v_mfma_f32_16x16x32_bf16 a[132:135], a[236:239], v[156:159], a[132:135]\n"
    "ds_read_b64_tr_b16 a[252:253], v8 offset:12800\n"
    "ds_read_b64_tr_b16 a[254:255], v8 offset:13056\n"
    "v_mfma_f32_16x16x32_bf16 a[132:135], a[232:235], v[152:155], a[132:135]\n"
    "ds_read_b64_tr_b16 a[208:209], v8 offset:16896\n"
    "v_lshlrev_b32 v44, 3, v1\n"
    "v_mfma_f32_16x16x32_bf16 a[132:135], a[228:231], v[148:151], a[132:135]\n"
    "ds_read_b64_tr_b16 a[210:211], v8 offset:17152\n"
    "ds_read_b64_tr_b16 a[212:213], v8 offset:20992\n"
    "v_mfma_f32_16x16x32_bf16 a[132:135], a[224:227], v[144:147], a[132:135]\n"
    "ds_read_b64_tr_b16 a[214:215], v8 offset:21248\n"
    "v_mov_b32 v32, 0\n"
    "v_mfma_f32_16x16x32_bf16 a[140:143], a[192:195], v[144:147], a[140:143]\n"
    "ds_read_b64_tr_b16 a[216:217], v8 offset:25088\n"
    "v_cndmask_b32 v24, v45, v44, vcc\n"
    "v_mfma_f32_16x16x32_bf16 a[140:143], a[196:199], v[148:151], a[140:143]\n"
    "ds_read_b64_tr_b16 a[218:219], v8 offset:25344\n"
    "ds_read_b64_tr_b16 a[220:221], v8 offset:29184\n"
    "v_mfma_f32_16x16x32_bf16 a[140:143], a[200:203], v[152:155], a[140:143]\n"
    "v_mov_b32 v33, 0\n"
    "v_mfma_f32_16x16x32_bf16 a[140:143], a[204:207], v[156:159], a[140:143]\n"
    "ds_read_b64_tr_b16 a[222:223], v8 offset:29440\n"
    "v_mfma_f32_16x16x32_bf16 a[136:139], a[204:207], v[92:95], a[136:139]\n"
    "v_mfma_f32_16x16x32_bf16 a[136:139], a[200:203], v[88:91], a[136:139]\n"
    "v_mfma_f32_16x16x32_bf16 a[136:139], a[196:199], v[84:87], a[136:139]\n"
    "v_mfma_f32_16x16x32_bf16 a[136:139], a[192:195], v[80:83], a[136:139]\n"
    "s_waitcnt lgkmcnt(0)\n"
    "v_mfma_f32_16x16x32_bf16 a[144:147], a[240:243], v[80:83], a[144:147]\n"
    "s_waitcnt vmcnt(8)\n"
    "v_mfma_f32_16x16x32_bf16 a[144:147], a[244:247], v[84:87], a[144:147]\n"
    "s_barrier\n"
    "v_mfma_f32_16x16x32_bf16 a[144:147], a[248:251], v[88:91], a[144:147]\n"
    "ds_read_b64_tr_b16 a[224:225], v36 offset:32768\n"
    "v_mfma_f32_16x16x32_bf16 a[144:147], a[252:255], v[92:95], a[144:147]\n"
    "ds_read_b64_tr_b16 a[226:227], v36 offset:33024\n"
    "v_mfma_f32_16x16x32_bf16 a[148:151], a[252:255], v[156:159], a[148:151]\n"
    "ds_read_b64_tr_b16 a[228:229], v36 offset:36864\n"
    "v_mfma_f32_16x16x32_bf16 a[148:151], a[248:251], v[152:155], a[148:151]\n"
    "v_mfma_f32_16x16x32_bf16 a[148:151], a[244:247], v[148:151], a[148:151]\n"
    "ds_read_b64_tr_b16 a[230:231], v36 offset:37120\n"
    "v_mfma_f32_16x16x32_bf16 a[148:151], a[240:243], v[144:147], a[148:151]\n"
    "ds_read_b64_tr_b16 a[232:233], v36 offset:40960\n"
    "v_mfma_f32_16x16x32_bf16 a[156:159], a[208:211], v[144:147], a[156:159]\n"
    "ds_read_b64_tr_b16 a[234:235], v36 offset:41216\n"
    "v_mfma_f32_16x16x32_bf16 a[156:159], a[212:215], v[148:151], a[156:159]\n"
    "s_add_u32 m0, s65, 0x10000\n"
    "ds_read_b64_tr_b16 a[236:237], v36 offset:45056\n"
    "v_mfma_f32_16x16x32_bf16 a[156:159], a[216:219], v[152:155], a[156:159]\n"
    "s_xor_b32 s69, s80, 0\n"
    "buffer_load_dwordx4 v26, s[16:19], s69 offen offset:0 lds\n"
    "v_mfma_f32_16x16x32_bf16 a[156:159], a[220:223], v[156:159], a[156:159]\n"
    "ds_read_b64_tr_b16 a[238:239], v36 offset:45312\n"
    "v_mfma_f32_16x16x32_bf16 a[152:155], a[220:223], v[92:95], a[152:155]\n"
    "buffer_load_dwordx4 v27, s[16:19], s69 offen offset:1024 lds\n"
    "v_mfma_f32_16x16x32_bf16 a[152:155], a[216:219], v[88:91], a[152:155]\n"
    "v_mfma_f32_16x16x32_bf16 a[152:155], a[212:215], v[84:87], a[152:155]\n"
    "buffer_load_dwordx4 v28, s[16:19], s69 offen offset:2048 lds\n"
    "v_mfma_f32_16x16x32_bf16 a[152:155], a[208:211], v[80:83], a[152:155]\n"
    "s_waitcnt lgkmcnt(0)\n"
    "v_mfma_f32_16x16x32_bf16 a[64:67], a[224:227], v[96:99], a[64:67]\n"
    "ds_read_b64_tr_b16 a[240:241], v36 offset:49152\n"
    "v_mfma_f32_16x16x32_bf16 a[64:67], a[228:231], v[100:103], a[64:67]\n"
    "ds_read_b64_tr_b16 a[242:243], v36 offset:49408\n"
    "v_mfma_f32_16x16x32_bf16 a[64:67], a[232:235], v[104:107], a[64:67]\n"
    "v_mfma_f32_16x16x32_bf16 a[64:67], a[236:239], v[108:111], a[64:67]\n"
    "ds_read_b64_tr_b16 a[244:245], v36 offset:53248\n"
    "v_mfma_f32_16x16x32_bf16 a[68:71], a[236:239], v[172:175], a[68:71]\n"
    "ds_read_b64_tr_b16 a[246:247], v36 offset:53504\n"
    "v_mfma_f32_16x16x32_bf16 a[68:71], a[232:235], v[168:171], a[68:71]\n"
    "v_mfma_f32_16x16x32_bf16 a[68:71], a[228:231], v[164:167], a[68:71]\n"
    "ds_read_b64_tr_b16 a[248:249], v36 offset:57344\n"
    "v_mfma_f32_16x16x32_bf16 a[68:71], a[224:227], v[160:163], a[68:71]\n"
    "ds_read_b64_tr_b16 a[250:251], v36 offset:57600\n"
    "v_mfma_f32_16x16x32_bf16 a[72:75], a[224:227], v[224:227], a[72:75]\n"
    "v_mfma_f32_16x16x32_bf16 a[72:75], a[228:231], v[228:231], a[72:75]\n"
    "ds_read_b64_tr_b16 a[252:253], v36 offset:61440\n"
    "v_mfma_f32_16x16x32_bf16 a[72:75], a[232:235], v[232:235], a[72:75]\n"
    "buffer_store_dwordx2 v[32:33], v24, s[24:27], s51 offen offset:0 sc1\n"
    "v_mfma_f32_16x16x32_bf16 a[72:75], a[236:239], v[236:239], a[72:75]\n"
    "ds_read_b64_tr_b16 a[254:255], v36 offset:61696\n"
    "v_mfma_f32_16x16x32_bf16 a[76:79], a[236:239], a[44:47], a[76:79]\n"
    "buffer_load_dwordx2 v[30:31], v23, s[24:27], s51 offen offset:256 sc1\n"
    "v_mfma_f32_16x16x32_bf16 a[76:79], a[232:235], a[40:43], a[76:79]\n"
    "v_mfma_f32_16x16x32_bf16 a[76:79], a[228:231], a[36:39], a[76:79]\n"
    "buffer_load_dwordx4 v29, s[16:19], s69 offen offset:3072 lds\n"
    "v_mfma_f32_16x16x32_bf16 a[76:79], a[224:227], a[32:35], a[76:79]\n"
    "s_waitcnt lgkmcnt(0)\n"
    "v_mfma_f32_16x16x32_bf16 a[92:95], a[240:243], a[32:35], a[92:95]\n"
    "ds_read_b64_tr_b16 a[224:225], v8 offset:32768\n"
    "v_mfma_f32_16x16x32_bf16 a[92:95], a[244:247], a[36:39], a[92:95]\n"
    "ds_read_b64_tr_b16 a[226:227], v8 offset:33024\n"
    "v_mfma_f32_16x16x32_bf16 a[92:95], a[248:251], a[40:43], a[92:95]\n"
    "v_mfma_f32_16x16x32_bf16 a[92:95], a[252:255], a[44:47], a[92:95]\n"
    "ds_read_b64_tr_b16 a[228:229], v8 offset:36864\n"
    "v_mfma_f32_16x16x32_bf16 a[88:91], a[252:255], v[236:239], a[88:91]\n"
    "ds_read_b64_tr_b16 a[230:231], v8 offset:37120\n"
    "v_mfma_f32_16x16x32_bf16 a[88:91], a[248:251], v[232:235], a[88:91]\n"
    "v_mfma_f32_16x16x32_bf16 a[88:91], a[244:247], v[228:231], a[88:91]\n"
    "ds_read_b64_tr_b16 a[232:233], v8 offset:40960\n"
    "v_mfma_f32_16x16x32_bf16 a[88:91], a[240:243], v[224:227], a[88:91]\n"
    "ds_read_b64_tr_b16 a[234:235], v8 offset:41216\n"
    "v_mfma_f32_16x16x32_bf16 a[84:87], a[240:243], v[160:163], a[84:87]\n"
    "v_mfma_f32_16x16x32_bf16 a[84:87], a[244:247], v[164:167], a[84:87]\n"
    "s_add_u32 m0, s65, 0x14000\n"
    "ds_read_b64_tr_b16 a[236:237], v8 offset:45056\n"
    "v_mfma_f32_16x16x32_bf16 a[84:87], a[248:251], v[168:171], a[84:87]\n"
    "s_xor_b32 s69, s80, 0x100000\n"
    "buffer_load_dwordx4 v26, s[16:19], s69 offen offset:0 lds\n"
    "v_mfma_f32_16x16x32_bf16 a[84:87], a[252:255], v[172:175], a[84:87]\n"
    "ds_read_b64_tr_b16 a[238:239], v8 offset:45312\n"
    "v_mfma_f32_16x16x32_bf16 a[80:83], a[252:255], v[108:111], a[80:83]\n"
    "buffer_load_dwordx4 v27, s[16:19], s69 offen offset:1024 lds\n"
    "v_mfma_f32_16x16x32_bf16 a[80:83], a[248:251], v[104:107], a[80:83]\n"
    "v_mfma_f32_16x16x32_bf16 a[80:83], a[244:247], v[100:103], a[80:83]\n"
    "buffer_load_dwordx4 v28, s[16:19], s69 offen offset:2048 lds\n"
    "v_mfma_f32_16x16x32_bf16 a[80:83], a[240:243], v[96:99], a[80:83]\n"
    "s_waitcnt lgkmcnt(0)\n"
    "v_mfma_f32_16x16x32_bf16 a[96:99], a[224:227], v[96:99], a[96:99]\n"
    "ds_read_b64_tr_b16 a[240:241], v8 offset:49152\n"
    "v_mfma_f32_16x16x32_bf16 a[96:99], a[228:231], v[100:103], a[96:99]\n"
    "ds_read_b64_tr_b16 a[242:243], v8 offset:49408\n"
    "v_mfma_f32_16x16x32_bf16 a[96:99], a[232:235], v[104:107], a[96:99]\n"
    "v_mfma_f32_16x16x32_bf16 a[96:99], a[236:239], v[108:111], a[96:99]\n"
    "ds_read_b64_tr_b16 a[244:245], v8 offset:53248\n"
    "v_mfma_f32_16x16x32_bf16 a[100:103], a[236:239], v[172:175], a[100:103]\n"
    "ds_read_b64_tr_b16 a[246:247], v8 offset:53504\n"
    "v_mfma_f32_16x16x32_bf16 a[100:103], a[232:235], v[168:171], a[100:103]\n"
    "v_mfma_f32_16x16x32_bf16 a[100:103], a[228:231], v[164:167], a[100:103]\n"
    "ds_read_b64_tr_b16 a[248:249], v8 offset:57344\n"
    "v_mfma_f32_16x16x32_bf16 a[100:103], a[224:227], v[160:163], a[100:103]\n"
    "ds_read_b64_tr_b16 a[250:251], v8 offset:57600\n"
    "v_mfma_f32_16x16x32_bf16 a[104:107], a[224:227], v[224:227], a[104:107]\n"
    "v_mfma_f32_16x16x32_bf16 a[104:107], a[228:231], v[228:231], a[104:107]\n"
    "ds_read_b64_tr_b16 a[252:253], v8 offset:61440\n"
    "v_mfma_f32_16x16x32_bf16 a[104:107], a[232:235], v[232:235], a[104:107]\n"
    "buffer_load_dwordx4 v29, s[16:19], s69 offen offset:3072 lds\n"
    "v_mfma_f32_16x16x32_bf16 a[104:107], a[236:239], v[236:239], a[104:107]\n"
    "ds_read_b64_tr_b16 a[254:255], v8 offset:61696\n"
    "v_mfma_f32_16x16x32_bf16 a[108:111], a[236:239], a[44:47], a[108:111]\n"
    "v_mfma_f32_16x16x32_bf16 a[108:111], a[232:235], a[40:43], a[108:111]\n"
    "v_mfma_f32_16x16x32_bf16 a[108:111], a[228:231], a[36:39], a[108:111]\n"
    "v_mfma_f32_16x16x32_bf16 a[108:111], a[224:227], a[32:35], a[108:111]\n"
    "s_waitcnt lgkmcnt(0)\n"
    "v_mfma_f32_16x16x32_bf16 a[124:127], a[240:243], a[32:35], a[124:127]\n"
    "ds_read_b64_tr_b16 a[224:225], v36 offset:33280\n"
    "ds_read_b64_tr_b16 a[226:227], v36 offset:33536\n"
    "v_mfma_f32_16x16x32_bf16 a[124:127], a[244:247], a[36:39], a[124:127]\n"
    "ds_read_b64_tr_b16 a[228:229], v36 offset:37376\n"
    "v_mfma_f32_16x16x32_bf16 a[124:127], a[248:251], a[40:43], a[124:127]\n"
    "ds_read_b64_tr_b16 a[230:231], v36 offset:37632\n"
    "ds_read_b64_tr_b16 a[232:233], v36 offset:41472\n"
    "v_mfma_f32_16x16x32_bf16 a[124:127], a[252:255], a[44:47], a[124:127]\n"
    "ds_read_b64_tr_b16 a[234:235], v36 offset:41728\n"
    "v_mfma_f32_16x16x32_bf16 a[120:123], a[252:255], v[236:239], a[120:123]\n"
    "ds_read_b64_tr_b16 a[236:237], v36 offset:45568\n"
    "ds_read_b64_tr_b16 a[238:239], v36 offset:45824\n"
    "v_mfma_f32_16x16x32_bf16 a[120:123], a[248:251], v[232:235], a[120:123]\n"
    "ds_read_b64_tr_b16 a[192:193], v36 offset:49664\n"
    "v_mfma_f32_16x16x32_bf16 a[120:123], a[244:247], v[228:231], a[120:123]\n"
    "ds_read_b64_tr_b16 a[194:195], v36 offset:49920\n"
    "ds_read_b64_tr_b16 a[196:197], v36 offset:53760\n"
    "v_mfma_f32_16x16x32_bf16 a[120:123], a[240:243], v[224:227], a[120:123]\n"
    "ds_read_b64_tr_b16 a[198:199], v36 offset:54016\n"
    "v_mfma_f32_16x16x32_bf16 a[116:119], a[240:243], v[160:163], a[116:119]\n"
    "ds_read_b64_tr_b16 a[200:201], v36 offset:57856\n"
    "v_mfma_f32_16x16x32_bf16 a[116:119], a[244:247], v[164:167], a[116:119]\n"
    "ds_read_b64_tr_b16 a[202:203], v36 offset:58112\n"
    "ds_read_b64_tr_b16 a[204:205], v36 offset:61952\n"
    "v_mfma_f32_16x16x32_bf16 a[116:119], a[248:251], v[168:171], a[116:119]\n"
    "v_mfma_f32_16x16x32_bf16 a[116:119], a[252:255], v[172:175], a[116:119]\n"
    "ds_read_b64_tr_b16 a[206:207], v36 offset:62208\n"
    "v_mfma_f32_16x16x32_bf16 a[112:115], a[252:255], v[108:111], a[112:115]\n"
    "v_mfma_f32_16x16x32_bf16 a[112:115], a[248:251], v[104:107], a[112:115]\n"
    "v_mfma_f32_16x16x32_bf16 a[112:115], a[244:247], v[100:103], a[112:115]\n"
    "v_mfma_f32_16x16x32_bf16 a[112:115], a[240:243], v[96:99], a[112:115]\n"
    "s_waitcnt lgkmcnt(0)\n"
    "v_mfma_f32_16x16x32_bf16 a[128:131], a[224:227], v[96:99], a[128:131]\n"
    "ds_read_b64_tr_b16 a[240:241], v8 offset:33280\n"
    "ds_read_b64_tr_b16 a[242:243], v8 offset:33536\n"
    "v_mfma_f32_16x16x32_bf16 a[128:131], a[228:231], v[100:103], a[128:131]\n"
    "ds_read_b64_tr_b16 a[244:245], v8 offset:37376\n"
    "v_mfma_f32_16x16x32_bf16 a[128:131], a[232:235], v[104:107], a[128:131]\n"
    "ds_read_b64_tr_b16 a[246:247], v8 offset:37632\n"
    "ds_read_b64_tr_b16 a[248:249], v8 offset:41472\n"
    "v_mfma_f32_16x16x32_bf16 a[128:131], a[236:239], v[108:111], a[128:131]\n"
    "ds_read_b64_tr_b16 a[250:251], v8 offset:41728\n"
    "v_mfma_f32_16x16x32_bf16 a[132:135], a[236:239], v[172:175], a[132:135]\n"
    "ds_read_b64_tr_b16 a[252:253], v8 offset:45568\n"
    "ds_read_b64_tr_b16 a[254:255], v8 offset:45824\n"
    "v_mfma_f32_16x16x32_bf16 a[132:135], a[232:235], v[168:171], a[132:135]\n"
    "ds_read_b64_tr_b16 a[208:209], v8 offset:49664\n"
    "v_mfma_f32_16x16x32_bf16 a[132:135], a[228:231], v[164:167], a[132:135]\n"
    "ds_read_b64_tr_b16 a[210:211], v8 offset:49920\n"
    "ds_read_b64_tr_b16 a[212:213], v8 offset:53760\n"
    "v_mfma_f32_16x16x32_bf16 a[132:135], a[224:227], v[160:163], a[132:135]\n"
    "ds_read_b64_tr_b16 a[214:215], v8 offset:54016\n"
    "v_mfma_f32_16x16x32_bf16 a[140:143], a[192:195], v[160:163], a[140:143]\n"
    "ds_read_b64_tr_b16 a[216:217], v8 offset:57856\n"
    "v_mfma_f32_16x16x32_bf16 a[140:143], a[196:199], v[164:167], a[140:143]\n"
    "ds_read_b64_tr_b16 a[218:219], v8 offset:58112\n"
    "ds_read_b64_tr_b16 a[220:221], v8 offset:61952\n"
    "v_mfma_f32_16x16x32_bf16 a[140:143], a[200:203], v[168:171], a[140:143]\n"
    "v_mfma_f32_16x16x32_bf16 a[140:143], a[204:207], v[172:175], a[140:143]\n"
    "ds_read_b64_tr_b16 a[222:223], v8 offset:62208\n"
    "v_mfma_f32_16x16x32_bf16 a[136:139], a[204:207], v[108:111], a[136:139]\n"
    "v_mfma_f32_16x16x32_bf16 a[136:139], a[200:203], v[104:107], a[136:139]\n"
    "v_mfma_f32_16x16x32_bf16 a[136:139], a[196:199], v[100:103], a[136:139]\n"
    "v_mfma_f32_16x16x32_bf16 a[136:139], a[192:195], v[96:99], a[136:139]\n"
    "s_waitcnt lgkmcnt(0)\n"
    "v_mfma_f32_16x16x32_bf16 a[144:147], a[240:243], v[96:99], a[144:147]\n"
    "s_waitcnt vmcnt(10)\n"
    "v_mfma_f32_16x16x32_bf16 a[144:147], a[244:247], v[100:103], a[144:147]\n"
    "s_barrier\n"
    "v_mfma_f32_16x16x32_bf16 a[144:147], a[248:251], v[104:107], a[144:147]\n"
    "ds_read_b64_tr_b16 a[224:225], v9 offset:0\n"
    "v_mfma_f32_16x16x32_bf16 a[144:147], a[252:255], v[108:111], a[144:147]\n"
    "ds_read_b64_tr_b16 a[226:227], v9 offset:256\n"
    "v_mfma_f32_16x16x32_bf16 a[148:151], a[252:255], v[172:175], a[148:151]\n"
    "ds_read_b64_tr_b16 a[228:229], v9 offset:4096\n"
    "v_mfma_f32_16x16x32_bf16 a[148:151], a[248:251], v[168:171], a[148:151]\n"
    "v_mfma_f32_16x16x32_bf16 a[148:151], a[244:247], v[164:167], a[148:151]\n"
    "ds_read_b64_tr_b16 a[230:231], v9 offset:4352\n"
    "v_mfma_f32_16x16x32_bf16 a[148:151], a[240:243], v[160:163], a[148:151]\n"
    "ds_read_b64_tr_b16 a[232:233], v9 offset:8192\n"
    "v_mfma_f32_16x16x32_bf16 a[156:159], a[208:211], v[160:163], a[156:159]\n"
    "ds_read_b64_tr_b16 a[234:235], v9 offset:8448\n"
    "v_mfma_f32_16x16x32_bf16 a[156:159], a[212:215], v[164:167], a[156:159]\n"
    "s_add_u32 m0, s65, 0x18000\n"
    "ds_read_b64_tr_b16 a[236:237], v9 offset:12288\n"
    "v_mfma_f32_16x16x32_bf16 a[156:159], a[216:219], v[168:171], a[156:159]\n"
    "s_xor_b32 s69, s80, 0x200000\n"
    "buffer_load_dwordx4 v26, s[16:19], s69 offen offset:0 lds\n"
    "v_mfma_f32_16x16x32_bf16 a[156:159], a[220:223], v[172:175], a[156:159]\n"
    "ds_read_b64_tr_b16 a[238:239], v9 offset:12544\n"
    "v_mfma_f32_16x16x32_bf16 a[152:155], a[220:223], v[108:111], a[152:155]\n"
    "buffer_load_dwordx4 v27, s[16:19], s69 offen offset:1024 lds\n"
    "v_mfma_f32_16x16x32_bf16 a[152:155], a[216:219], v[104:107], a[152:155]\n"
    "v_mfma_f32_16x16x32_bf16 a[152:155], a[212:215], v[100:103], a[152:155]\n"
    "buffer_load_dwordx4 v28, s[16:19], s69 offen offset:2048 lds\n"
    "v_mfma_f32_16x16x32_bf16 a[152:155], a[208:211], v[96:99], a[152:155]\n"
    "s_waitcnt lgkmcnt(0)\n"
    "v_mfma_f32_16x16x32_bf16 a[64:67], a[224:227], v[112:115], a[64:67]\n"
    "ds_read_b64_tr_b16 a[240:241], v9 offset:16384\n"
    "v_mfma_f32_16x16x32_bf16 a[64:67], a[228:231], v[116:119], a[64:67]\n"
    "ds_read_b64_tr_b16 a[242:243], v9 offset:16640\n"
    "v_mfma_f32_16x16x32_bf16 a[64:67], a[232:235], v[120:123], a[64:67]\n"
    "v_mfma_f32_16x16x32_bf16 a[64:67], a[236:239], v[124:127], a[64:67]\n"
    "ds_read_b64_tr_b16 a[244:245], v9 offset:20480\n"
    "v_mfma_f32_16x16x32_bf16 a[68:71], a[236:239], v[188:191], a[68:71]\n"
    "ds_read_b64_tr_b16 a[246:247], v9 offset:20736\n"
    "v_mfma_f32_16x16x32_bf16 a[68:71], a[232:235], v[184:187], a[68:71]\n"
    "v_mfma_f32_16x16x32_bf16 a[68:71], a[228:231], v[180:183], a[68:71]\n"
    "ds_read_b64_tr_b16 a[248:249], v9 offset:24576\n"
    "v_mfma_f32_16x16x32_bf16 a[68:71], a[224:227], v[176:179], a[68:71]\n"
    "ds_read_b64_tr_b16 a[250:251], v9 offset:24832\n"
    "v_mfma_f32_16x16x32_bf16 a[72:75], a[224:227], v[240:243], a[72:75]\n"
    "v_mfma_f32_16x16x32_bf16 a[72:75], a[228:231], v[244:247], a[72:75]\n"
    "ds_read_b64_tr_b16 a[252:253], v9 offset:28672\n"
    "v_mfma_f32_16x16x32_bf16 a[72:75], a[232:235], v[248:251], a[72:75]\n"
    "buffer_load_dwordx4 v29, s[16:19], s69 offen offset:3072 lds\n"
    "v_mfma_f32_16x16x32_bf16 a[72:75], a[236:239], v[252:255], a[72:75]\n"
    "s_add_u32 m0, s65, 0x1c000\n"
    "ds_read_b64_tr_b16 a[254:255], v9 offset:28928\n"
    "v_mfma_f32_16x16x32_bf16 a[76:79], a[236:239], a[60:63], a[76:79]\n"
    "s_xor_b32 s69, s80, 0x300000\n"
    "buffer_load_dwordx4 v26, s[16:19], s69 offen offset:0 lds\n"
    "v_mfma_f32_16x16x32_bf16 a[76:79], a[232:235], a[56:59], a[76:79]\n"
    "v_mfma_f32_16x16x32_bf16 a[76:79], a[228:231], a[52:55], a[76:79]\n"
    "buffer_load_dwordx4 v27, s[16:19], s69 offen offset:1024 lds\n"
    "v_mfma_f32_16x16x32_bf16 a[76:79], a[224:227], a[48:51], a[76:79]\n"
    "s_waitcnt lgkmcnt(0)\n"
    "v_mfma_f32_16x16x32_bf16 a[92:95], a[240:243], a[48:51], a[92:95]\n"
    "ds_read_b64_tr_b16 a[224:225], v21 offset:0\n"
    "v_mfma_f32_16x16x32_bf16 a[92:95], a[244:247], a[52:55], a[92:95]\n"
    "ds_read_b64_tr_b16 a[226:227], v21 offset:256\n"
    "v_mfma_f32_16x16x32_bf16 a[92:95], a[248:251], a[56:59], a[92:95]\n"
    "s_waitcnt vmcnt(11)\n"
    "v_min_u32 v25, 11, v1\n"
    "v_mfma_f32_16x16x32_bf16 a[92:95], a[252:255], a[60:63], a[92:95]\n"
    "ds_read_b64_tr_b16 a[228:229], v21 offset:4096\n"
    "v_mfma_f32_16x16x32_bf16 a[88:91], a[252:255], v[252:255], a[88:91]\n"
    "ds_read_b64_tr_b16 a[230:231], v21 offset:4352\n"
    "v_mfma_f32_16x16x32_bf16 a[88:91], a[248:251], v[248:251], a[88:91]\n"
    "v_lshrrev_b32 v25, v25, v31\n"
    "v_and_b32 v25, 1, v25\n"
    "v_mfma_f32_16x16x32_bf16 a[88:91], a[244:247], v[244:247], a[88:91]\n"
    "ds_read_b64_tr_b16 a[232:233], v21 offset:8192\n"
    "v_mfma_f32_16x16x32_bf16 a[88:91], a[240:243], v[240:243], a[88:91]\n"
    "ds_read_b64_tr_b16 a[234:235], v21 offset:8448\n"
    "v_mfma_f32_16x16x32_bf16 a[84:87], a[240:243], v[176:179], a[84:87]\n"
    "v_cmp_eq_u32 vcc, 0, v25\n"
    "v_mov_b32 v33, v30\n"
    "v_mfma_f32_16x16x32_bf16 a[84:87], a[244:247], v[180:183], a[84:87]\n"
    "ds_read_b64_tr_b16 a[236:237], v21 offset:12288\n"
    "v_mfma_f32_16x16x32_bf16 a[84:87], a[248:251], v[184:187], a[84:87]\n"
    "buffer_load_dwordx4 v28, s[16:19], s69 offen offset:2048 lds\n"
    "v_mfma_f32_16x16x32_bf16 a[84:87], a[252:255], v[188:191], a[84:87]\n"
    "ds_read_b64_tr_b16 a[238:239], v21 offset:12544\n"
    "v_mfma_f32_16x16x32_bf16 a[80:83], a[252:255], v[124:127], a[80:83]\n"
    "buffer_load_dwordx4 v29, s[16:19], s69 offen offset:3072 lds\n"
    "v_mfma_f32_16x16x32_bf16 a[80:83], a[248:251], v[120:123], a[80:83]\n"
    "s_and_b32 s87, vcc_lo, 0xfff\n"
    "v_mov_b32 v32, s87\n"
    "v_mfma_f32_16x16x32_bf16 a[80:83], a[244:247], v[116:119], a[80:83]\n"
    "v_mfma_f32_16x16x32_bf16 a[80:83], a[240:243], v[112:115], a[80:83]\n"
    "s_waitcnt lgkmcnt(0)\n"
    "v_mfma_f32_16x16x32_bf16 a[96:99], a[224:227], v[112:115], a[96:99]\n"
    "ds_read_b64_tr_b16 a[240:241], v21 offset:16384\n"
    "v_mfma_f32_16x16x32_bf16 a[96:99], a[228:231], v[116:119], a[96:99]\n"
    "ds_read_b64_tr_b16 a[242:243], v21 offset:16640\n"
    "v_mfma_f32_16x16x32_bf16 a[96:99], a[232:235], v[120:123], a[96:99]\n"
    "v_mfma_f32_16x16x32_bf16 a[96:99], a[236:239], v[124:127], a[96:99]\n"
    "ds_read_b64_tr_b16 a[244:245], v21 offset:20480\n"
    "v_mfma_f32_16x16x32_bf16 a[100:103], a[236:239], v[188:191], a[100:103]\n"
    "ds_read_b64_tr_b16 a[246:247], v21 offset:20736\n"
    "v_mfma_f32_16x16x32_bf16 a[100:103], a[232:235], v[184:187], a[100:103]\n"
    "v_mfma_f32_16x16x32_bf16 a[100:103], a[228:231], v[180:183], a[100:103]\n"
    "ds_read_b64_tr_b16 a[248:249], v21 offset:24576\n"
    "v_mfma_f32_16x16x32_bf16 a[100:103], a[224:227], v[176:179], a[100:103]\n"
    "ds_read_b64_tr_b16 a[250:251], v21 offset:24832\n"
    "v_mfma_f32_16x16x32_bf16 a[104:107], a[224:227], v[240:243], a[104:107]\n"
    "v_mfma_f32_16x16x32_bf16 a[104:107], a[228:231], v[244:247], a[104:107]\n"
    "ds_read_b64_tr_b16 a[252:253], v21 offset:28672\n"
    "v_mfma_f32_16x16x32_bf16 a[104:107], a[232:235], v[248:251], a[104:107]\n"
    "buffer_store_dwordx2 v[32:33], v22, s[24:27], 0 offen offset:0 sc1\n"
    "v_mfma_f32_16x16x32_bf16 a[104:107], a[236:239], v[252:255], a[104:107]\n"
    "ds_read_b64_tr_b16 a[254:255], v21 offset:28928\n"
    "v_mfma_f32_16x16x32_bf16 a[108:111], a[236:239], a[60:63], a[108:111]\n"
    "v_mfma_f32_16x16x32_bf16 a[108:111], a[232:235], a[56:59], a[108:111]\n"
    "v_mfma_f32_16x16x32_bf16 a[108:111], a[228:231], a[52:55], a[108:111]\n"
    "v_mfma_f32_16x16x32_bf16 a[108:111], a[224:227], a[48:51], a[108:111]\n"
    "s_waitcnt lgkmcnt(0)\n"
    "v_mfma_f32_16x16x32_bf16 a[124:127], a[240:243], a[48:51], a[124:127]\n"
    "ds_read_b64_tr_b16 a[224:225], v9 offset:512\n"
    "ds_read_b64_tr_b16 a[226:227], v9 offset:768\n"
    "v_mfma_f32_16x16x32_bf16 a[124:127], a[244:247], a[52:55], a[124:127]\n"
    "ds_read_b64_tr_b16 a[228:229], v9 offset:4608\n"
    "v_mfma_f32_16x16x32_bf16 a[124:127], a[248:251], a[56:59], a[124:127]\n"
    "ds_read_b64_tr_b16 a[230:231], v9 offset:4864\n"
    "ds_read_b64_tr_b16 a[232:233], v9 offset:8704\n"
    "v_mfma_f32_16x16x32_bf16 a[124:127], a[252:255], a[60:63], a[124:127]\n"
    "ds_read_b64_tr_b16 a[234:235], v9 offset:8960\n"
    "v_mfma_f32_16x16x32_bf16 a[120:123], a[252:255], v[252:255], a[120:123]\n"
    "ds_read_b64_tr_b16 a[236:237], v9 offset:12800\n"
    "ds_read_b64_tr_b16 a[238:239], v9 offset:13056\n"
    "v_mfma_f32_16x16x32_bf16 a[120:123], a[248:251], v[248:251], a[120:123]\n"
    "ds_read_b64_tr_b16 a[192:193], v9 offset:16896\n"
    "v_mfma_f32_16x16x32_bf16 a[120:123], a[244:247], v[244:247], a[120:123]\n"
    "ds_read_b64_tr_b16 a[194:195], v9 offset:17152\n"
    "ds_read_b64_tr_b16 a[196:197], v9 offset:20992\n"
    "v_mfma_f32_16x16x32_bf16 a[120:123], a[240:243], v[240:243], a[120:123]\n"
    "ds_read_b64_tr_b16 a[198:199], v9 offset:21248\n"
    "v_mfma_f32_16x16x32_bf16 a[116:119], a[240:243], v[176:179], a[116:119]\n"
    "ds_read_b64_tr_b16 a[200:201], v9 offset:25088\n"
    "v_mfma_f32_16x16x32_bf16 a[116:119], a[244:247], v[180:183], a[116:119]\n"
    "ds_read_b64_tr_b16 a[202:203], v9 offset:25344\n"
    "ds_read_b64_tr_b16 a[204:205], v9 offset:29184\n"
    "v_mfma_f32_16x16x32_bf16 a[116:119], a[248:251], v[184:187], a[116:119]\n"
    "v_mfma_f32_16x16x32_bf16 a[116:119], a[252:255], v[188:191], a[116:119]\n"
    "ds_read_b64_tr_b16 a[206:207], v9 offset:29440\n"
    "v_mfma_f32_16x16x32_bf16 a[112:115], a[252:255], v[124:127], a[112:115]\n"
    "v_mfma_f32_16x16x32_bf16 a[112:115], a[248:251], v[120:123], a[112:115]\n"
    "v_mfma_f32_16x16x32_bf16 a[112:115], a[244:247], v[116:119], a[112:115]\n"
    "v_mfma_f32_16x16x32_bf16 a[112:115], a[240:243], v[112:115], a[112:115]\n"
    "s_waitcnt lgkmcnt(0)\n"
    "v_mfma_f32_16x16x32_bf16 a[128:131], a[224:227], v[112:115], a[128:131]\n"
    "ds_read_b64_tr_b16 a[240:241], v21 offset:512\n"
    "ds_read_b64_tr_b16 a[242:243], v21 offset:768\n"
    "v_mfma_f32_16x16x32_bf16 a[128:131], a[228:231], v[116:119], a[128:131]\n"
    "ds_read_b64_tr_b16 a[244:245], v21 offset:4608\n"
    "v_mfma_f32_16x16x32_bf16 a[128:131], a[232:235], v[120:123], a[128:131]\n"
    "ds_read_b64_tr_b16 a[246:247], v21 offset:4864\n"
    "ds_read_b64_tr_b16 a[248:249], v21 offset:8704\n"
    "v_mfma_f32_16x16x32_bf16 a[128:131], a[236:239], v[124:127], a[128:131]\n"
    "ds_read_b64_tr_b16 a[250:251], v21 offset:8960\n"
    "v_mfma_f32_16x16x32_bf16 a[132:135], a[236:239], v[188:191], a[132:135]\n"
    "ds_read_b64_tr_b16 a[252:253], v21 offset:12800\n"
    "ds_read_b64_tr_b16 a[254:255], v21 offset:13056\n"
    "v_mfma_f32_16x16x32_bf16 a[132:135], a[232:235], v[184:187], a[132:135]\n"
    "ds_read_b64_tr_b16 a[208:209], v21 offset:16896\n"
    "v_mfma_f32_16x16x32_bf16 a[132:135], a[228:231], v[180:183], a[132:135]\n"
    "ds_read_b64_tr_b16 a[210:211], v21 offset:17152\n"
    "ds_read_b64_tr_b16 a[212:213], v21 offset:20992\n"
    "v_mfma_f32_16x16x32_bf16 a[132:135], a[224:227], v[176:179], a[132:135]\n"
    "ds_read_b64_tr_b16 a[214:215], v21 offset:21248\n"
    "v_mfma_f32_16x16x32_bf16 a[140:143], a[192:195], v[176:179], a[140:143]\n"
    "ds_read_b64_tr_b16 a[216:217], v21 offset:25088\n"
    "v_mfma_f32_16x16x32_bf16 a[140:143], a[196:199], v[180:183], a[140:143]\n"
    "ds_read_b64_tr_b16 a[218:219], v21 offset:25344\n"
    "ds_read_b64_tr_b16 a[220:221], v21 offset:29184\n"
    "v_mfma_f32_16x16x32_bf16 a[140:143], a[200:203], v[184:187], a[140:143]\n"
    "v_mfma_f32_16x16x32_bf16 a[140:143], a[204:207], v[188:191], a[140:143]\n"
    "ds_read_b64_tr_b16 a[222:223], v21 offset:29440\n"
    "v_mfma_f32_16x16x32_bf16 a[136:139], a[204:207], v[124:127], a[136:139]\n"
    "v_mfma_f32_16x16x32_bf16 a[136:139], a[200:203], v[120:123], a[136:139]\n"
    "v_mfma_f32_16x16x32_bf16 a[136:139], a[196:199], v[116:119], a[136:139]\n"
    "v_mfma_f32_16x16x32_bf16 a[136:139], a[192:195], v[112:115], a[136:139]\n"
    "s_waitcnt lgkmcnt(0)\n"
    "v_mfma_f32_16x16x32_bf16 a[144:147], a[240:243], v[112:115], a[144:147]\n"
    "s_waitcnt vmcnt(13)\n"
    "v_mfma_f32_16x16x32_bf16 a[144:147], a[244:247], v[116:119], a[144:147]\n"
    "s_barrier\n"
    "v_mfma_f32_16x16x32_bf16 a[144:147], a[248:251], v[120:123], a[144:147]\n"
    "ds_read_b64_tr_b16 a[224:225], v34 offset:0\n"
    "v_mfma_f32_16x16x32_bf16 a[144:147], a[252:255], v[124:127], a[144:147]\n"
    "ds_read_b64_tr_b16 a[226:227], v34 offset:256\n"
    "v_mfma_f32_16x16x32_bf16 a[148:151], a[252:255], v[188:191], a[148:151]\n"
    "ds_read_b64_tr_b16 a[228:229], v34 offset:4096\n"
    "v_mfma_f32_16x16x32_bf16 a[148:151], a[248:251], v[184:187], a[148:151]\n"
    "v_mfma_f32_16x16x32_bf16 a[148:151], a[244:247], v[180:183], a[148:151]\n"
    "ds_read_b64_tr_b16 a[230:231], v34 offset:4352\n"
    "v_mfma_f32_16x16x32_bf16 a[148:151], a[240:243], v[176:179], a[148:151]\n"
    "ds_read_b64_tr_b16 a[232:233], v34 offset:8192\n"
    "v_mfma_f32_16x16x32_bf16 a[156:159], a[208:211], v[176:179], a[156:159]\n"
    "ds_read_b64_tr_b16 a[234:235], v34 offset:8448\n"
    "v_mfma_f32_16x16x32_bf16 a[156:159], a[212:215], v[180:183], a[156:159]\n"
    "s_add_u32 m0, s65, 0x20000\n"
    "ds_read_b64_tr_b16 a[236:237], v34 offset:12288\n"
    "v_mfma_f32_16x16x32_bf16 a[156:159], a[216:219], v[184:187], a[156:159]\n"
    "s_xor_b32 s69, s80, 0x80\n"
    "buffer_load_dwordx4 v26, s[16:19], s69 offen offset:0 lds\n"
    "v_mfma_f32_16x16x32_bf16 a[156:159], a[220:223], v[188:191], a[156:159]\n"
    "ds_read_b64_tr_b16 a[238:239], v34 offset:12544\n"
    "v_mfma_f32_16x16x32_bf16 a[152:155], a[220:223], v[124:127], a[152:155]\n"
    "buffer_load_dwordx4 v27, s[16:19], s69 offen offset:1024 lds\n"
    "v_mfma_f32_16x16x32_bf16 a[152:155], a[216:219], v[120:123], a[152:155]\n"
    "v_mfma_f32_16x16x32_bf16 a[152:155], a[212:215], v[116:119], a[152:155]\n"
    "buffer_load_dwordx4 v28, s[16:19], s69 offen offset:2048 lds\n"
    "v_mfma_f32_16x16x32_bf16 a[152:155], a[208:211], v[112:115], a[152:155]\n"
    "s_waitcnt lgkmcnt(0)\n"
    "v_mfma_f32_16x16x32_bf16 a[160:163], a[224:227], v[64:67], 0\n"
    "ds_read_b64_tr_b16 a[240:241], v34 offset:8\n"
    "v_mfma_f32_16x16x32_bf16 a[160:163], a[228:231], v[68:71], a[160:163]\n"
    "ds_read_b64_tr_b16 a[242:243], v34 offset:264\n"
    "v_mfma_f32_16x16x32_bf16 a[160:163], a[232:235], v[72:75], a[160:163]\n"
    "v_mfma_f32_16x16x32_bf16 a[160:163], a[236:239], v[76:79], a[160:163]\n"
    "ds_read_b64_tr_b16 a[244:245], v34 offset:4104\n"
    "v_mfma_f32_16x16x32_bf16 a[164:167], a[236:239], v[140:143], 0\n"
    "ds_read_b64_tr_b16 a[246:247], v34 offset:4360\n"
    "v_mul_f32 v8, v37, v37\n"
    "v_mfma_f32_16x16x32_bf16 a[164:167], a[232:235], v[136:139], a[164:167]\n"
    "v_mul_f32 v9, v38, v38\n"
    "v_mul_f32 v21, v39, v39\n"
    "v_mfma_f32_16x16x32_bf16 a[164:167], a[228:231], v[132:135], a[164:167]\n"
    "ds_read_b64_tr_b16 a[248:249], v34 offset:8200\n"
    "v_mul_f32 v36, v20, v20\n"
    "v_mfma_f32_16x16x32_bf16 a[164:167], a[224:227], v[128:131], a[164:167]\n"
    "ds_read_b64_tr_b16 a[250:251], v34 offset:8456\n"
    "v_accvgpr_read_b32 v40, a64\n"
    "v_mfma_f32_16x16x32_bf16 a[168:171], a[224:227], v[192:195], 0\n"
    "v_accvgpr_read_b32 v44, a80\n"
    "v_accvgpr_read_b32 v41, a65\n"
    "v_mfma_f32_16x16x32_bf16 a[168:171], a[228:231], v[196:199], a[168:171]\n"
    "ds_read_b64_tr_b16 a[252:253], v34 offset:12296\n"
    "v_accvgpr_read_b32 v45, a81\n"
    "v_mfma_f32_16x16x32_bf16 a[168:171], a[232:235], v[200:203], a[168:171]\n"
    "buffer_load_dwordx4 v29, s[16:19], s69 offen offset:3072 lds\n"
    "v_mfma_f32_16x16x32_bf16 a[168:171], a[236:239], v[204:207], a[168:171]\n"
    "s_add_u32 m0, s65, 0x24000\n"
    "ds_read_b64_tr_b16 a[254:255], v34 offset:12552\n"
    "v_accvgpr_read_b32 v42, a66\n"
    "v_mfma_f32_16x16x32_bf16 a[172:175], a[236:239], a[12:15], 0\n"
    "s_xor_b32 s69, s80, 0x100080\n"
    "buffer_load_dwordx4 v26, s[16:19], s69 offen offset:0 lds\n"
    "v_mfma_f32_16x16x32_bf16 a[172:175], a[232:235], a[8:11], a[172:175]\n"
    "v_accvgpr_read_b32 v46, a82\n"
    "v_accvgpr_read_b32 v43, a67\n"
    "v_mfma_f32_16x16x32_bf16 a[172:175], a[228:231], a[4:7], a[172:175]\n"
    "buffer_load_dwordx4 v27, s[16:19], s69 offen offset:1024 lds\n"
    "v_mfma_f32_16x16x32_bf16 a[172:175], a[224:227], a[0:3], a[172:175]\n"
    "s_waitcnt lgkmcnt(0)\n"
    "v_accvgpr_read_b32 v47, a83\n"
    "v_mul_f32 v40, v8, v40\n"
    "v_mfma_f32_16x16x32_bf16 a[188:191], a[240:243], a[0:3], 0\n"
    "ds_read_b64_tr_b16 a[224:225], v34 offset:512\n"
    "v_mul_f32 v41, v8, v41\n"
    "v_mfma_f32_16x16x32_bf16 a[188:191], a[244:247], a[4:7], a[188:191]\n"
    "ds_read_b64_tr_b16 a[226:227], v34 offset:768\n"
    "v_mul_f32 v42, v8, v42\n"
    "v_mfma_f32_16x16x32_bf16 a[188:191], a[248:251], a[8:11], a[188:191]\n"
    "v_mul_f32 v43, v8, v43\n"
    "v_mul_f32 v40, v40, v44\n"
    "v_mfma_f32_16x16x32_bf16 a[188:191], a[252:255], a[12:15], a[188:191]\n"
    "ds_read_b64_tr_b16 a[228:229], v34 offset:4608\n"
    "v_mul_f32 v41, v41, v45\n"
    "v_mfma_f32_16x16x32_bf16 a[184:187], a[252:255], v[204:207], 0\n"
    "ds_read_b64_tr_b16 a[230:231], v34 offset:4864\n"
    "v_mul_f32 v42, v42, v46\n"
    "v_mfma_f32_16x16x32_bf16 a[184:187], a[248:251], v[200:203], a[184:187]\n"
    "v_mul_f32 v43, v43, v47\n"
    "v_cvt_pk_bf16_f32 v56, v40, v41\n"
    "v_mfma_f32_16x16x32_bf16 a[184:187], a[244:247], v[196:199], a[184:187]\n"
    "ds_read_b64_tr_b16 a[232:233], v34 offset:8704\n"
    "v_cvt_pk_bf16_f32 v57, v42, v43\n"
    "v_mfma_f32_16x16x32_bf16 a[184:187], a[240:243], v[192:195], a[184:187]\n"
    "ds_read_b64_tr_b16 a[234:235], v34 offset:8960\n"
    "v_accvgpr_read_b32 v48, a96\n"
    "v_mfma_f32_16x16x32_bf16 a[180:183], a[240:243], v[128:131], 0\n"
    "v_accvgpr_read_b32 v52, a112\n"
    "v_accvgpr_read_b32 v49, a97\n"
    "v_mfma_f32_16x16x32_bf16 a[180:183], a[244:247], v[132:135], a[180:183]\n"
    "ds_read_b64_tr_b16 a[236:237], v34 offset:12800\n"
    "v_accvgpr_read_b32 v53, a113\n"
    "v_mfma_f32_16x16x32_bf16 a[180:183], a[248:251], v[136:139], a[180:183]\n"
    "buffer_load_dwordx4 v28, s[16:19], s69 offen offset:2048 lds\n"
    "v_mfma_f32_16x16x32_bf16 a[180:183], a[252:255], v[140:143], a[180:183]\n"
    "ds_read_b64_tr_b16 a[238:239], v34 offset:13056\n"
    "v_accvgpr_read_b32 v50, a98\n"
    "v_mfma_f32_16x16x32_bf16 a[176:179], a[252:255], v[76:79], 0\n"
    "buffer_load_dwordx4 v29, s[16:19], s69 offen offset:3072 lds\n"
    "v_mfma_f32_16x16x32_bf16 a[176:179], a[248:251], v[72:75], a[176:179]\n"
    "v_accvgpr_read_b32 v54, a114\n"
    "v_accvgpr_read_b32 v51, a99\n"
    "v_mfma_f32_16x16x32_bf16 a[176:179], a[244:247], v[68:71], a[176:179]\n"
    "v_accvgpr_read_b32 v55, a115\n"
    "v_mul_f32 v48, v8, v48\n"
    "v_mfma_f32_16x16x32_bf16 a[176:179], a[240:243], v[64:67], a[176:179]\n"
    "s_waitcnt lgkmcnt(0)\n"
    "v_mul_f32 v49, v8, v49\n"
    "v_mul_f32 v50, v8, v50\n"
    "v_mfma_f32_16x16x32_bf16 a[192:195], a[224:227], v[64:67], 0\n"
    "ds_read_b64_tr_b16 a[240:241], v34 offset:520\n"
    "v_mul_f32 v51, v8, v51\n"
    "v_mfma_f32_16x16x32_bf16 a[192:195], a[228:231], v[68:71], a[192:195]\n"
    "ds_read_b64_tr_b16 a[242:243], v34 offset:776\n"
    "v_mul_f32 v48, v48, v52\n"
    "v_mfma_f32_16x16x32_bf16 a[192:195], a[232:235], v[72:75], a[192:195]\n"
    "v_mul_f32 v49, v49, v53\n"
    "v_mul_f32 v50, v50, v54\n"
    "v_mfma_f32_16x16x32_bf16 a[192:195], a[236:239], v[76:79], a[192:195]\n"
    "ds_read_b64_tr_b16 a[244:245], v34 offset:4616\n"
    "v_mul_f32 v51, v51, v55\n"
    "v_mfma_f32_16x16x32_bf16 a[196:199], a[236:239], v[140:143], 0\n"
    "ds_read_b64_tr_b16 a[246:247], v34 offset:4872\n"
    "v_cvt_pk_bf16_f32 v58, v48, v49\n"
    "v_mfma_f32_16x16x32_bf16 a[196:199], a[232:235], v[136:139], a[196:199]\n"
    "v_cvt_pk_bf16_f32 v59, v50, v51\n"
    "v_mfma_f32_16x16x32_bf16 a[196:199], a[228:231], v[132:135], a[196:199]\n"
    "ds_read_b64_tr_b16 a[248:249], v34 offset:8712\n"
    "s_add_u32 s68, s42, s58\n"
    "v_mfma_f32_16x16x32_bf16 a[196:199], a[224:227], v[128:131], a[196:199]\n"
    "ds_read_b64_tr_b16 a[250:251], v34 offset:8968\n"
    "v_mfma_f32_16x16x32_bf16 a[200:203], a[224:227], v[192:195], 0\n"
    "buffer_store_dwordx4 v[56:59], v2, s[24:27], s68 offen offset:0 sc1\n"
    "v_mfma_f32_16x16x32_bf16 a[200:203], a[228:231], v[196:199], a[200:203]\n"
    "ds_read_b64_tr_b16 a[252:253], v34 offset:12808\n"
    "v_accvgpr_read_b32 v40, a68\n"
    "v_mfma_f32_16x16x32_bf16 a[200:203], a[232:235], v[200:203], a[200:203]\n"
    "v_accvgpr_read_b32 v44, a84\n"
    "v_accvgpr_read_b32 v41, a69\n"
    "v_mfma_f32_16x16x32_bf16 a[200:203], a[236:239], v[204:207], a[200:203]\n"
    "ds_read_b64_tr_b16 a[254:255], v34 offset:13064\n"
    "v_accvgpr_read_b32 v45, a85\n"
    "v_mfma_f32_16x16x32_bf16 a[204:207], a[236:239], a[12:15], 0\n"
    "v_accvgpr_read_b32 v42, a70\n"
    "v_accvgpr_read_b32 v46, a86\n"
    "v_mfma_f32_16x16x32_bf16 a[204:207], a[232:235], a[8:11], a[204:207]\n"
    "v_accvgpr_read_b32 v43, a71\n"
    "v_accvgpr_read_b32 v47, a87\n"
    "v_mfma_f32_16x16x32_bf16 a[204:207], a[228:231], a[4:7], a[204:207]\n"
    "v_mul_f32 v40, v9, v40\n"
    "v_mul_f32 v41, v9, v41\n"
    "v_mfma_f32_16x16x32_bf16 a[204:207], a[224:227], a[0:3], a[204:207]\n"
    "s_waitcnt lgkmcnt(0)\n"
    "v_mul_f32 v42, v9, v42\n"
    "v_mul_f32 v43, v9, v43\n"
    "v_mfma_f32_16x16x32_bf16 a[220:223], a[240:243], a[0:3], 0\n"
    "s_waitcnt vmcnt(18)\n"
    "v_mul_f32 v40, v40, v44\n"
    "v_mul_f32 v41, v41, v45\n"
    "v_mfma_f32_16x16x32_bf16 a[220:223], a[244:247], a[4:7], a[220:223]\n"
    "s_barrier\n"
    "v_mul_f32 v42, v42, v46\n"
    "v_mul_f32 v43, v43, v47\n"
    "v_mfma_f32_16x16x32_bf16 a[220:223], a[248:251], a[8:11], a[220:223]\n"
    "ds_read_b64_tr_b16 a[224:225], v34 offset:16384\n"
    "v_cvt_pk_bf16_f32 v60, v40, v41\n"
    "v_mfma_f32_16x16x32_bf16 a[220:223], a[252:255], a[12:15], a[220:223]\n"
    "ds_read_b64_tr_b16 a[226:227], v34 offset:16640\n"
    "v_cvt_pk_bf16_f32 v61, v42, v43\n"
    "v_mfma_f32_16x16x32_bf16 a[216:219], a[252:255], v[204:207], 0\n"
    "ds_read_b64_tr_b16 a[228:229], v34 offset:20480\n"
    "v_accvgpr_read_b32 v48, a100\n"
    "v_mfma_f32_16x16x32_bf16 a[216:219], a[248:251], v[200:203], a[216:219]\n"
    "v_accvgpr_read_b32 v52, a116\n"
    "v_accvgpr_read_b32 v49, a101\n"
    "v_mfma_f32_16x16x32_bf16 a[216:219], a[244:247], v[196:199], a[216:219]\n"
    "ds_read_b64_tr_b16 a[230:231], v34 offset:20736\n"
    "v_accvgpr_read_b32 v53, a117\n"
    "v_mfma_f32_16x16x32_bf16 a[216:219], a[240:243], v[192:195], a[216:219]\n"
    "ds_read_b64_tr_b16 a[232:233], v34 offset:24576\n"
    "v_accvgpr_read_b32 v50, a102\n"
    "v_mfma_f32_16x16x32_bf16 a[212:215], a[240:243], v[128:131], 0\n"
    "ds_read_b64_tr_b16 a[234:235], v34 offset:24832\n"
    "v_accvgpr_read_b32 v54, a118\n"
    "v_mfma_f32_16x16x32_bf16 a[212:215], a[244:247], v[132:135], a[212:215]\n"
    "s_add_u32 m0, s65, 0x10000\n"
    "ds_read_b64_tr_b16 a[236:237], v34 offset:28672\n"
    "v_accvgpr_read_b32 v51, a103\n"
    "v_mfma_f32_16x16x32_bf16 a[212:215], a[248:251], v[136:139], a[212:215]\n"
    "s_xor_b32 s69, s80, 0x200080\n"
    "buffer_load_dwordx4 v26, s[16:19], s69 offen offset:0 lds\n"
    "v_mfma_f32_16x16x32_bf16 a[212:215], a[252:255], v[140:143], a[212:215]\n"
    "ds_read_b64_tr_b16 a[238:239], v34 offset:28928\n"
    "v_accvgpr_read_b32 v55, a119\n"
    "v_mfma_f32_16x16x32_bf16 a[208:211], a[252:255], v[76:79], 0\n"
    "buffer_load_dwordx4 v27, s[16:19], s69 offen offset:1024 lds\n"
    "v_mfma_f32_16x16x32_bf16 a[208:211], a[248:251], v[72:75], a[208:211]\n"
    "v_mul_f32 v48, v9, v48\n"
    "v_mul_f32 v49, v9, v49\n"
    "v_mfma_f32_16x16x32_bf16 a[208:211], a[244:247], v[68:71], a[208:211]\n"
    "buffer_load_dwordx4 v28, s[16:19], s69 offen offset:2048 lds\n"
    "v_mfma_f32_16x16x32_bf16 a[208:211], a[240:243], v[64:67], a[208:211]\n"
    "s_waitcnt lgkmcnt(0)\n"
    "v_mul_f32 v50, v9, v50\n"
    "v_mul_f32 v51, v9, v51\n"
    "v_mfma_f32_16x16x32_bf16 a[160:163], a[224:227], v[80:83], a[160:163]\n"
    "ds_read_b64_tr_b16 a[240:241], v34 offset:16392\n"
    "v_mul_f32 v48, v48, v52\n"
    "v_mfma_f32_16x16x32_bf16 a[160:163], a[228:231], v[84:87], a[160:163]\n"
    "ds_read_b64_tr_b16 a[242:243], v34 offset:16648\n"
    "v_mul_f32 v49, v49, v53\n"
    "v_mfma_f32_16x16x32_bf16 a[160:163], a[232:235], v[88:91], a[160:163]\n"
    "v_mul_f32 v50, v50, v54\n"
    "v_mul_f32 v51, v51, v55\n"
    "v_mfma_f32_16x16x32_bf16 a[160:163], a[236:239], v[92:95], a[160:163]\n"
    "ds_read_b64_tr_b16 a[244:245], v34 offset:20488\n"
    "v_cvt_pk_bf16_f32 v62, v48, v49\n"
    "v_mfma_f32_16x16x32_bf16 a[164:167], a[236:239], v[156:159], a[164:167]\n"
    "ds_read_b64_tr_b16 a[246:247], v34 offset:20744\n"
    "v_cvt_pk_bf16_f32 v63, v50, v51\n"
    "v_mfma_f32_16x16x32_bf16 a[164:167], a[232:235], v[152:155], a[164:167]\n"
    "s_add_u32 s68, s42, s59\n"
    "buffer_store_dwordx4 v[60:63], v2, s[24:27], s68 offen offset:0 sc1\n"
    "v_mfma_f32_16x16x32_bf16 a[164:167], a[228:231], v[148:151], a[164:167]\n"
    "ds_read_b64_tr_b16 a[248:249], v34 offset:24584\n"
    "v_accvgpr_read_b32 v40, a72\n"
    "v_mfma_f32_16x16x32_bf16 a[164:167], a[224:227], v[144:147], a[164:167]\n"
    "ds_read_b64_tr_b16 a[250:251], v34 offset:24840\n"
    "v_accvgpr_read_b32 v44, a88\n"
    "v_mfma_f32_16x16x32_bf16 a[168:171], a[224:227], v[208:211], a[168:171]\n"
    "v_accvgpr_read_b32 v41, a73\n"
    "v_accvgpr_read_b32 v45, a89\n"
    "v_mfma_f32_16x16x32_bf16 a[168:171], a[228:231], v[212:215], a[168:171]\n"
    "ds_read_b64_tr_b16 a[252:253], v34 offset:28680\n"
    "v_accvgpr_read_b32 v42, a74\n"
    "v_mfma_f32_16x16x32_bf16 a[168:171], a[232:235], v[216:219], a[168:171]\n"
    "buffer_load_dwordx4 v29, s[16:19], s69 offen offset:3072 lds\n"
    "v_mfma_f32_16x16x32_bf16 a[168:171], a[236:239], v[220:223], a[168:171]\n"
    "ds_read_b64_tr_b16 a[254:255], v34 offset:28936\n"
    "v_accvgpr_read_b32 v46, a90\n"
    "v_mfma_f32_16x16x32_bf16 a[172:175], a[236:239], a[28:31], a[172:175]\n"
    "v_accvgpr_read_b32 v43, a75\n"
    "v_accvgpr_read_b32 v47, a91\n"
    "v_mfma_f32_16x16x32_bf16 a[172:175], a[232:235], a[24:27], a[172:175]\n"
    "v_mul_f32 v40, v21, v40\n"
    "v_mul_f32 v41, v21, v41\n"
    "v_mfma_f32_16x16x32_bf16 a[172:175], a[228:231], a[20:23], a[172:175]\n"
    "v_mul_f32 v42, v21, v42\n"
    "v_mul_f32 v43, v21, v43\n"
    "v_mfma_f32_16x16x32_bf16 a[172:175], a[224:227], a[16:19], a[172:175]\n"
    "s_waitcnt lgkmcnt(0)\n"
    "v_mul_f32 v40, v40, v44\n"
    "v_mul_f32 v41, v41, v45\n"
    "v_mfma_f32_16x16x32_bf16 a[188:191], a[240:243], a[16:19], a[188:191]\n"
    "ds_read_b64_tr_b16 a[224:225], v34 offset:16896\n"
    "v_mul_f32 v42, v42, v46\n"
    "v_mfma_f32_16x16x32_bf16 a[188:191], a[244:247], a[20:23], a[188:191]\n"
    "ds_read_b64_tr_b16 a[226:227], v34 offset:17152\n"
    "v_mul_f32 v43, v43, v47\n"
    "v_mfma_f32_16x16x32_bf16 a[188:191], a[248:251], a[24:27], a[188:191]\n"
    "v_cvt_pk_bf16_f32 v56, v40, v41\n"
    "v_cvt_pk_bf16_f32 v57, v42, v43\n"
    "v_mfma_f32_16x16x32_bf16 a[188:191], a[252:255], a[28:31], a[188:191]\n"
    "ds_read_b64_tr_b16 a[228:229], v34 offset:20992\n"
    "v_accvgpr_read_b32 v48, a104\n"
    "v_mfma_f32_16x16x32_bf16 a[184:187], a[252:255], v[220:223], a[184:187]\n"
    "ds_read_b64_tr_b16 a[230:231], v34 offset:21248\n"
    "v_accvgpr_read_b32 v52, a120\n"
    "v_mfma_f32_16x16x32_bf16 a[184:187], a[248:251], v[216:219], a[184:187]\n"
    "v_accvgpr_read_b32 v49, a105\n"
    "v_accvgpr_read_b32 v53, a121\n"
    "v_mfma_f32_16x16x32_bf16 a[184:187], a[244:247], v[212:215], a[184:187]\n"
    "ds_read_b64_tr_b16 a[232:233], v34 offset:25088\n"
    "v_accvgpr_read_b32 v50, a106\n"
    "v_mfma_f32_16x16x32_bf16 a[184:187], a[240:243], v[208:211], a[184:187]\n"
    "ds_read_b64_tr_b16 a[234:235], v34 offset:25344\n"
    "v_accvgpr_read_b32 v54, a122\n"
    "v_mfma_f32_16x16x32_bf16 a[180:183], a[240:243], v[144:147], a[180:183]\n"
    "v_accvgpr_read_b32 v51, a107\n"
    "v_accvgpr_read_b32 v55, a123\n"
    "v_mfma_f32_16x16x32_bf16 a[180:183], a[244:247], v[148:151], a[180:183]\n"
    "ds_read_b64_tr_b16 a[236:237], v34 offset:29184\n"
    "v_mul_f32 v48, v21, v48\n"
    "v_mfma_f32_16x16x32_bf16 a[180:183], a[248:251], v[152:155], a[180:183]\n"
    "v_mul_f32 v49, v21, v49\n"
    "v_mul_f32 v50, v21, v50\n"
    "v_mfma_f32_16x16x32_bf16 a[180:183], a[252:255], v[156:159], a[180:183]\n"
    "ds_read_b64_tr_b16 a[238:239], v34 offset:29440\n"
    "v_mul_f32 v51, v21, v51\n"
    "v_mfma_f32_16x16x32_bf16 a[176:179], a[252:255], v[92:95], a[176:179]\n"
    "v_mul_f32 v48, v48, v52\n"
    "v_mul_f32 v49, v49, v53\n"
    "v_mfma_f32_16x16x32_bf16 a[176:179], a[248:251], v[88:91], a[176:179]\n"
    "v_mul_f32 v50, v50, v54\n"
    "v_mul_f32 v51, v51, v55\n"
    "v_mfma_f32_16x16x32_bf16 a[176:179], a[244:247], v[84:87], a[176:179]\n"
    "v_cvt_pk_bf16_f32 v58, v48, v49\n"
    "v_cvt_pk_bf16_f32 v59, v50, v51\n"
    "v_mfma_f32_16x16x32_bf16 a[176:179], a[240:243], v[80:83], a[176:179]\n"
    "s_waitcnt lgkmcnt(0)\n"
    "s_add_u32 s68, s42, s62\n"
    "buffer_store_dwordx4 v[56:59], v2, s[24:27], s68 offen offset:0 sc1\n"
    "v_mfma_f32_16x16x32_bf16 a[192:195], a[224:227], v[80:83], a[192:195]\n"
    "ds_read_b64_tr_b16 a[240:241], v34 offset:16904\n"
    "v_accvgpr_read_b32 v40, a76\n"
    "v_mfma_f32_16x16x32_bf16 a[192:195], a[228:231], v[84:87], a[192:195]\n"
    "ds_read_b64_tr_b16 a[242:243], v34 offset:17160\n"
    "v_accvgpr_read_b32 v44, a92\n"
    "v_mfma_f32_16x16x32_bf16 a[192:195], a[232:235], v[88:91], a[192:195]\n"
    "v_accvgpr_read_b32 v41, a77\n"
    "v_accvgpr_read_b32 v45, a93\n"
    "v_mfma_f32_16x16x32_bf16 a[192:195], a[236:239], v[92:95], a[192:195]\n"
    "ds_read_b64_tr_b16 a[244:245], v34 offset:21000\n"
    "v_accvgpr_read_b32 v42, a78\n"
    "v_mfma_f32_16x16x32_bf16 a[196:199], a[236:239], v[156:159], a[196:199]\n"
    "ds_read_b64_tr_b16 a[246:247], v34 offset:21256\n"
    "v_accvgpr_read_b32 v46, a94\n"
    "v_mfma_f32_16x16x32_bf16 a[196:199], a[232:235], v[152:155], a[196:199]\n"
    "v_accvgpr_read_b32 v43, a79\n"
    "v_accvgpr_read_b32 v47, a95\n"
    "v_mfma_f32_16x16x32_bf16 a[196:199], a[228:231], v[148:151], a[196:199]\n"
    "ds_read_b64_tr_b16 a[248:249], v34 offset:25096\n"
    "v_mul_f32 v40, v36, v40\n"
    "v_mfma_f32_16x16x32_bf16 a[196:199], a[224:227], v[144:147], a[196:199]\n"
    "ds_read_b64_tr_b16 a[250:251], v34 offset:25352\n"
    "v_mul_f32 v41, v36, v41\n"
    "v_mfma_f32_16x16x32_bf16 a[200:203], a[224:227], v[208:211], a[200:203]\n"
    "v_mul_f32 v42, v36, v42\n"
    "v_mul_f32 v43, v36, v43\n"
    "v_mfma_f32_16x16x32_bf16 a[200:203], a[228:231], v[212:215], a[200:203]\n"
    "ds_read_b64_tr_b16 a[252:253], v34 offset:29192\n"
    "v_mul_f32 v40, v40, v44\n"
    "v_mfma_f32_16x16x32_bf16 a[200:203], a[232:235], v[216:219], a[200:203]\n"
    "v_mul_f32 v41, v41, v45\n"
    "v_mul_f32 v42, v42, v46\n"
    "v_mfma_f32_16x16x32_bf16 a[200:203], a[236:239], v[220:223], a[200:203]\n"
    "ds_read_b64_tr_b16 a[254:255], v34 offset:29448\n"
    "v_mul_f32 v43, v43, v47\n"
    "v_mfma_f32_16x16x32_bf16 a[204:207], a[236:239], a[28:31], a[204:207]\n"
    "v_cvt_pk_bf16_f32 v60, v40, v41\n"
    "v_cvt_pk_bf16_f32 v61, v42, v43\n"
    "v_mfma_f32_16x16x32_bf16 a[204:207], a[232:235], a[24:27], a[204:207]\n"
    "v_accvgpr_read_b32 v48, a108\n"
    "v_accvgpr_read_b32 v52, a124\n"
    "v_mfma_f32_16x16x32_bf16 a[204:207], a[228:231], a[20:23], a[204:207]\n"
    "v_accvgpr_read_b32 v49, a109\n"
    "v_accvgpr_read_b32 v53, a125\n"
    "v_mfma_f32_16x16x32_bf16 a[204:207], a[224:227], a[16:19], a[204:207]\n"
    "s_waitcnt lgkmcnt(0)\n"
    "v_accvgpr_read_b32 v50, a110\n"
    "v_accvgpr_read_b32 v54, a126\n"
    "v_mfma_f32_16x16x32_bf16 a[220:223], a[240:243], a[16:19], a[220:223]\n"
    "s_waitcnt vmcnt(20)\n"
    "v_accvgpr_read_b32 v51, a111\n"
    "v_accvgpr_read_b32 v55, a127\n"
    "v_mfma_f32_16x16x32_bf16 a[220:223], a[244:247], a[20:23], a[220:223]\n"
    "s_barrier\n"
    "v_mul_f32 v48, v36, v48\n"
    "v_mul_f32 v49, v36, v49\n"
    "v_mfma_f32_16x16x32_bf16 a[220:223], a[248:251], a[24:27], a[220:223]\n"
    "ds_read_b64_tr_b16 a[224:225], v34 offset:32768\n"
    "v_mul_f32 v50, v36, v50\n"
    "v_mfma_f32_16x16x32_bf16 a[220:223], a[252:255], a[28:31], a[220:223]\n"
    "ds_read_b64_tr_b16 a[226:227], v34 offset:33024\n"
    "v_mul_f32 v51, v36, v51\n"
    "v_mfma_f32_16x16x32_bf16 a[216:219], a[252:255], v[220:223], a[216:219]\n"
    "ds_read_b64_tr_b16 a[228:229], v34 offset:36864\n"
    "v_mul_f32 v48, v48, v52\n"
    "v_mfma_f32_16x16x32_bf16 a[216:219], a[248:251], v[216:219], a[216:219]\n"
    "v_mul_f32 v49, v49, v53\n"
    "v_mul_f32 v50, v50, v54\n"
    "v_mfma_f32_16x16x32_bf16 a[216:219], a[244:247], v[212:215], a[216:219]\n"
    "ds_read_b64_tr_b16 a[230:231], v34 offset:37120\n"
    "v_mul_f32 v51, v51, v55\n"
    "v_mfma_f32_16x16x32_bf16 a[216:219], a[240:243], v[208:211], a[216:219]\n"
    "ds_read_b64_tr_b16 a[232:233], v34 offset:40960\n"
    "v_cvt_pk_bf16_f32 v62, v48, v49\n"
    "v_mfma_f32_16x16x32_bf16 a[212:215], a[240:243], v[144:147], a[212:215]\n"
    "ds_read_b64_tr_b16 a[234:235], v34 offset:41216\n"
    "v_cvt_pk_bf16_f32 v63, v50, v51\n"
    "v_mfma_f32_16x16x32_bf16 a[212:215], a[244:247], v[148:151], a[212:215]\n"
    "s_add_u32 m0, s65, 0x14000\n"
    "ds_read_b64_tr_b16 a[236:237], v34 offset:45056\n"
    "s_add_u32 s68, s42, s63\n"
    "v_mfma_f32_16x16x32_bf16 a[212:215], a[248:251], v[152:155], a[212:215]\n"
    "s_xor_b32 s69, s80, 0x300080\n"
    "buffer_load_dwordx4 v26, s[16:19], s69 offen offset:0 lds\n"
    "v_mfma_f32_16x16x32_bf16 a[212:215], a[252:255], v[156:159], a[212:215]\n"
    "ds_read_b64_tr_b16 a[238:239], v34 offset:45312\n"
    "v_mfma_f32_16x16x32_bf16 a[208:211], a[252:255], v[92:95], a[208:211]\n"
    "buffer_load_dwordx4 v27, s[16:19], s69 offen offset:1024 lds\n"
    "v_mfma_f32_16x16x32_bf16 a[208:211], a[248:251], v[88:91], a[208:211]\n"
    "buffer_store_dwordx4 v[60:63], v2, s[24:27], s68 offen offset:0 sc1\n"
    "v_mfma_f32_16x16x32_bf16 a[208:211], a[244:247], v[84:87], a[208:211]\n"
    "buffer_load_dwordx4 v28, s[16:19], s69 offen offset:2048 lds\n"
    "v_mfma_f32_16x16x32_bf16 a[208:211], a[240:243], v[80:83], a[208:211]\n"
    "s_waitcnt lgkmcnt(0)\n"
    "v_accvgpr_read_b32 v40, a128\n"
    "v_accvgpr_read_b32 v44, a136\n"
    "v_mfma_f32_16x16x32_bf16 a[160:163], a[224:227], v[96:99], a[160:163]\n"
    "ds_read_b64_tr_b16 a[240:241], v34 offset:32776\n"
    "v_accvgpr_read_b32 v41, a129\n"
    "v_mfma_f32_16x16x32_bf16 a[160:163], a[228:231], v[100:103], a[160:163]\n"
    "ds_read_b64_tr_b16 a[242:243], v34 offset:33032\n"
    "v_accvgpr_read_b32 v45, a137\n"
    "v_mfma_f32_16x16x32_bf16 a[160:163], a[232:235], v[104:107], a[160:163]\n"
    "v_accvgpr_read_b32 v42, a130\n"
    "v_accvgpr_read_b32 v46, a138\n"
    "v_mfma_f32_16x16x32_bf16 a[160:163], a[236:239], v[108:111], a[160:163]\n"
    "ds_read_b64_tr_b16 a[244:245], v34 offset:36872\n"
    "v_accvgpr_read_b32 v43, a131\n"
    "v_mfma_f32_16x16x32_bf16 a[164:167], a[236:239], v[172:175], a[164:167]\n"
    "ds_read_b64_tr_b16 a[246:247], v34 offset:37128\n"
    "v_accvgpr_read_b32 v47, a139\n"
    "v_mfma_f32_16x16x32_bf16 a[164:167], a[232:235], v[168:171], a[164:167]\n"
    "v_mul_f32 v40, v8, v40\n"
    "v_mul_f32 v41, v8, v41\n"
    "v_mfma_f32_16x16x32_bf16 a[164:167], a[228:231], v[164:167], a[164:167]\n"
    "ds_read_b64_tr_b16 a[248:249], v34 offset:40968\n"
    "v_mul_f32 v42, v8, v42\n"
    "v_mfma_f32_16x16x32_bf16 a[164:167], a[224:227], v[160:163], a[164:167]\n"
    "ds_read_b64_tr_b16 a[250:251], v34 offset:41224\n"
    "v_mul_f32 v43, v8, v43\n"
    "v_mfma_f32_16x16x32_bf16 a[168:171], a[224:227], v[224:227], a[168:171]\n"
    "v_mul_f32 v40, v40, v44\n"
    "v_mul_f32 v41, v41, v45\n"
    "v_mfma_f32_16x16x32_bf16 a[168:171], a[228:231], v[228:231], a[168:171]\n"
    "ds_read_b64_tr_b16 a[252:253], v34 offset:45064\n"
    "v_mul_f32 v42, v42, v46\n"
    "v_mfma_f32_16x16x32_bf16 a[168:171], a[232:235], v[232:235], a[168:171]\n"
    "buffer_load_dwordx4 v29, s[16:19], s69 offen offset:3072 lds\n"
    "v_mfma_f32_16x16x32_bf16 a[168:171], a[236:239], v[236:239], a[168:171]\n"
    "ds_read_b64_tr_b16 a[254:255], v34 offset:45320\n"
    "v_mul_f32 v43, v43, v47\n"
    "v_mfma_f32_16x16x32_bf16 a[172:175], a[236:239], a[44:47], a[172:175]\n"
    "v_cvt_pk_bf16_f32 v56, v40, v41\n"
    "v_cvt_pk_bf16_f32 v57, v42, v43\n"
    "v_mfma_f32_16x16x32_bf16 a[172:175], a[232:235], a[40:43], a[172:175]\n"
    "v_accvgpr_read_b32 v48, a144\n"
    "v_accvgpr_read_b32 v52, a152\n"
    "v_mfma_f32_16x16x32_bf16 a[172:175], a[228:231], a[36:39], a[172:175]\n"
    "v_accvgpr_read_b32 v49, a145\n"
    "v_accvgpr_read_b32 v53, a153\n"
    "v_mfma_f32_16x16x32_bf16 a[172:175], a[224:227], a[32:35], a[172:175]\n"
    "s_waitcnt lgkmcnt(0)\n"
    "v_accvgpr_read_b32 v50, a146\n"
    "v_accvgpr_read_b32 v54, a154\n"
    "v_mfma_f32_16x16x32_bf16 a[188:191], a[240:243], a[32:35], a[188:191]\n"
    "ds_read_b64_tr_b16 a[224:225], v34 offset:33280\n"
    "v_accvgpr_read_b32 v51, a147\n"
    "v_mfma_f32_16x16x32_bf16 a[188:191], a[244:247], a[36:39], a[188:191]\n"
    "ds_read_b64_tr_b16 a[226:227], v34 offset:33536\n"
    "v_accvgpr_read_b32 v55, a155\n"
    "v_mfma_f32_16x16x32_bf16 a[188:191], a[248:251], a[40:43], a[188:191]\n"
    "v_mul_f32 v48, v8, v48\n"
    "v_mul_f32 v49, v8, v49\n"
    "v_mfma_f32_16x16x32_bf16 a[188:191], a[252:255], a[44:47], a[188:191]\n"
    "ds_read_b64_tr_b16 a[228:229], v34 offset:37376\n"
    "v_mul_f32 v50, v8, v50\n"
    "v_mfma_f32_16x16x32_bf16 a[184:187], a[252:255], v[236:239], a[184:187]\n"
    "ds_read_b64_tr_b16 a[230:231], v34 offset:37632\n"
    "v_mul_f32 v51, v8, v51\n"
    "v_mfma_f32_16x16x32_bf16 a[184:187], a[248:251], v[232:235], a[184:187]\n"
    "v_mul_f32 v48, v48, v52\n"
    "v_mul_f32 v49, v49, v53\n"
    "v_mfma_f32_16x16x32_bf16 a[184:187], a[244:247], v[228:231], a[184:187]\n"
    "ds_read_b64_tr_b16 a[232:233], v34 offset:41472\n"
    "v_mul_f32 v50, v50, v54\n"
    "v_mfma_f32_16x16x32_bf16 a[184:187], a[240:243], v[224:227], a[184:187]\n"
    "ds_read_b64_tr_b16 a[234:235], v34 offset:41728\n"
    "v_mul_f32 v51, v51, v55\n"
    "v_mfma_f32_16x16x32_bf16 a[180:183], a[240:243], v[160:163], a[180:183]\n"
    "v_cvt_pk_bf16_f32 v58, v48, v49\n"
    "v_cvt_pk_bf16_f32 v59, v50, v51\n"
    "v_mfma_f32_16x16x32_bf16 a[180:183], a[244:247], v[164:167], a[180:183]\n"
    "ds_read_b64_tr_b16 a[236:237], v34 offset:45568\n"
    "s_add_u32 s68, s43, s58\n"
    "v_mfma_f32_16x16x32_bf16 a[180:183], a[248:251], v[168:171], a[180:183]\n"
    "buffer_store_dwordx4 v[56:59], v2, s[24:27], s68 offen offset:0 sc1\n"
    "v_mfma_f32_16x16x32_bf16 a[180:183], a[252:255], v[172:175], a[180:183]\n"
    "ds_read_b64_tr_b16 a[238:239], v34 offset:45824\n"
    "v_accvgpr_read_b32 v40, a132\n"
    "v_mfma_f32_16x16x32_bf16 a[176:179], a[252:255], v[108:111], a[176:179]\n"
    "v_accvgpr_read_b32 v44, a140\n"
    "v_accvgpr_read_b32 v41, a133\n"
    "v_mfma_f32_16x16x32_bf16 a[176:179], a[248:251], v[104:107], a[176:179]\n"
    "v_accvgpr_read_b32 v45, a141\n"
    "v_accvgpr_read_b32 v42, a134\n"
    "v_mfma_f32_16x16x32_bf16 a[176:179], a[244:247], v[100:103], a[176:179]\n"
    "v_accvgpr_read_b32 v46, a142\n"
    "v_accvgpr_read_b32 v43, a135\n"
    "v_mfma_f32_16x16x32_bf16 a[176:179], a[240:243], v[96:99], a[176:179]\n"
    "s_waitcnt lgkmcnt(0)\n"
    "v_accvgpr_read_b32 v47, a143\n"
    "v_mul_f32 v40, v9, v40\n"
    "v_mfma_f32_16x16x32_bf16 a[192:195], a[224:227], v[96:99], a[192:195]\n"
    "ds_read_b64_tr_b16 a[240:241], v34 offset:33288\n"
    "v_mul_f32 v41, v9, v41\n"
    "v_mfma_f32_16x16x32_bf16 a[192:195], a[228:231], v[100:103], a[192:195]\n"
    "ds_read_b64_tr_b16 a[242:243], v34 offset:33544\n"
    "v_mul_f32 v42, v9, v42\n"
    "v_mfma_f32_16x16x32_bf16 a[192:195], a[232:235], v[104:107], a[192:195]\n"
    "v_mul_f32 v43, v9, v43\n"
    "v_mul_f32 v40, v40, v44\n"
    "v_mfma_f32_16x16x32_bf16 a[192:195], a[236:239], v[108:111], a[192:195]\n"
    "ds_read_b64_tr_b16 a[244:245], v34 offset:37384\n"
    "v_mul_f32 v41, v41, v45\n"
    "v_mfma_f32_16x16x32_bf16 a[196:199], a[236:239], v[172:175], a[196:199]\n"
    "ds_read_b64_tr_b16 a[246:247], v34 offset:37640\n"
    "v_mul_f32 v42, v42, v46\n"
    "v_mfma_f32_16x16x32_bf16 a[196:199], a[232:235], v[168:171], a[196:199]\n"
    "v_mul_f32 v43, v43, v47\n"
    "v_cvt_pk_bf16_f32 v60, v40, v41\n"
    "v_mfma_f32_16x16x32_bf16 a[196:199], a[228:231], v[164:167], a[196:199]\n"
    "ds_read_b64_tr_b16 a[248:249], v34 offset:41480\n"
    "v_cvt_pk_bf16_f32 v61, v42, v43\n"
    "v_mfma_f32_16x16x32_bf16 a[196:199], a[224:227], v[160:163], a[196:199]\n"
    "ds_read_b64_tr_b16 a[250:251], v34 offset:41736\n"
    "v_accvgpr_read_b32 v48, a148\n"
    "v_mfma_f32_16x16x32_bf16 a[200:203], a[224:227], v[224:227], a[200:203]\n"
    "v_accvgpr_read_b32 v52, a156\n"
    "v_accvgpr_read_b32 v49, a149\n"
    "v_mfma_f32_16x16x32_bf16 a[200:203], a[228:231], v[228:231], a[200:203]\n"
    "ds_read_b64_tr_b16 a[252:253], v34 offset:45576\n"
    "v_accvgpr_read_b32 v53, a157\n"
    "v_mfma_f32_16x16x32_bf16 a[200:203], a[232:235], v[232:235], a[200:203]\n"
    "v_accvgpr_read_b32 v50, a150\n"
    "v_accvgpr_read_b32 v54, a158\n"
    "v_mfma_f32_16x16x32_bf16 a[200:203], a[236:239], v[236:239], a[200:203]\n"
    "ds_read_b64_tr_b16 a[254:255], v34 offset:45832\n"
    "v_accvgpr_read_b32 v51, a151\n"
    "v_mfma_f32_16x16x32_bf16 a[204:207], a[236:239], a[44:47], a[204:207]\n"
    "v_accvgpr_read_b32 v55, a159\n"
    "v_mul_f32 v48, v9, v48\n"
    "v_mfma_f32_16x16x32_bf16 a[204:207], a[232:235], a[40:43], a[204:207]\n"
    "v_mul_f32 v49, v9, v49\n"
    "v_mul_f32 v50, v9, v50\n"
    "v_mfma_f32_16x16x32_bf16 a[204:207], a[228:231], a[36:39], a[204:207]\n"
    "v_mul_f32 v51, v9, v51\n"
    "v_mul_f32 v48, v48, v52\n"
    "v_mfma_f32_16x16x32_bf16 a[204:207], a[224:227], a[32:35], a[204:207]\n"
    "s_waitcnt lgkmcnt(0)\n"
    "v_mul_f32 v49, v49, v53\n"
    "v_mul_f32 v50, v50, v54\n"
    "v_mfma_f32_16x16x32_bf16 a[220:223], a[240:243], a[32:35], a[220:223]\n"
    "s_waitcnt vmcnt(22)\n"
    "v_mul_f32 v51, v51, v55\n"
    "v_cvt_pk_bf16_f32 v62, v48, v49\n"
    "v_mfma_f32_16x16x32_bf16 a[220:223], a[244:247], a[36:39], a[220:223]\n"
    "s_barrier\n"
    "v_cvt_pk_bf16_f32 v63, v50, v51\n"
    "v_mfma_f32_16x16x32_bf16 a[220:223], a[248:251], a[40:43], a[220:223]\n"
    "ds_read_b64_tr_b16 a[224:225], v34 offset:49152\n"
    "s_add_u32 s68, s43, s59\n"
    "v_mfma_f32_16x16x32_bf16 a[220:223], a[252:255], a[44:47], a[220:223]\n"
    "ds_read_b64_tr_b16 a[226:227], v34 offset:49408\n"
    "v_mfma_f32_16x16x32_bf16 a[216:219], a[252:255], v[236:239], a[216:219]\n"
    "ds_read_b64_tr_b16 a[228:229], v34 offset:53248\n"
    "v_mfma_f32_16x16x32_bf16 a[216:219], a[248:251], v[232:235], a[216:219]\n"
    "buffer_store_dwordx4 v[60:63], v2, s[24:27], s68 offen offset:0 sc1\n"
    "v_mfma_f32_16x16x32_bf16 a[216:219], a[244:247], v[228:231], a[216:219]\n"
    "ds_read_b64_tr_b16 a[230:231], v34 offset:53504\n"
    "v_mul_f32 v37, s96, v37\n"
    "v_mfma_f32_16x16x32_bf16 a[216:219], a[240:243], v[224:227], a[216:219]\n"
    "ds_read_b64_tr_b16 a[232:233], v34 offset:57344\n"
    "v_mul_f32 v38, s96, v38\n"
    "v_mfma_f32_16x16x32_bf16 a[212:215], a[240:243], v[160:163], a[212:215]\n"
    "ds_read_b64_tr_b16 a[234:235], v34 offset:57600\n"
    "v_mul_f32 v39, s96, v39\n"
    "v_mfma_f32_16x16x32_bf16 a[212:215], a[244:247], v[164:167], a[212:215]\n"
    "ds_read_b64_tr_b16 a[236:237], v34 offset:61440\n"
    "v_mul_f32 v20, s96, v20\n"
    "v_mfma_f32_16x16x32_bf16 a[212:215], a[248:251], v[168:171], a[212:215]\n"
    "buffer_load_dwordx2 v[26:27], v23, s[24:27], s51 offen offset:256 sc1\n"
    "v_mfma_f32_16x16x32_bf16 a[212:215], a[252:255], v[172:175], a[212:215]\n"
    "s_add_u32 m0, s65, 0\n"
    "ds_read_b64_tr_b16 a[238:239], v34 offset:61696\n"
    "v_lshrrev_b32 v24, 4, v1\n"
    "v_mfma_f32_16x16x32_bf16 a[208:211], a[252:255], v[108:111], a[208:211]\n"
    "s_mov_b32 s69, 0\n"
    "buffer_load_dwordx4 v10, s[8:11], s69 offen offset:0 lds\n"
    "v_mfma_f32_16x16x32_bf16 a[208:211], a[248:251], v[104:107], a[208:211]\n"
    "v_lshrrev_b32 v8, 1, v24\n"
    "v_lshlrev_b32 v8, 11, v8\n"
    "v_mfma_f32_16x16x32_bf16 a[208:211], a[244:247], v[100:103], a[208:211]\n"
    "buffer_load_dwordx4 v11, s[8:11], s69 offen offset:1024 lds\n"
    "v_mfma_f32_16x16x32_bf16 a[208:211], a[240:243], v[96:99], a[208:211]\n"
    "s_waitcnt lgkmcnt(0)\n"
    "v_and_b32 v25, 1, v24\n"
    "v_lshl_add_u32 v8, v25, 8, v8\n"
    "v_mfma_f32_16x16x32_bf16 a[160:163], a[224:227], v[112:115], a[160:163]\n"
    "ds_read_b64_tr_b16 a[240:241], v34 offset:49160\n"
    "v_lshl_add_u32 v8, v25, 5, v8\n"
    "v_mfma_f32_16x16x32_bf16 a[160:163], a[228:231], v[116:119], a[160:163]\n"
    "ds_read_b64_tr_b16 a[242:243], v34 offset:49416\n"
    "v_bfe_u32 v25, v1, 2, 2\n"
    "v_mfma_f32_16x16x32_bf16 a[160:163], a[232:235], v[120:123], a[160:163]\n"
    "v_lshl_add_u32 v8, v25, 6, v8\n"
    "v_and_b32 v25, 3, v1\n"
    "v_mfma_f32_16x16x32_bf16 a[160:163], a[236:239], v[124:127], a[160:163]\n"
    "ds_read_b64_tr_b16 a[244:245], v34 offset:53256\n"
    "v_lshl_add_u32 v8, v25, 3, v8\n"
    "v_mfma_f32_16x16x32_bf16 a[164:167], a[236:239], v[188:191], a[164:167]\n"
    "ds_read_b64_tr_b16 a[246:247], v34 offset:53512\n"
    "v_add_u32 v8, 0x8000, v8\n"
    "v_mfma_f32_16x16x32_bf16 a[164:167], a[232:235], v[184:187], a[164:167]\n"
    "v_xor_b32 v9, 32, v8\n"
    "v_and_b32 v25, 7, v1\n"
    "v_mfma_f32_16x16x32_bf16 a[164:167], a[228:231], v[180:183], a[164:167]\n"
    "ds_read_b64_tr_b16 a[248:249], v34 offset:57352\n"
    "v_xor_b32 v25, s44, v25\n"
    "v_mfma_f32_16x16x32_bf16 a[164:167], a[224:227], v[176:179], a[164:167]\n"
    "ds_read_b64_tr_b16 a[250:251], v34 offset:57608\n"
    "v_lshlrev_b32 v21, 3, v25\n"
    "v_mfma_f32_16x16x32_bf16 a[168:171], a[224:227], v[240:243], a[168:171]\n"
    "v_mfma_f32_16x16x32_bf16 a[168:171], a[228:231], v[244:247], a[168:171]\n"
    "ds_read_b64_tr_b16 a[252:253], v34 offset:61448\n"
    "v_mfma_f32_16x16x32_bf16 a[168:171], a[232:235], v[248:251], a[168:171]\n"
    "buffer_load_dwordx4 v12, s[8:11], s69 offen offset:2048 lds\n"
    "v_mfma_f32_16x16x32_bf16 a[168:171], a[236:239], v[252:255], a[168:171]\n"
    "ds_read_b64_tr_b16 a[254:255], v34 offset:61704\n"
    "v_mfma_f32_16x16x32_bf16 a[172:175], a[236:239], a[60:63], a[172:175]\n"
    "buffer_load_dwordx4 v13, s[8:11], s69 offen offset:3072 lds\n"
    "v_mfma_f32_16x16x32_bf16 a[172:175], a[232:235], a[56:59], a[172:175]\n"
    "v_mfma_f32_16x16x32_bf16 a[172:175], a[228:231], a[52:55], a[172:175]\n"
    "v_mfma_f32_16x16x32_bf16 a[172:175], a[224:227], a[48:51], a[172:175]\n"
    "s_waitcnt lgkmcnt(0)\n"
    "v_mfma_f32_16x16x32_bf16 a[188:191], a[240:243], a[48:51], a[188:191]\n"
    "ds_read_b64_tr_b16 a[224:225], v34 offset:49664\n"
    "v_mfma_f32_16x16x32_bf16 a[188:191], a[244:247], a[52:55], a[188:191]\n"
    "ds_read_b64_tr_b16 a[226:227], v34 offset:49920\n"
    "v_mfma_f32_16x16x32_bf16 a[188:191], a[248:251], a[56:59], a[188:191]\n"
    "v_mfma_f32_16x16x32_bf16 a[188:191], a[252:255], a[60:63], a[188:191]\n"
    "ds_read_b64_tr_b16 a[228:229], v34 offset:53760\n"
    "v_mfma_f32_16x16x32_bf16 a[184:187], a[252:255], v[252:255], a[184:187]\n"
    "ds_read_b64_tr_b16 a[230:231], v34 offset:54016\n"
    "v_mfma_f32_16x16x32_bf16 a[184:187], a[248:251], v[248:251], a[184:187]\n"
    "v_mfma_f32_16x16x32_bf16 a[184:187], a[244:247], v[244:247], a[184:187]\n"
    "ds_read_b64_tr_b16 a[232:233], v34 offset:57856\n"
    "v_mfma_f32_16x16x32_bf16 a[184:187], a[240:243], v[240:243], a[184:187]\n"
    "ds_read_b64_tr_b16 a[234:235], v34 offset:58112\n"
    "v_mfma_f32_16x16x32_bf16 a[180:183], a[240:243], v[176:179], a[180:183]\n"
    "v_mfma_f32_16x16x32_bf16 a[180:183], a[244:247], v[180:183], a[180:183]\n"
    "ds_read_b64_tr_b16 a[236:237], v34 offset:61952\n"
    "v_mfma_f32_16x16x32_bf16 a[180:183], a[248:251], v[184:187], a[180:183]\n"
    "v_mfma_f32_16x16x32_bf16 a[180:183], a[252:255], v[188:191], a[180:183]\n"
    "ds_read_b64_tr_b16 a[238:239], v34 offset:62208\n"
    "v_mfma_f32_16x16x32_bf16 a[176:179], a[252:255], v[124:127], a[176:179]\n"
    "v_mfma_f32_16x16x32_bf16 a[176:179], a[248:251], v[120:123], a[176:179]\n"
    "v_mfma_f32_16x16x32_bf16 a[176:179], a[244:247], v[116:119], a[176:179]\n"
    "v_mfma_f32_16x16x32_bf16 a[176:179], a[240:243], v[112:115], a[176:179]\n"
    "s_waitcnt lgkmcnt(0)\n"
    "v_mfma_f32_16x16x32_bf16 a[192:195], a[224:227], v[112:115], a[192:195]\n"
    "ds_read_b64_tr_b16 a[240:241], v34 offset:49672\n"
    "v_mfma_f32_16x16x32_bf16 a[192:195], a[228:231], v[116:119], a[192:195]\n"
    "ds_read_b64_tr_b16 a[242:243], v34 offset:49928\n"
    "v_mfma_f32_16x16x32_bf16 a[192:195], a[232:235], v[120:123], a[192:195]\n"
    "v_mfma_f32_16x16x32_bf16 a[192:195], a[236:239], v[124:127], a[192:195]\n"
    "ds_read_b64_tr_b16 a[244:245], v34 offset:53768\n"
    "v_mfma_f32_16x16x32_bf16 a[196:199], a[236:239], v[188:191], a[196:199]\n"
    "ds_read_b64_tr_b16 a[246:247], v34 offset:54024\n"
    "v_mfma_f32_16x16x32_bf16 a[196:199], a[232:235], v[184:187], a[196:199]\n"
    "v_mfma_f32_16x16x32_bf16 a[196:199], a[228:231], v[180:183], a[196:199]\n"
    "ds_read_b64_tr_b16 a[248:249], v34 offset:57864\n"
    "v_mfma_f32_16x16x32_bf16 a[196:199], a[224:227], v[176:179], a[196:199]\n"
    "ds_read_b64_tr_b16 a[250:251], v34 offset:58120\n"
    "v_mfma_f32_16x16x32_bf16 a[200:203], a[224:227], v[240:243], a[200:203]\n"
    "v_mfma_f32_16x16x32_bf16 a[200:203], a[228:231], v[244:247], a[200:203]\n"
    "ds_read_b64_tr_b16 a[252:253], v34 offset:61960\n"
    "v_mfma_f32_16x16x32_bf16 a[200:203], a[232:235], v[248:251], a[200:203]\n"
    "v_mfma_f32_16x16x32_bf16 a[200:203], a[236:239], v[252:255], a[200:203]\n"
    "ds_read_b64_tr_b16 a[254:255], v34 offset:62216\n"
    "v_mfma_f32_16x16x32_bf16 a[204:207], a[236:239], a[60:63], a[204:207]\n"
    "v_mfma_f32_16x16x32_bf16 a[204:207], a[232:235], a[56:59], a[204:207]\n"
    "v_mfma_f32_16x16x32_bf16 a[204:207], a[228:231], a[52:55], a[204:207]\n"
    "v_mfma_f32_16x16x32_bf16 a[204:207], a[224:227], a[48:51], a[204:207]\n"
    "s_waitcnt lgkmcnt(0)\n"
    "v_mfma_f32_16x16x32_bf16 a[220:223], a[240:243], a[48:51], a[220:223]\n"
    "s_waitcnt vmcnt(23)\n"
    "v_mfma_f32_16x16x32_bf16 a[220:223], a[244:247], a[52:55], a[220:223]\n"
    "s_barrier\n"
    "v_mfma_f32_16x16x32_bf16 a[220:223], a[248:251], a[56:59], a[220:223]\n"
    "ds_read_b64_tr_b16 a[224:225], v35 offset:0\n"
    "v_mfma_f32_16x16x32_bf16 a[220:223], a[252:255], a[60:63], a[220:223]\n"
    "ds_read_b64_tr_b16 a[226:227], v35 offset:256\n"
    "v_mfma_f32_16x16x32_bf16 a[216:219], a[252:255], v[252:255], a[216:219]\n"
    "ds_read_b64_tr_b16 a[228:229], v35 offset:4096\n"
    "v_mfma_f32_16x16x32_bf16 a[216:219], a[248:251], v[248:251], a[216:219]\n"
    "v_mfma_f32_16x16x32_bf16 a[216:219], a[244:247], v[244:247], a[216:219]\n"
    "ds_read_b64_tr_b16 a[230:231], v35 offset:4352\n"
    "v_mfma_f32_16x16x32_bf16 a[216:219], a[240:243], v[240:243], a[216:219]\n"
    "ds_read_b64_tr_b16 a[232:233], v35 offset:8192\n"
    "v_mfma_f32_16x16x32_bf16 a[212:215], a[240:243], v[176:179], a[212:215]\n"
    "ds_read_b64_tr_b16 a[234:235], v35 offset:8448\n"
    "v_mfma_f32_16x16x32_bf16 a[212:215], a[244:247], v[180:183], a[212:215]\n"
    "s_add_u32 m0, s65, 0x8000\n"
    "ds_read_b64_tr_b16 a[236:237], v35 offset:12288\n"
    "v_mfma_f32_16x16x32_bf16 a[212:215], a[248:251], v[184:187], a[212:215]\n"
    "s_mov_b32 s69, 0\n"
    "buffer_load_dwordx4 v14, s[12:15], s69 offen offset:0 lds\n"
    "v_mfma_f32_16x16x32_bf16 a[212:215], a[252:255], v[188:191], a[212:215]\n"
    "ds_read_b64_tr_b16 a[238:239], v35 offset:12544\n"
    "v_mfma_f32_16x16x32_bf16 a[208:211], a[252:255], v[124:127], a[208:211]\n"
    "buffer_load_dwordx4 v15, s[12:15], s69 offen offset:1024 lds\n"
    "v_mfma_f32_16x16x32_bf16 a[208:211], a[248:251], v[120:123], a[208:211]\n"
    "v_mfma_f32_16x16x32_bf16 a[208:211], a[244:247], v[116:119], a[208:211]\n"
    "buffer_load_dwordx4 v14, s[12:15], s69 offen offset:2048 lds\n"
    "v_mfma_f32_16x16x32_bf16 a[208:211], a[240:243], v[112:115], a[208:211]\n"
    "s_waitcnt lgkmcnt(0)\n"
    "v_mfma_f32_16x16x32_bf16 a[64:67], a[224:227], v[64:67], 0\n"
    "ds_read_b64_tr_b16 a[240:241], v35 offset:8\n"
    "v_mfma_f32_16x16x32_bf16 a[64:67], a[228:231], v[68:71], a[64:67]\n"
    "ds_read_b64_tr_b16 a[242:243], v35 offset:264\n"
    "v_mfma_f32_16x16x32_bf16 a[64:67], a[232:235], v[72:75], a[64:67]\n"
    "v_mfma_f32_16x16x32_bf16 a[64:67], a[236:239], v[76:79], a[64:67]\n"
    "ds_read_b64_tr_b16 a[244:245], v35 offset:4104\n"
    "v_mfma_f32_16x16x32_bf16 a[68:71], a[236:239], v[140:143], 0\n"
    "ds_read_b64_tr_b16 a[246:247], v35 offset:4360\n"
    "v_accvgpr_read_b32 v40, a160\n"
    "v_mfma_f32_16x16x32_bf16 a[68:71], a[232:235], v[136:139], a[68:71]\n"
    "v_accvgpr_read_b32 v41, a161\n"
    "v_accvgpr_read_b32 v42, a162\n"
    "v_mfma_f32_16x16x32_bf16 a[68:71], a[228:231], v[132:135], a[68:71]\n"
    "ds_read_b64_tr_b16 a[248:249], v35 offset:8200\n"
    "v_accvgpr_read_b32 v43, a163\n"
    "v_mfma_f32_16x16x32_bf16 a[68:71], a[224:227], v[128:131], a[68:71]\n"
    "ds_read_b64_tr_b16 a[250:251], v35 offset:8456\n"
    "v_accvgpr_read_b32 v44, a176\n"
    "v_mfma_f32_16x16x32_bf16 a[72:75], a[224:227], v[192:195], 0\n"
    "v_accvgpr_read_b32 v45, a177\n"
    "v_accvgpr_read_b32 v46, a178\n"
    "v_mfma_f32_16x16x32_bf16 a[72:75], a[228:231], v[196:199], a[72:75]\n"
    "ds_read_b64_tr_b16 a[252:253], v35 offset:12296\n"
    "v_accvgpr_read_b32 v47, a179\n"
    "v_mfma_f32_16x16x32_bf16 a[72:75], a[232:235], v[200:203], a[72:75]\n"
    "buffer_load_dwordx4 v15, s[12:15], s69 offen offset:3072 lds\n"
    "v_mfma_f32_16x16x32_bf16 a[72:75], a[236:239], v[204:207], a[72:75]\n"
    "s_add_u32 m0, s65, 0x4000\n"
    "ds_read_b64_tr_b16 a[254:255], v35 offset:12552\n"
    "v_mul_f32 v40, v37, v40\n"
    "v_mfma_f32_16x16x32_bf16 a[76:79], a[236:239], a[12:15], 0\n"
    "s_mov_b32 s69, 0x4000\n"
    "buffer_load_dwordx4 v10, s[8:11], s69 offen offset:0 lds\n"
    "v_mfma_f32_16x16x32_bf16 a[76:79], a[232:235], a[8:11], a[76:79]\n"
    "s_waitcnt vmcnt(9)\n"
    "v_mov_b32 v33, v26\n"
    "v_mul_f32 v41, v37, v41\n"
    "v_mfma_f32_16x16x32_bf16 a[76:79], a[228:231], a[4:7], a[76:79]\n"
    "buffer_load_dwordx4 v11, s[8:11], s69 offen offset:1024 lds\n"
    "v_mfma_f32_16x16x32_bf16 a[76:79], a[224:227], a[0:3], a[76:79]\n"
    "s_waitcnt lgkmcnt(0)\n"
    "v_mul_f32 v42, v37, v42\n"
    "v_mul_f32 v43, v37, v43\n"
    "v_mfma_f32_16x16x32_bf16 a[92:95], a[240:243], a[0:3], 0\n"
    "ds_read_b64_tr_b16 a[224:225], v35 offset:512\n"
    "v_mul_f32 v44, v37, v44\n"
    "v_mfma_f32_16x16x32_bf16 a[92:95], a[244:247], a[4:7], a[92:95]\n"
    "ds_read_b64_tr_b16 a[226:227], v35 offset:768\n"
    "v_mul_f32 v45, v37, v45\n"
    "v_mfma_f32_16x16x32_bf16 a[92:95], a[248:251], a[8:11], a[92:95]\n"
    "v_mul_f32 v46, v37, v46\n"
    "v_mul_f32 v47, v37, v47\n"
    "v_mfma_f32_16x16x32_bf16 a[92:95], a[252:255], a[12:15], a[92:95]\n"
    "ds_read_b64_tr_b16 a[228:229], v35 offset:4608\n"
    "v_cvt_pk_bf16_f32 v40, v40, v41\n"
    "v_mfma_f32_16x16x32_bf16 a[88:91], a[252:255], v[204:207], 0\n"
    "ds_read_b64_tr_b16 a[230:231], v35 offset:4864\n"
    "v_cvt_pk_bf16_f32 v41, v42, v43\n"
    "v_mfma_f32_16x16x32_bf16 a[88:91], a[248:251], v[200:203], a[88:91]\n"
    "v_cvt_pk_bf16_f32 v42, v44, v45\n"
    "v_cvt_pk_bf16_f32 v43, v46, v47\n"
    "v_mfma_f32_16x16x32_bf16 a[88:91], a[244:247], v[196:199], a[88:91]\n"
    "ds_read_b64_tr_b16 a[232:233], v35 offset:8704\n"
    "v_accvgpr_write_b32 a128, v40\n"
    "v_mfma_f32_16x16x32_bf16 a[88:91], a[240:243], v[192:195], a[88:91]\n"
    "ds_read_b64_tr_b16 a[234:235], v35 offset:8960\n"
    "v_accvgpr_write_b32 a129, v41\n"
    "v_mfma_f32_16x16x32_bf16 a[84:87], a[240:243], v[128:131], 0\n"
    "v_accvgpr_write_b32 a130, v42\n"
    "v_accvgpr_write_b32 a131, v43\n"
    "v_mfma_f32_16x16x32_bf16 a[84:87], a[244:247], v[132:135], a[84:87]\n"
    "ds_read_b64_tr_b16 a[236:237], v35 offset:12800\n"
    "v_accvgpr_read_b32 v48, a164\n"
    "v_mfma_f32_16x16x32_bf16 a[84:87], a[248:251], v[136:139], a[84:87]\n"
    "buffer_store_dwordx2 v[32:33], v22, s[24:27], 0 offen offset:0 sc1\n"
    "v_mfma_f32_16x16x32_bf16 a[84:87], a[252:255], v[140:143], a[84:87]\n"
    "ds_read_b64_tr_b16 a[238:239], v35 offset:13056\n"
    "v_accvgpr_read_b32 v49, a165\n"
    "v_mfma_f32_16x16x32_bf16 a[80:83], a[252:255], v[76:79], 0\n"
    "buffer_load_dwordx4 v12, s[8:11], s69 offen offset:2048 lds\n"
    "v_mfma_f32_16x16x32_bf16 a[80:83], a[248:251], v[72:75], a[80:83]\n"
    "v_accvgpr_read_b32 v50, a166\n"
    "v_accvgpr_read_b32 v51, a167\n"
    "v_mfma_f32_16x16x32_bf16 a[80:83], a[244:247], v[68:71], a[80:83]\n"
    "buffer_load_dwordx4 v13, s[8:11], s69 offen offset:3072 lds\n"
    "v_mfma_f32_16x16x32_bf16 a[80:83], a[240:243], v[64:67], a[80:83]\n"
    "s_waitcnt lgkmcnt(0)\n"
    "v_accvgpr_read_b32 v52, a180\n"
    "v_accvgpr_read_b32 v53, a181\n"
    "v_mfma_f32_16x16x32_bf16 a[96:99], a[224:227], v[64:67], 0\n"
    "ds_read_b64_tr_b16 a[240:241], v35 offset:520\n"
    "v_accvgpr_read_b32 v54, a182\n"
    "v_mfma_f32_16x16x32_bf16 a[96:99], a[228:231], v[68:71], a[96:99]\n"
    "ds_read_b64_tr_b16 a[242:243], v35 offset:776\n"
    "v_accvgpr_read_b32 v55, a183\n"
    "v_mfma_f32_16x16x32_bf16 a[96:99], a[232:235], v[72:75], a[96:99]\n"
    "v_mul_f32 v48, v38, v48\n"
    "v_mul_f32 v49, v38, v49\n"
    "v_mfma_f32_16x16x32_bf16 a[96:99], a[236:239], v[76:79], a[96:99]\n"
    "ds_read_b64_tr_b16 a[244:245], v35 offset:4616\n"
    "v_mul_f32 v50, v38, v50\n"
    "v_mfma_f32_16x16x32_bf16 a[100:103], a[236:239], v[140:143], 0\n"
    "ds_read_b64_tr_b16 a[246:247], v35 offset:4872\n"
    "v_mul_f32 v51, v38, v51\n"
    "v_mfma_f32_16x16x32_bf16 a[100:103], a[232:235], v[136:139], a[100:103]\n"
    "v_mul_f32 v52, v38, v52\n"
    "v_mul_f32 v53, v38, v53\n"
    "v_mfma_f32_16x16x32_bf16 a[100:103], a[228:231], v[132:135], a[100:103]\n"
    "ds_read_b64_tr_b16 a[248:249], v35 offset:8712\n"
    "v_mul_f32 v54, v38, v54\n"
    "v_mfma_f32_16x16x32_bf16 a[100:103], a[224:227], v[128:131], a[100:103]\n"
    "ds_read_b64_tr_b16 a[250:251], v35 offset:8968\n"
    "v_mul_f32 v55, v38, v55\n"
    "v_mfma_f32_16x16x32_bf16 a[104:107], a[224:227], v[192:195], 0\n"
    "v_cvt_pk_bf16_f32 v48, v48, v49\n"
    "v_cvt_pk_bf16_f32 v49, v50, v51\n"
    "v_mfma_f32_16x16x32_bf16 a[104:107], a[228:231], v[196:199], a[104:107]\n"
    "ds_read_b64_tr_b16 a[252:253], v35 offset:12808\n"
    "v_cvt_pk_bf16_f32 v50, v52, v53\n"
    "v_mfma_f32_16x16x32_bf16 a[104:107], a[232:235], v[200:203], a[104:107]\n"
    "v_cvt_pk_bf16_f32 v51, v54, v55\n"
    "v_accvgpr_write_b32 a132, v48\n"
    "v_mfma_f32_16x16x32_bf16 a[104:107], a[236:239], v[204:207], a[104:107]\n"
    "ds_read_b64_tr_b16 a[254:255], v35 offset:13064\n"
    "v_accvgpr_write_b32 a133, v49\n"
    "v_mfma_f32_16x16x32_bf16 a[108:111], a[236:239], a[12:15], 0\n"
    "v_accvgpr_write_b32 a134, v50\n"
    "v_accvgpr_write_b32 a135, v51\n"
    "v_mfma_f32_16x16x32_bf16 a[108:111], a[232:235], a[8:11], a[108:111]\n"
    "v_accvgpr_read_b32 v56, a168\n"
    "v_accvgpr_read_b32 v57, a169\n"
    "v_mfma_f32_16x16x32_bf16 a[108:111], a[228:231], a[4:7], a[108:111]\n"
    "v_accvgpr_read_b32 v58, a170\n"
    "v_accvgpr_read_b32 v59, a171\n"
    "v_mfma_f32_16x16x32_bf16 a[108:111], a[224:227], a[0:3], a[108:111]\n"
    "s_waitcnt lgkmcnt(0)\n"
    "v_accvgpr_read_b32 v60, a184\n"
    "v_accvgpr_read_b32 v61, a185\n"
    "v_mfma_f32_16x16x32_bf16 a[124:127], a[240:243], a[0:3], 0\n"
    "s_nop 0\n"
    "v_accvgpr_read_b32 v62, a186\n"
    "v_accvgpr_read_b32 v63, a187\n"
    "v_mfma_f32_16x16x32_bf16 a[124:127], a[244:247], a[4:7], a[124:127]\n"
    "s_barrier\n"
    "v_mul_f32 v56, v39, v56\n"
    "v_mul_f32 v57, v39, v57\n"
    "v_mfma_f32_16x16x32_bf16 a[124:127], a[248:251], a[8:11], a[124:127]\n"
    "ds_read_b64_tr_b16 a[224:225], v35 offset:16384\n"
    "v_mul_f32 v58, v39, v58\n"
    "v_mfma_f32_16x16x32_bf16 a[124:127], a[252:255], a[12:15], a[124:127]\n"
    "ds_read_b64_tr_b16 a[226:227], v35 offset:16640\n"
    "v_mul_f32 v59, v39, v59\n"
    "v_mfma_f32_16x16x32_bf16 a[120:123], a[252:255], v[204:207], 0\n"
    "ds_read_b64_tr_b16 a[228:229], v35 offset:20480\n"
    "v_mul_f32 v60, v39, v60\n"
    "v_mfma_f32_16x16x32_bf16 a[120:123], a[248:251], v[200:203], a[120:123]\n"
    "v_mul_f32 v61, v39, v61\n"
    "v_mul_f32 v62, v39, v62\n"
    "v_mfma_f32_16x16x32_bf16 a[120:123], a[244:247], v[196:199], a[120:123]\n"
    "ds_read_b64_tr_b16 a[230:231], v35 offset:20736\n"
    "v_mul_f32 v63, v39, v63\n"
    "v_mfma_f32_16x16x32_bf16 a[120:123], a[240:243], v[192:195], a[120:123]\n"
    "ds_read_b64_tr_b16 a[232:233], v35 offset:24576\n"
    "v_cvt_pk_bf16_f32 v56, v56, v57\n"
    "v_mfma_f32_16x16x32_bf16 a[116:119], a[240:243], v[128:131], 0\n"
    "ds_read_b64_tr_b16 a[234:235], v35 offset:24832\n"
    "v_cvt_pk_bf16_f32 v57, v58, v59\n"
    "v_mfma_f32_16x16x32_bf16 a[116:119], a[244:247], v[132:135], a[116:119]\n"
    "s_add_u32 m0, s65, 0xc000\n"
    "ds_read_b64_tr_b16 a[236:237], v35 offset:28672\n"
    "v_cvt_pk_bf16_f32 v58, v60, v61\n"
    "v_mfma_f32_16x16x32_bf16 a[116:119], a[248:251], v[136:139], a[116:119]\n"
    "s_mov_b32 s69, 0x4000\n"
    "buffer_load_dwordx4 v14, s[12:15], s69 offen offset:0 lds\n"
    "v_mfma_f32_16x16x32_bf16 a[116:119], a[252:255], v[140:143], a[116:119]\n"
    "ds_read_b64_tr_b16 a[238:239], v35 offset:28928\n"
    "v_cvt_pk_bf16_f32 v59, v62, v63\n"
    "v_mfma_f32_16x16x32_bf16 a[112:115], a[252:255], v[76:79], 0\n"
    "buffer_load_dwordx4 v15, s[12:15], s69 offen offset:1024 lds\n"
    "v_mfma_f32_16x16x32_bf16 a[112:115], a[248:251], v[72:75], a[112:115]\n"
    "v_accvgpr_write_b32 a136, v56\n"
    "v_accvgpr_write_b32 a137, v57\n"
    "v_mfma_f32_16x16x32_bf16 a[112:115], a[244:247], v[68:71], a[112:115]\n"
    "buffer_load_dwordx4 v14, s[12:15], s69 offen offset:2048 lds\n"
    "v_mfma_f32_16x16x32_bf16 a[112:115], a[240:243], v[64:67], a[112:115]\n"
    "s_waitcnt lgkmcnt(0)\n"
    "v_accvgpr_write_b32 a138, v58\n"
    "v_accvgpr_write_b32 a139, v59\n"
    "v_mfma_f32_16x16x32_bf16 a[64:67], a[224:227], v[80:83], a[64:67]\n"
    "ds_read_b64_tr_b16 a[240:241], v35 offset:16392\n"
    "v_accvgpr_read_b32 v40, a172\n"
    "v_mfma_f32_16x16x32_bf16 a[64:67], a[228:231], v[84:87], a[64:67]\n"
    "ds_read_b64_tr_b16 a[242:243], v35 offset:16648\n"
    "v_accvgpr_read_b32 v41, a173\n"
    "v_mfma_f32_16x16x32_bf16 a[64:67], a[232:235], v[88:91], a[64:67]\n"
    "v_accvgpr_read_b32 v42, a174\n"
    "v_accvgpr_read_b32 v43, a175\n"
    "v_mfma_f32_16x16x32_bf16 a[64:67], a[236:239], v[92:95], a[64:67]\n"
    "ds_read_b64_tr_b16 a[244:245], v35 offset:20488\n"
    "v_accvgpr_read_b32 v44, a188\n"
    "v_mfma_f32_16x16x32_bf16 a[68:71], a[236:239], v[156:159], a[68:71]\n"
    "ds_read_b64_tr_b16 a[246:247], v35 offset:20744\n"
    "v_accvgpr_read_b32 v45, a189\n"
    "v_mfma_f32_16x16x32_bf16 a[68:71], a[232:235], v[152:155], a[68:71]\n"
    "v_accvgpr_read_b32 v46, a190\n"
    "v_accvgpr_read_b32 v47, a191\n"
    "v_mfma_f32_16x16x32_bf16 a[68:71], a[228:231], v[148:151], a[68:71]\n"
    "ds_read_b64_tr_b16 a[248:249], v35 offset:24584\n"
    "v_mul_f32 v40, v20, v40\n"
    "v_mfma_f32_16x16x32_bf16 a[68:71], a[224:227], v[144:147], a[68:71]\n"
    "ds_read_b64_tr_b16 a[250:251], v35 offset:24840\n"
    "v_mul_f32 v41, v20, v41\n"
    "v_mfma_f32_16x16x32_bf16 a[72:75], a[224:227], v[208:211], a[72:75]\n"
    "v_mul_f32 v42, v20, v42\n"
    "v_mul_f32 v43, v20, v43\n"
    "v_mfma_f32_16x16x32_bf16 a[72:75], a[228:231], v[212:215], a[72:75]\n"
    "ds_read_b64_tr_b16 a[252:253], v35 offset:28680\n"
    "v_mul_f32 v44, v20, v44\n"
    "v_mfma_f32_16x16x32_bf16 a[72:75], a[232:235], v[216:219], a[72:75]\n"
    "buffer_load_dwordx4 v15, s[12:15], s69 offen offset:3072 lds\n"
    "v_mfma_f32_16x16x32_bf16 a[72:75], a[236:239], v[220:223], a[72:75]\n"
    "ds_read_b64_tr_b16 a[254:255], v35 offset:28936\n"
    "v_mul_f32 v45, v20, v45\n"
    "v_mfma_f32_16x16x32_bf16 a[76:79], a[236:239], a[28:31], a[76:79]\n"
    "v_mul_f32 v46, v20, v46\n"
    "v_mul_f32 v47, v20, v47\n"
    "v_mfma_f32_16x16x32_bf16 a[76:79], a[232:235], a[24:27], a[76:79]\n"
    "v_cvt_pk_bf16_f32 v40, v40, v41\n"
    "v_cvt_pk_bf16_f32 v41, v42, v43\n"
    "v_mfma_f32_16x16x32_bf16 a[76:79], a[228:231], a[20:23], a[76:79]\n"
    "v_cvt_pk_bf16_f32 v42, v44, v45\n"
    "v_cvt_pk_bf16_f32 v43, v46, v47\n"
    "v_mfma_f32_16x16x32_bf16 a[76:79], a[224:227], a[16:19], a[76:79]\n"
    "s_waitcnt lgkmcnt(0)\n"
    "v_accvgpr_write_b32 a140, v40\n"
    "v_accvgpr_write_b32 a141, v41\n"
    "v_mfma_f32_16x16x32_bf16 a[92:95], a[240:243], a[16:19], a[92:95]\n"
    "ds_read_b64_tr_b16 a[224:225], v35 offset:16896\n"
    "v_accvgpr_write_b32 a142, v42\n"
    "v_mfma_f32_16x16x32_bf16 a[92:95], a[244:247], a[20:23], a[92:95]\n"
    "ds_read_b64_tr_b16 a[226:227], v35 offset:17152\n"
    "v_accvgpr_write_b32 a143, v43\n"
    "v_mfma_f32_16x16x32_bf16 a[92:95], a[248:251], a[24:27], a[92:95]\n"
    "v_accvgpr_read_b32 v48, a192\n"
    "v_accvgpr_read_b32 v49, a193\n"
    "v_mfma_f32_16x16x32_bf16 a[92:95], a[252:255], a[28:31], a[92:95]\n"
    "ds_read_b64_tr_b16 a[228:229], v35 offset:20992\n"
    "v_accvgpr_read_b32 v50, a194\n"
    "v_mfma_f32_16x16x32_bf16 a[88:91], a[252:255], v[220:223], a[88:91]\n"
    "ds_read_b64_tr_b16 a[230:231], v35 offset:21248\n"
    "v_accvgpr_read_b32 v51, a195\n"
    "v_mfma_f32_16x16x32_bf16 a[88:91], a[248:251], v[216:219], a[88:91]\n"
    "v_accvgpr_read_b32 v52, a208\n"
    "v_accvgpr_read_b32 v53, a209\n"
    "v_mfma_f32_16x16x32_bf16 a[88:91], a[244:247], v[212:215], a[88:91]\n"
    "ds_read_b64_tr_b16 a[232:233], v35 offset:25088\n"
    "v_accvgpr_read_b32 v54, a210\n"
    "v_mfma_f32_16x16x32_bf16 a[88:91], a[240:243], v[208:211], a[88:91]\n"
    "ds_read_b64_tr_b16 a[234:235], v35 offset:25344\n"
    "v_accvgpr_read_b32 v55, a211\n"
    "v_mfma_f32_16x16x32_bf16 a[84:87], a[240:243], v[144:147], a[84:87]\n"
    "v_mul_f32 v48, v37, v48\n"
    "v_mul_f32 v49, v37, v49\n"
    "v_mfma_f32_16x16x32_bf16 a[84:87], a[244:247], v[148:151], a[84:87]\n"
    "ds_read_b64_tr_b16 a[236:237], v35 offset:29184\n"
    "v_mul_f32 v50, v37, v50\n"
    "v_mfma_f32_16x16x32_bf16 a[84:87], a[248:251], v[152:155], a[84:87]\n"
    "v_mul_f32 v51, v37, v51\n"
    "v_mul_f32 v52, v37, v52\n"
    "v_mfma_f32_16x16x32_bf16 a[84:87], a[252:255], v[156:159], a[84:87]\n"
    "ds_read_b64_tr_b16 a[238:239], v35 offset:29440\n"
    "v_mul_f32 v53, v37, v53\n"
    "v_mfma_f32_16x16x32_bf16 a[80:83], a[252:255], v[92:95], a[80:83]\n"
    "v_mul_f32 v54, v37, v54\n"
    "v_mul_f32 v55, v37, v55\n"
    "v_mfma_f32_16x16x32_bf16 a[80:83], a[248:251], v[88:91], a[80:83]\n"
    "v_cvt_pk_bf16_f32 v48, v48, v49\n"
    "v_cvt_pk_bf16_f32 v49, v50, v51\n"
    "v_mfma_f32_16x16x32_bf16 a[80:83], a[244:247], v[84:87], a[80:83]\n"
    "v_cvt_pk_bf16_f32 v50, v52, v53\n"
    "v_cvt_pk_bf16_f32 v51, v54, v55\n"
    "v_mfma_f32_16x16x32_bf16 a[80:83], a[240:243], v[80:83], a[80:83]\n"
    "s_waitcnt lgkmcnt(0)\n"
    "v_accvgpr_write_b32 a144, v48\n"
    "v_accvgpr_write_b32 a145, v49\n"
    "v_mfma_f32_16x16x32_bf16 a[96:99], a[224:227], v[80:83], a[96:99]\n"
    "ds_read_b64_tr_b16 a[240:241], v35 offset:16904\n"
    "v_accvgpr_write_b32 a146, v50\n"
    "v_mfma_f32_16x16x32_bf16 a[96:99], a[228:231], v[84:87], a[96:99]\n"
    "ds_read_b64_tr_b16 a[242:243], v35 offset:17160\n"
    "v_accvgpr_write_b32 a147, v51\n"
    "v_mfma_f32_16x16x32_bf16 a[96:99], a[232:235], v[88:91], a[96:99]\n"
    "v_accvgpr_read_b32 v56, a196\n"
    "v_accvgpr_read_b32 v57, a197\n"
    "v_mfma_f32_16x16x32_bf16 a[96:99], a[236:239], v[92:95], a[96:99]\n"
    "ds_read_b64_tr_b16 a[244:245], v35 offset:21000\n"
    "v_accvgpr_read_b32 v58, a198\n"
    "v_mfma_f32_16x16x32_bf16 a[100:103], a[236:239], v[156:159], a[100:103]\n"
    "ds_read_b64_tr_b16 a[246:247], v35 offset:21256\n"
    "v_accvgpr_read_b32 v59, a199\n"
    "v_mfma_f32_16x16x32_bf16 a[100:103], a[232:235], v[152:155], a[100:103]\n"
    "v_accvgpr_read_b32 v60, a212\n"
    "v_accvgpr_read_b32 v61, a213\n"
    "v_mfma_f32_16x16x32_bf16 a[100:103], a[228:231], v[148:151], a[100:103]\n"
    "ds_read_b64_tr_b16 a[248:249], v35 offset:25096\n"
    "v_accvgpr_read_b32 v62, a214\n"
    "v_mfma_f32_16x16x32_bf16 a[100:103], a[224:227], v[144:147], a[100:103]\n"
    "ds_read_b64_tr_b16 a[250:251], v35 offset:25352\n"
    "v_accvgpr_read_b32 v63, a215\n"
    "v_mfma_f32_16x16x32_bf16 a[104:107], a[224:227], v[208:211], a[104:107]\n"
    "v_mul_f32 v56, v38, v56\n"
    "v_mul_f32 v57, v38, v57\n"
    "v_mfma_f32_16x16x32_bf16 a[104:107], a[228:231], v[212:215], a[104:107]\n"
    "ds_read_b64_tr_b16 a[252:253], v35 offset:29192\n"
    "v_mul_f32 v58, v38, v58\n"
    "v_mfma_f32_16x16x32_bf16 a[104:107], a[232:235], v[216:219], a[104:107]\n"
    "v_mul_f32 v59, v38, v59\n"
    "v_mul_f32 v60, v38, v60\n"
    "v_mfma_f32_16x16x32_bf16 a[104:107], a[236:239], v[220:223], a[104:107]\n"
    "ds_read_b64_tr_b16 a[254:255], v35 offset:29448\n"
    "v_mul_f32 v61, v38, v61\n"
    "v_mfma_f32_16x16x32_bf16 a[108:111], a[236:239], a[28:31], a[108:111]\n"
    "v_mul_f32 v62, v38, v62\n"
    "v_mul_f32 v63, v38, v63\n"
    "v_mfma_f32_16x16x32_bf16 a[108:111], a[232:235], a[24:27], a[108:111]\n"
    "v_cvt_pk_bf16_f32 v56, v56, v57\n"
    "v_cvt_pk_bf16_f32 v57, v58, v59\n"
    "v_mfma_f32_16x16x32_bf16 a[108:111], a[228:231], a[20:23], a[108:111]\n"
    "v_cvt_pk_bf16_f32 v58, v60, v61\n"
    "v_cvt_pk_bf16_f32 v59, v62, v63\n"
    "v_mfma_f32_16x16x32_bf16 a[108:111], a[224:227], a[16:19], a[108:111]\n"
    "s_waitcnt lgkmcnt(0)\n"
    "v_accvgpr_write_b32 a148, v56\n"
    "v_accvgpr_write_b32 a149, v57\n"
    "v_mfma_f32_16x16x32_bf16 a[124:127], a[240:243], a[16:19], a[124:127]\n"
    "s_nop 0\n"
    "v_accvgpr_write_b32 a150, v58\n"
    "v_accvgpr_write_b32 a151, v59\n"
    "v_mfma_f32_16x16x32_bf16 a[124:127], a[244:247], a[20:23], a[124:127]\n"
    "s_barrier\n"
    "v_accvgpr_read_b32 v40, a200\n"
    "v_accvgpr_read_b32 v41, a201\n"
    "v_mfma_f32_16x16x32_bf16 a[124:127], a[248:251], a[24:27], a[124:127]\n"
    "ds_read_b64_tr_b16 a[224:225], v34 offset:0\n"
    "v_accvgpr_read_b32 v42, a202\n"
    "v_mfma_f32_16x16x32_bf16 a[124:127], a[252:255], a[28:31], a[124:127]\n"
    "ds_read_b64_tr_b16 a[226:227], v34 offset:256\n"
    "v_accvgpr_read_b32 v43, a203\n"
    "v_mfma_f32_16x16x32_bf16 a[120:123], a[252:255], v[220:223], a[120:123]\n"
    "ds_read_b64_tr_b16 a[228:229], v34 offset:4096\n"
    "v_accvgpr_read_b32 v44, a216\n"
    "v_mfma_f32_16x16x32_bf16 a[120:123], a[248:251], v[216:219], a[120:123]\n"
    "v_accvgpr_read_b32 v45, a217\n"
    "v_accvgpr_read_b32 v46, a218\n"
    "v_mfma_f32_16x16x32_bf16 a[120:123], a[244:247], v[212:215], a[120:123]\n"
    "ds_read_b64_tr_b16 a[230:231], v34 offset:4352\n"
    "v_accvgpr_read_b32 v47, a219\n"
    "v_mfma_f32_16x16x32_bf16 a[120:123], a[240:243], v[208:211], a[120:123]\n"
    "ds_read_b64_tr_b16 a[232:233], v34 offset:8192\n"
    "v_mul_f32 v40, v39, v40\n"
    "v_mfma_f32_16x16x32_bf16 a[116:119], a[240:243], v[144:147], a[116:119]\n"
    "ds_read_b64_tr_b16 a[234:235], v34 offset:8448\n"
    "v_mul_f32 v41, v39, v41\n"
    "v_mfma_f32_16x16x32_bf16 a[116:119], a[244:247], v[148:151], a[116:119]\n"
    "s_add_u32 m0, s65, 0x20000\n"
    "ds_read_b64_tr_b16 a[236:237], v34 offset:12288\n"
    "v_mul_f32 v42, v39, v42\n"
    "v_mfma_f32_16x16x32_bf16 a[116:119], a[248:251], v[152:155], a[116:119]\n"
    "s_mov_b32 s69, 0\n"
    "buffer_load_dwordx4 v16, s[28:31], s69 offen offset:0 lds\n"
    "v_mfma_f32_16x16x32_bf16 a[116:119], a[252:255], v[156:159], a[116:119]\n"
    "ds_read_b64_tr_b16 a[238:239], v34 offset:12544\n"
    "v_mul_f32 v43, v39, v43\n"
    "v_mfma_f32_16x16x32_bf16 a[112:115], a[252:255], v[92:95], a[112:115]\n"
    "buffer_load_dwordx4 v17, s[28:31], s69 offen offset:1024 lds\n"
    "v_mfma_f32_16x16x32_bf16 a[112:115], a[248:251], v[88:91], a[112:115]\n"
    "v_mul_f32 v44, v39, v44\n"
    "v_mul_f32 v45, v39, v45\n"
    "v_mfma_f32_16x16x32_bf16 a[112:115], a[244:247], v[84:87], a[112:115]\n"
    "buffer_load_dwordx4 v18, s[28:31], s69 offen offset:2048 lds\n"
    "v_mfma_f32_16x16x32_bf16 a[112:115], a[240:243], v[80:83], a[112:115]\n"
    "s_waitcnt lgkmcnt(0)\n"
    "v_mul_f32 v46, v39, v46\n"
    "v_mul_f32 v47, v39, v47\n"
    "v_mfma_f32_16x16x32_bf16 a[64:67], a[224:227], v[96:99], a[64:67]\n"
    "ds_read_b64_tr_b16 a[240:241], v34 offset:8\n"
    "v_cvt_pk_bf16_f32 v40, v40, v41\n"
    "v_mfma_f32_16x16x32_bf16 a[64:67], a[228:231], v[100:103], a[64:67]\n"
    "ds_read_b64_tr_b16 a[242:243], v34 offset:264\n"
    "v_cvt_pk_bf16_f32 v41, v42, v43\n"
    "v_mfma_f32_16x16x32_bf16 a[64:67], a[232:235], v[104:107], a[64:67]\n"
    "v_cvt_pk_bf16_f32 v42, v44, v45\n"
    "v_cvt_pk_bf16_f32 v43, v46, v47\n"
    "v_mfma_f32_16x16x32_bf16 a[64:67], a[236:239], v[108:111], a[64:67]\n"
    "ds_read_b64_tr_b16 a[244:245], v34 offset:4104\n"
    "v_accvgpr_write_b32 a152, v40\n"
    "v_mfma_f32_16x16x32_bf16 a[68:71], a[236:239], v[172:175], a[68:71]\n"
    "ds_read_b64_tr_b16 a[246:247], v34 offset:4360\n"
    "v_accvgpr_write_b32 a153, v41\n"
    "v_mfma_f32_16x16x32_bf16 a[68:71], a[232:235], v[168:171], a[68:71]\n"
    "v_accvgpr_write_b32 a154, v42\n"
    "v_accvgpr_write_b32 a155, v43\n"
    "v_mfma_f32_16x16x32_bf16 a[68:71], a[228:231], v[164:167], a[68:71]\n"
    "ds_read_b64_tr_b16 a[248:249], v34 offset:8200\n"
    "v_accvgpr_read_b32 v48, a204\n"
    "v_mfma_f32_16x16x32_bf16 a[68:71], a[224:227], v[160:163], a[68:71]\n"
    "ds_read_b64_tr_b16 a[250:251], v34 offset:8456\n"
    "v_accvgpr_read_b32 v49, a205\n"
    "v_mfma_f32_16x16x32_bf16 a[72:75], a[224:227], v[224:227], a[72:75]\n"
    "v_accvgpr_read_b32 v50, a206\n"
    "v_accvgpr_read_b32 v51, a207\n"
    "v_mfma_f32_16x16x32_bf16 a[72:75], a[228:231], v[228:231], a[72:75]\n"
    "ds_read_b64_tr_b16 a[252:253], v34 offset:12296\n"
    "v_accvgpr_read_b32 v52, a220\n"
    "v_mfma_f32_16x16x32_bf16 a[72:75], a[232:235], v[232:235], a[72:75]\n"
    "buffer_load_dwordx4 v19, s[28:31], s69 offen offset:3072 lds\n"
    "v_mfma_f32_16x16x32_bf16 a[72:75], a[236:239], v[236:239], a[72:75]\n"
    "s_add_u32 m0, s65, 0x24000\n"
    "ds_read_b64_tr_b16 a[254:255], v34 offset:12552\n"
    "v_accvgpr_read_b32 v53, a221\n"
    "v_mfma_f32_16x16x32_bf16 a[76:79], a[236:239], a[44:47], a[76:79]\n"
    "s_mov_b32 s69, 0x10000\n"
    "buffer_load_dwordx4 v16, s[28:31], s69 offen offset:0 lds\n"
    "v_mfma_f32_16x16x32_bf16 a[76:79], a[232:235], a[40:43], a[76:79]\n"
    "v_accvgpr_read_b32 v54, a222\n"
    "v_accvgpr_read_b32 v55, a223\n"
    "v_mfma_f32_16x16x32_bf16 a[76:79], a[228:231], a[36:39], a[76:79]\n"
    "buffer_load_dwordx4 v17, s[28:31], s69 offen offset:1024 lds\n"
    "v_mfma_f32_16x16x32_bf16 a[76:79], a[224:227], a[32:35], a[76:79]\n"
    "s_waitcnt lgkmcnt(0)\n"
    "v_mul_f32 v48, v20, v48\n"
    "v_mul_f32 v49, v20, v49\n"
    "v_mfma_f32_16x16x32_bf16 a[92:95], a[240:243], a[32:35], a[92:95]\n"
    "s_nop 0\n"
    "v_mul_f32 v50, v20, v50\n"
    "v_mul_f32 v51, v20, v51\n"
    "v_mfma_f32_16x16x32_bf16 a[92:95], a[244:247], a[36:39], a[92:95]\n"
    "s_barrier\n"
    "v_mul_f32 v52, v20, v52\n"
    "v_mul_f32 v53, v20, v53\n"
    "v_mfma_f32_16x16x32_bf16 a[92:95], a[248:251], a[40:43], a[92:95]\n"
    "ds_read_b64_tr_b16 a[224:225], v34 offset:16384\n"
    "v_mul_f32 v54, v20, v54\n"
    "v_mfma_f32_16x16x32_bf16 a[92:95], a[252:255], a[44:47], a[92:95]\n"
    "ds_read_b64_tr_b16 a[226:227], v34 offset:16640\n"
    "v_mul_f32 v55, v20, v55\n"
    "v_mfma_f32_16x16x32_bf16 a[88:91], a[252:255], v[236:239], a[88:91]\n"
    "ds_read_b64_tr_b16 a[228:229], v34 offset:20480\n"
    "v_cvt_pk_bf16_f32 v48, v48, v49\n"
    "v_mfma_f32_16x16x32_bf16 a[88:91], a[248:251], v[232:235], a[88:91]\n"
    "v_cvt_pk_bf16_f32 v49, v50, v51\n"
    "v_cvt_pk_bf16_f32 v50, v52, v53\n"
    "v_mfma_f32_16x16x32_bf16 a[88:91], a[244:247], v[228:231], a[88:91]\n"
    "ds_read_b64_tr_b16 a[230:231], v34 offset:20736\n"
    "v_cvt_pk_bf16_f32 v51, v54, v55\n"
    "v_mfma_f32_16x16x32_bf16 a[88:91], a[240:243], v[224:227], a[88:91]\n"
    "ds_read_b64_tr_b16 a[232:233], v34 offset:24576\n"
    "v_accvgpr_write_b32 a156, v48\n"
    "v_mfma_f32_16x16x32_bf16 a[84:87], a[240:243], v[160:163], a[84:87]\n"
    "ds_read_b64_tr_b16 a[234:235], v34 offset:24832\n"
    "v_accvgpr_write_b32 a157, v49\n"
    "v_mfma_f32_16x16x32_bf16 a[84:87], a[244:247], v[164:167], a[84:87]\n"
    "ds_read_b64_tr_b16 a[236:237], v34 offset:28672\n"
    "v_accvgpr_write_b32 a158, v50\n"
    "v_mfma_f32_16x16x32_bf16 a[84:87], a[248:251], v[168:171], a[84:87]\n"
    "buffer_load_dwordx4 v18, s[28:31], s69 offen offset:2048 lds\n"
    "v_mfma_f32_16x16x32_bf16 a[84:87], a[252:255], v[172:175], a[84:87]\n"
    "ds_read_b64_tr_b16 a[238:239], v34 offset:28928\n"
    "v_accvgpr_write_b32 a159, v51\n"
    "v_mfma_f32_16x16x32_bf16 a[80:83], a[252:255], v[108:111], a[80:83]\n"
    "buffer_load_dwordx4 v19, s[28:31], s69 offen offset:3072 lds\n"
    "v_mfma_f32_16x16x32_bf16 a[80:83], a[248:251], v[104:107], a[80:83]\n"
    "v_mfma_f32_16x16x32_bf16 a[80:83], a[244:247], v[100:103], a[80:83]\n"
    "v_mfma_f32_16x16x32_bf16 a[80:83], a[240:243], v[96:99], a[80:83]\n"
    "s_waitcnt lgkmcnt(0)\n"
    "v_mfma_f32_16x16x32_bf16 a[64:67], a[224:227], v[112:115], a[64:67]\n"
    "ds_read_b64_tr_b16 a[240:241], v34 offset:16392\n"
    "v_mfma_f32_16x16x32_bf16 a[64:67], a[228:231], v[116:119], a[64:67]\n"
    "ds_read_b64_tr_b16 a[242:243], v34 offset:16648\n"
    "v_mfma_f32_16x16x32_bf16 a[64:67], a[232:235], v[120:123], a[64:67]\n"
    "v_mfma_f32_16x16x32_bf16 a[64:67], a[236:239], v[124:127], a[64:67]\n"
    "ds_read_b64_tr_b16 a[244:245], v34 offset:20488\n"
    "v_mfma_f32_16x16x32_bf16 a[68:71], a[236:239], v[188:191], a[68:71]\n"
    "ds_read_b64_tr_b16 a[246:247], v34 offset:20744\n"
    "v_mfma_f32_16x16x32_bf16 a[68:71], a[232:235], v[184:187], a[68:71]\n"
    "v_mfma_f32_16x16x32_bf16 a[68:71], a[228:231], v[180:183], a[68:71]\n"
    "ds_read_b64_tr_b16 a[248:249], v34 offset:24584\n"
    "v_mfma_f32_16x16x32_bf16 a[68:71], a[224:227], v[176:179], a[68:71]\n"
    "ds_read_b64_tr_b16 a[250:251], v34 offset:24840\n"
    "v_mfma_f32_16x16x32_bf16 a[72:75], a[224:227], v[240:243], a[72:75]\n"
    "v_mfma_f32_16x16x32_bf16 a[72:75], a[228:231], v[244:247], a[72:75]\n"
    "ds_read_b64_tr_b16 a[252:253], v34 offset:28680\n"
    "v_mfma_f32_16x16x32_bf16 a[72:75], a[232:235], v[248:251], a[72:75]\n"
    "v_mfma_f32_16x16x32_bf16 a[72:75], a[236:239], v[252:255], a[72:75]\n"
    "ds_read_b64_tr_b16 a[254:255], v34 offset:28936\n"
    "v_mfma_f32_16x16x32_bf16 a[76:79], a[236:239], a[60:63], a[76:79]\n"
    "v_mfma_f32_16x16x32_bf16 a[76:79], a[232:235], a[56:59], a[76:79]\n"
    "v_mfma_f32_16x16x32_bf16 a[76:79], a[228:231], a[52:55], a[76:79]\n"
    "v_mfma_f32_16x16x32_bf16 a[76:79], a[224:227], a[48:51], a[76:79]\n"
    "s_waitcnt lgkmcnt(0)\n"
    "v_mfma_f32_16x16x32_bf16 a[92:95], a[240:243], a[48:51], a[92:95]\n"
    "ds_read_b64_tr_b16 a[224:225], v34 offset:512\n"
    "v_mfma_f32_16x16x32_bf16 a[92:95], a[244:247], a[52:55], a[92:95]\n"
    "ds_read_b64_tr_b16 a[226:227], v34 offset:768\n"
    "v_mfma_f32_16x16x32_bf16 a[92:95], a[248:251], a[56:59], a[92:95]\n"
    "v_mfma_f32_16x16x32_bf16 a[92:95], a[252:255], a[60:63], a[92:95]\n"
    "ds_read_b64_tr_b16 a[228:229], v34 offset:4608\n"
    "v_mfma_f32_16x16x32_bf16 a[88:91], a[252:255], v[252:255], a[88:91]\n"
    "ds_read_b64_tr_b16 a[230:231], v34 offset:4864\n"
    "v_mfma_f32_16x16x32_bf16 a[88:91], a[248:251], v[248:251], a[88:91]\n"
    "v_mfma_f32_16x16x32_bf16 a[88:91], a[244:247], v[244:247], a[88:91]\n"
    "ds_read_b64_tr_b16 a[232:233], v34 offset:8704\n"
    "v_mfma_f32_16x16x32_bf16 a[88:91], a[240:243], v[240:243], a[88:91]\n"
    "ds_read_b64_tr_b16 a[234:235], v34 offset:8960\n"
    "v_mfma_f32_16x16x32_bf16 a[84:87], a[240:243], v[176:179], a[84:87]\n"
    "v_mfma_f32_16x16x32_bf16 a[84:87], a[244:247], v[180:183], a[84:87]\n"
    "ds_read_b64_tr_b16 a[236:237], v34 offset:12800\n"
    "v_mfma_f32_16x16x32_bf16 a[84:87], a[248:251], v[184:187], a[84:87]\n"
    "v_mfma_f32_16x16x32_bf16 a[84:87], a[252:255], v[188:191], a[84:87]\n"
    "ds_read_b64_tr_b16 a[238:239], v34 offset:13056\n"
    "v_mfma_f32_16x16x32_bf16 a[80:83], a[252:255], v[124:127], a[80:83]\n"
    "v_mfma_f32_16x16x32_bf16 a[80:83], a[248:251], v[120:123], a[80:83]\n"
    "v_mfma_f32_16x16x32_bf16 a[80:83], a[244:247], v[116:119], a[80:83]\n"
    "v_mfma_f32_16x16x32_bf16 a[80:83], a[240:243], v[112:115], a[80:83]\n"
    "s_waitcnt lgkmcnt(0)\n"
    "v_mfma_f32_16x16x32_bf16 a[96:99], a[224:227], v[96:99], a[96:99]\n"
    "ds_read_b64_tr_b16 a[240:241], v34 offset:520\n"
    "v_mfma_f32_16x16x32_bf16 a[96:99], a[228:231], v[100:103], a[96:99]\n"
    "ds_read_b64_tr_b16 a[242:243], v34 offset:776\n"
    "v_mfma_f32_16x16x32_bf16 a[96:99], a[232:235], v[104:107], a[96:99]\n"
    "v_mfma_f32_16x16x32_bf16 a[96:99], a[236:239], v[108:111], a[96:99]\n"
    "ds_read_b64_tr_b16 a[244:245], v34 offset:4616\n"
    "v_mfma_f32_16x16x32_bf16 a[100:103], a[236:239], v[172:175], a[100:103]\n"
    "ds_read_b64_tr_b16 a[246:247], v34 offset:4872\n"
    "v_accvgpr_read_b32 v40, a64\n"
    "v_mfma_f32_16x16x32_bf16 a[100:103], a[232:235], v[168:171], a[100:103]\n"
    "v_accvgpr_read_b32 v41, a65\n"
    "v_accvgpr_read_b32 v42, a66\n"
    "v_mfma_f32_16x16x32_bf16 a[100:103], a[228:231], v[164:167], a[100:103]\n"
    "ds_read_b64_tr_b16 a[248:249], v34 offset:8712\n"
    "v_accvgpr_read_b32 v43, a67\n"
    "v_mfma_f32_16x16x32_bf16 a[100:103], a[224:227], v[160:163], a[100:103]\n"
    "ds_read_b64_tr_b16 a[250:251], v34 offset:8968\n"
    "v_accvgpr_read_b32 v44, a80\n"
    "v_mfma_f32_16x16x32_bf16 a[104:107], a[224:227], v[224:227], a[104:107]\n"
    "v_accvgpr_read_b32 v45, a81\n"
    "v_accvgpr_read_b32 v46, a82\n"
    "v_mfma_f32_16x16x32_bf16 a[104:107], a[228:231], v[228:231], a[104:107]\n"
    "ds_read_b64_tr_b16 a[252:253], v34 offset:12808\n"
    "v_accvgpr_read_b32 v47, a83\n"
    "v_mfma_f32_16x16x32_bf16 a[104:107], a[232:235], v[232:235], a[104:107]\n"
    "v_mul_f32 v40, v37, v40\n"
    "v_mul_f32 v41, v37, v41\n"
    "v_mfma_f32_16x16x32_bf16 a[104:107], a[236:239], v[236:239], a[104:107]\n"
    "ds_read_b64_tr_b16 a[254:255], v34 offset:13064\n"
    "v_mul_f32 v42, v37, v42\n"
    "v_mfma_f32_16x16x32_bf16 a[108:111], a[236:239], a[44:47], a[108:111]\n"
    "v_mul_f32 v43, v37, v43\n"
    "v_mul_f32 v44, v37, v44\n"
    "v_mfma_f32_16x16x32_bf16 a[108:111], a[232:235], a[40:43], a[108:111]\n"
    "v_mul_f32 v45, v37, v45\n"
    "v_mul_f32 v46, v37, v46\n"
    "v_mfma_f32_16x16x32_bf16 a[108:111], a[228:231], a[36:39], a[108:111]\n"
    "v_mul_f32 v47, v37, v47\n"
    "v_cvt_pk_bf16_f32 v40, v40, v41\n"
    "v_mfma_f32_16x16x32_bf16 a[108:111], a[224:227], a[32:35], a[108:111]\n"
    "s_waitcnt lgkmcnt(0)\n"
    "v_cvt_pk_bf16_f32 v41, v42, v43\n"
    "v_cvt_pk_bf16_f32 v42, v44, v45\n"
    "v_mfma_f32_16x16x32_bf16 a[124:127], a[240:243], a[32:35], a[124:127]\n"
    "ds_read_b64_tr_b16 a[224:225], v34 offset:16896\n"
    "v_cvt_pk_bf16_f32 v43, v46, v47\n"
    "v_mfma_f32_16x16x32_bf16 a[124:127], a[244:247], a[36:39], a[124:127]\n"
    "ds_read_b64_tr_b16 a[226:227], v34 offset:17152\n"
    "v_accvgpr_write_b32 a176, v40\n"
    "v_mfma_f32_16x16x32_bf16 a[124:127], a[248:251], a[40:43], a[124:127]\n"
    "v_accvgpr_write_b32 a177, v41\n"
    "v_accvgpr_write_b32 a178, v42\n"
    "v_mfma_f32_16x16x32_bf16 a[124:127], a[252:255], a[44:47], a[124:127]\n"
    "ds_read_b64_tr_b16 a[228:229], v34 offset:20992\n"
    "v_accvgpr_write_b32 a179, v43\n"
    "v_mfma_f32_16x16x32_bf16 a[120:123], a[252:255], v[236:239], a[120:123]\n"
    "ds_read_b64_tr_b16 a[230:231], v34 offset:21248\n"
    "v_accvgpr_read_b32 v48, a68\n"
    "v_mfma_f32_16x16x32_bf16 a[120:123], a[248:251], v[232:235], a[120:123]\n"
    "v_accvgpr_read_b32 v49, a69\n"
    "v_accvgpr_read_b32 v50, a70\n"
    "v_mfma_f32_16x16x32_bf16 a[120:123], a[244:247], v[228:231], a[120:123]\n"
    "ds_read_b64_tr_b16 a[232:233], v34 offset:25088\n"
    "v_accvgpr_read_b32 v51, a71\n"
    "v_mfma_f32_16x16x32_bf16 a[120:123], a[240:243], v[224:227], a[120:123]\n"
    "ds_read_b64_tr_b16 a[234:235], v34 offset:25344\n"
    "v_accvgpr_read_b32 v52, a84\n"
    "v_mfma_f32_16x16x32_bf16 a[116:119], a[240:243], v[160:163], a[116:119]\n"
    "v_accvgpr_read_b32 v53, a85\n"
    "v_accvgpr_read_b32 v54, a86\n"
    "v_mfma_f32_16x16x32_bf16 a[116:119], a[244:247], v[164:167], a[116:119]\n"
    "ds_read_b64_tr_b16 a[236:237], v34 offset:29184\n"
    "v_accvgpr_read_b32 v55, a87\n"
    "v_mfma_f32_16x16x32_bf16 a[116:119], a[248:251], v[168:171], a[116:119]\n"
    "buffer_load_dwordx2 v[26:27], v23, s[24:27], s51 offen offset:256 sc1\n"
    "v_mfma_f32_16x16x32_bf16 a[116:119], a[252:255], v[172:175], a[116:119]\n"
    "ds_read_b64_tr_b16 a[238:239], v34 offset:29440\n"
    "v_mul_f32 v48, v38, v48\n"
    "v_mfma_f32_16x16x32_bf16 a[112:115], a[252:255], v[108:111], a[112:115]\n"
    "v_mul_f32 v49, v38, v49\n"
    "v_mul_f32 v50, v38, v50\n"
    "v_mfma_f32_16x16x32_bf16 a[112:115], a[248:251], v[104:107], a[112:115]\n"
    "v_mul_f32 v51, v38, v51\n"
    "v_mul_f32 v52, v38, v52\n"
    "v_mfma_f32_16x16x32_bf16 a[112:115], a[244:247], v[100:103], a[112:115]\n"
    "v_mul_f32 v53, v38, v53\n"
    "v_mul_f32 v54, v38, v54\n"
    "v_mfma_f32_16x16x32_bf16 a[112:115], a[240:243], v[96:99], a[112:115]\n"
    "s_waitcnt lgkmcnt(0)\n"
    "v_mul_f32 v55, v38, v55\n"
    "v_cvt_pk_bf16_f32 v48, v48, v49\n"
    "v_mfma_f32_16x16x32_bf16 a[96:99], a[224:227], v[112:115], a[96:99]\n"
    "ds_read_b64_tr_b16 a[240:241], v34 offset:16904\n"
    "v_cvt_pk_bf16_f32 v49, v50, v51\n"
    "v_mfma_f32_16x16x32_bf16 a[96:99], a[228:231], v[116:119], a[96:99]\n"
    "ds_read_b64_tr_b16 a[242:243], v34 offset:17160\n"
    "v_cvt_pk_bf16_f32 v50, v52, v53\n"
    "v_mfma_f32_16x16x32_bf16 a[96:99], a[232:235], v[120:123], a[96:99]\n"
    "v_cvt_pk_bf16_f32 v51, v54, v55\n"
    "v_accvgpr_write_b32 a180, v48\n"
    "v_mfma_f32_16x16x32_bf16 a[96:99], a[236:239], v[124:127], a[96:99]\n"
    "ds_read_b64_tr_b16 a[244:245], v34 offset:21000\n"
    "v_accvgpr_write_b32 a181, v49\n"
    "v_mfma_f32_16x16x32_bf16 a[100:103], a[236:239], v[188:191], a[100:103]\n"
    "ds_read_b64_tr_b16 a[246:247], v34 offset:21256\n"
    "v_accvgpr_write_b32 a182, v50\n"
    "v_mfma_f32_16x16x32_bf16 a[100:103], a[232:235], v[184:187], a[100:103]\n"
    "v_accvgpr_write_b32 a183, v51\n"
    "v_accvgpr_read_b32 v40, a72\n"
    "v_mfma_f32_16x16x32_bf16 a[100:103], a[228:231], v[180:183], a[100:103]\n"
    "ds_read_b64_tr_b16 a[248:249], v34 offset:25096\n"
    "v_accvgpr_read_b32 v41, a73\n"
    "v_mfma_f32_16x16x32_bf16 a[100:103], a[224:227], v[176:179], a[100:103]\n"
    "ds_read_b64_tr_b16 a[250:251], v34 offset:25352\n"
    "v_accvgpr_read_b32 v42, a74\n"
    "v_mfma_f32_16x16x32_bf16 a[104:107], a[224:227], v[240:243], a[104:107]\n"
    "v_accvgpr_read_b32 v43, a75\n"
    "v_accvgpr_read_b32 v44, a88\n"
    "v_mfma_f32_16x16x32_bf16 a[104:107], a[228:231], v[244:247], a[104:107]\n"
    "ds_read_b64_tr_b16 a[252:253], v34 offset:29192\n"
    "v_accvgpr_read_b32 v45, a89\n"
    "v_mfma_f32_16x16x32_bf16 a[104:107], a[232:235], v[248:251], a[104:107]\n"
    "v_accvgpr_read_b32 v46, a90\n"
    "v_accvgpr_read_b32 v47, a91\n"
    "v_mfma_f32_16x16x32_bf16 a[104:107], a[236:239], v[252:255], a[104:107]\n"
    "ds_read_b64_tr_b16 a[254:255], v34 offset:29448\n"
    "v_mul_f32 v40, v39, v40\n"
    "v_mfma_f32_16x16x32_bf16 a[108:111], a[236:239], a[60:63], a[108:111]\n"
    "v_mul_f32 v41, v39, v41\n"
    "v_mul_f32 v42, v39, v42\n"
    "v_mfma_f32_16x16x32_bf16 a[108:111], a[232:235], a[56:59], a[108:111]\n"
    "v_mul_f32 v43, v39, v43\n"
    "v_mul_f32 v44, v39, v44\n"
    "v_mfma_f32_16x16x32_bf16 a[108:111], a[228:231], a[52:55], a[108:111]\n"
    "v_mul_f32 v45, v39, v45\n"
    "v_mul_f32 v46, v39, v46\n"
    "v_mfma_f32_16x16x32_bf16 a[108:111], a[224:227], a[48:51], a[108:111]\n"
    "s_waitcnt lgkmcnt(0)\n"
    "v_mul_f32 v47, v39, v47\n"
    "v_cvt_pk_bf16_f32 v40, v40, v41\n"
    "v_mfma_f32_16x16x32_bf16 a[124:127], a[240:243], a[48:51], a[124:127]\n"
    "s_waitcnt vmcnt(18)\n"
    "v_cvt_pk_bf16_f32 v41, v42, v43\n"
    "v_cvt_pk_bf16_f32 v42, v44, v45\n"
    "v_mfma_f32_16x16x32_bf16 a[124:127], a[244:247], a[52:55], a[124:127]\n"
    "s_barrier\n"
    "v_cvt_pk_bf16_f32 v43, v46, v47\n"
    "v_accvgpr_write_b32 a184, v40\n"
    "v_mfma_f32_16x16x32_bf16 a[124:127], a[248:251], a[56:59], a[124:127]\n"
    "ds_read_b128 a[192:195], v4 offset:0\n"
    "v_accvgpr_write_b32 a185, v41\n"
    "v_mfma_f32_16x16x32_bf16 a[124:127], a[252:255], a[60:63], a[124:127]\n"
    "ds_read_b128 a[196:199], v5 offset:0\n"
    "v_accvgpr_write_b32 a186, v42\n"
    "v_mfma_f32_16x16x32_bf16 a[120:123], a[252:255], v[252:255], a[120:123]\n"
    "ds_read_b128 a[200:203], v6 offset:0\n"
    "v_accvgpr_write_b32 a187, v43\n"
    "v_mfma_f32_16x16x32_bf16 a[120:123], a[248:251], v[248:251], a[120:123]\n"
    "v_accvgpr_read_b32 v40, a76\n"
    "v_accvgpr_read_b32 v41, a77\n"
    "v_mfma_f32_16x16x32_bf16 a[120:123], a[244:247], v[244:247], a[120:123]\n"
    "ds_read_b128 a[204:207], v7 offset:0\n"
    "v_accvgpr_read_b32 v42, a78\n"
    "v_mfma_f32_16x16x32_bf16 a[120:123], a[240:243], v[240:243], a[120:123]\n"
    "ds_read_b128 a[208:211], v4 offset:4096\n"
    "v_accvgpr_read_b32 v43, a79\n"
    "v_mfma_f32_16x16x32_bf16 a[116:119], a[240:243], v[176:179], a[116:119]\n"
    "ds_read_b128 a[212:215], v5 offset:4096\n"
    "v_accvgpr_read_b32 v44, a92\n"
    "v_mfma_f32_16x16x32_bf16 a[116:119], a[244:247], v[180:183], a[116:119]\n"
    "s_add_u32 m0, s65, 0x10000\n"
    "ds_read_b128 a[216:219], v6 offset:4096\n"
    "v_accvgpr_read_b32 v45, a93\n"
    "v_mfma_f32_16x16x32_bf16 a[116:119], a[248:251], v[184:187], a[116:119]\n"
    "s_mov_b32 s69, 0x8000\n"
    "buffer_load_dwordx4 v10, s[8:11], s69 offen offset:0 lds\n"
    "v_mfma_f32_16x16x32_bf16 a[116:119], a[252:255], v[188:191], a[116:119]\n"
    "ds_read_b128 a[220:223], v7 offset:4096\n"
    "v_accvgpr_read_b32 v46, a94\n"
    "v_mfma_f32_16x16x32_bf16 a[112:115], a[252:255], v[124:127], a[112:115]\n"
    "buffer_load_dwordx4 v11, s[8:11], s69 offen offset:1024 lds\n"
    "v_mfma_f32_16x16x32_bf16 a[112:115], a[248:251], v[120:123], a[112:115]\n"
    "v_accvgpr_read_b32 v47, a95\n"
    "v_mul_f32 v40, v20, v40\n"
    "v_mfma_f32_16x16x32_bf16 a[112:115], a[244:247], v[116:119], a[112:115]\n"
    "buffer_load_dwordx4 v12, s[8:11], s69 offen offset:2048 lds\n"
    "v_mfma_f32_16x16x32_bf16 a[112:115], a[240:243], v[112:115], a[112:115]\n"
    "v_mul_f32 v41, v20, v41\n"
    "v_mul_f32 v42, v20, v42\n"
    "buffer_load_dwordx4 v13, s[8:11], s69 offen offset:3072 lds\n"
    "v_mul_f32 v43, v20, v43\n"
    "v_mul_f32 v44, v20, v44\n"
    "v_mul_f32 v45, v20, v45\n"
    "v_mul_f32 v46, v20, v46\n"
    "v_mul_f32 v47, v20, v47\n"
    "v_cvt_pk_bf16_f32 v40, v40, v41\n"
    "v_cvt_pk_bf16_f32 v41, v42, v43\n"
    "v_cvt_pk_bf16_f32 v42, v44, v45\n"
    "v_cvt_pk_bf16_f32 v43, v46, v47\n"
    "v_accvgpr_write_b32 a188, v40\n"
    "v_accvgpr_write_b32 a189, v41\n"
    "v_accvgpr_write_b32 a190, v42\n"
    "v_accvgpr_write_b32 a191, v43\n"
    "v_and_b32 v96, 3, v1\n"
    "v_cmp_eq_u32 vcc, 0, v96\n"
    "v_mov_b32 v97, 0x3f803f80\n"
    "s_add_u32 s66, s65, 0x10000\n"
    "s_mov_b32 s67, 0x8000\n"
    "s_mov_b32 s99, 0x7060302\n"
    "v_cndmask_b32 v120, 0, v97, vcc\n"
    "v_cndmask_b32 v121, 0, v97, vcc\n"
    "v_cndmask_b32 v122, 0, v97, vcc\n"
    "v_cndmask_b32 v123, 0, v97, vcc\n"
    "s_waitcnt lgkmcnt(0)\n"
    "v_accvgpr_read_b32 v136, a108\n"
    "v_accvgpr_read_b32 v137, a109\n"
    "v_accvgpr_read_b32 v138, a110\n"
    "v_accvgpr_read_b32 v139, a111\n"
    "v_accvgpr_read_b32 v140, a124\n"
    "v_accvgpr_read_b32 v141, a125\n"
    "v_accvgpr_read_b32 v142, a126\n"
    "v_accvgpr_read_b32 v143, a127\n"
    "v_mul_f32 v136, v20, v136\n"
    "v_mul_f32 v137, v20, v137\n"
    "v_mul_f32 v138, v20, v138\n"
    "v_mul_f32 v139, v20, v139\n"
    "v_mul_f32 v140, v20, v140\n"
    "v_mul_f32 v141, v20, v141\n"
    "v_mul_f32 v142, v20, v142\n"
    "v_mul_f32 v143, v20, v143\n"
    "v_cvt_pk_bf16_f32 v28, v136, v137\n"
    "v_cvt_pk_bf16_f32 v29, v138, v139\n"
    "v_cvt_pk_bf16_f32 v30, v140, v141\n"
    "v_cvt_pk_bf16_f32 v31, v142, v143\n"
    "v_accvgpr_read_b32 v128, a104\n"
    "v_accvgpr_read_b32 v129, a105\n"
    "v_mfma_f32_16x16x32_bf16 v[92:95], a[192:195], a[140:143], 0\n"
    "v_accvgpr_read_b32 v130, a106\n"
    "v_accvgpr_read_b32 v131, a107\n"
    "v_accvgpr_read_b32 v132, a120\n"
    "v_mfma_f32_16x16x32_bf16 v[92:95], a[196:199], a[156:159], v[92:95]\n"
    "v_accvgpr_read_b32 v133, a121\n"
    "v_accvgpr_read_b32 v134, a122\n"
    "v_accvgpr_read_b32 v135, a123\n"
    "v_mfma_f32_16x16x32_bf16 v[92:95], a[200:203], a[188:191], v[92:95]\n"
    "v_mul_f32 v128, v39, v128\n"
    "v_mul_f32 v129, v39, v129\n"
    "v_mul_f32 v130, v39, v130\n"
    "v_mfma_f32_16x16x32_bf16 v[92:95], a[204:207], v[28:31], v[92:95]\n"
    "v_mul_f32 v131, v39, v131\n"
    "v_mul_f32 v132, v39, v132\n"
    "v_mul_f32 v133, v39, v133\n"
    "v_mul_f32 v134, v39, v134\n"
    "v_mul_f32 v135, v39, v135\n"
    "v_cvt_pk_bf16_f32 v124, v128, v129\n"
    "v_cvt_pk_bf16_f32 v125, v130, v131\n"
    "v_cvt_pk_bf16_f32 v126, v132, v133\n"
    "v_cvt_pk_bf16_f32 v127, v134, v135\n"
    "v_accvgpr_read_b32 v136, a100\n"
    "v_max3_f32 v103, v92, v93, v94\n"
    "v_accvgpr_read_b32 v137, a101\n"
    "v_mfma_f32_16x16x32_bf16 v[88:91], a[192:195], a[136:139], 0\n"
    "v_max_f32 v103, v103, v95\n"
    "v_accvgpr_read_b32 v138, a102\n"
    "v_mov_b32 v107, v103\n"
    "v_accvgpr_read_b32 v139, a103\n"
    "s_nop 0\n"
    "v_permlane16_swap_b32 v103, v107\n"
    "v_accvgpr_read_b32 v140, a116\n"
    "v_mfma_f32_16x16x32_bf16 v[88:91], a[196:199], a[152:155], v[88:91]\n"
    "v_max_f32 v103, v103, v107\n"
    "v_accvgpr_read_b32 v141, a117\n"
    "v_mov_b32 v107, v103\n"
    "v_accvgpr_read_b32 v142, a118\n"
    "s_nop 0\n"
    "v_permlane32_swap_b32 v103, v107\n"
    "v_accvgpr_read_b32 v143, a119\n"
    "v_mfma_f32_16x16x32_bf16 v[88:91], a[200:203], a[184:187], v[88:91]\n"
    "v_max_f32 v103, v103, v107\n"
    "v_mul_f32 v136, v38, v136\n"
    "v_sub_f32 v92, 0xc1400000, v103\n"
    "v_mul_f32 v137, v38, v137\n"
    "v_sub_f32 v93, 0xc1400000, v103\n"
    "v_mul_f32 v138, v38, v138\n"
    "v_mfma_f32_16x16x32_bf16 v[88:91], a[204:207], v[124:127], v[88:91]\n"
    "v_sub_f32 v94, 0xc1400000, v103\n"
    "v_mul_f32 v139, v38, v139\n"
    "v_sub_f32 v95, 0xc1400000, v103\n"
    "v_mul_f32 v140, v38, v140\n"
    "v_mul_f32 v141, v38, v141\n"
    "v_mul_f32 v142, v38, v142\n"
    "v_mul_f32 v143, v38, v143\n"
    "v_cvt_pk_bf16_f32 v60, v136, v137\n"
    "v_cvt_pk_bf16_f32 v61, v138, v139\n"
    "v_cvt_pk_bf16_f32 v62, v140, v141\n"
    "v_cvt_pk_bf16_f32 v63, v142, v143\n"
    "v_accvgpr_read_b32 v128, a96\n"
    "v_max3_f32 v102, v88, v89, v90\n"
    "v_accvgpr_read_b32 v129, a97\n"
    "v_mfma_f32_16x16x32_bf16 v[84:87], a[192:195], a[132:135], 0\n"
    "v_max_f32 v102, v102, v91\n"
    "v_accvgpr_read_b32 v130, a98\n"
    "v_mov_b32 v106, v102\n"
    "v_accvgpr_read_b32 v131, a99\n"
    "s_nop 0\n"
    "v_permlane16_swap_b32 v102, v106\n"
    "v_accvgpr_read_b32 v132, a112\n"
    "v_mfma_f32_16x16x32_bf16 v[84:87], a[196:199], a[148:151], v[84:87]\n"
    "v_max_f32 v102, v102, v106\n"
    "v_accvgpr_read_b32 v133, a113\n"
    "v_mov_b32 v106, v102\n"
    "v_accvgpr_read_b32 v134, a114\n"
    "s_nop 0\n"
    "v_permlane32_swap_b32 v102, v106\n"
    "v_accvgpr_read_b32 v135, a115\n"
    "v_mfma_f32_16x16x32_bf16 v[84:87], a[200:203], a[180:183], v[84:87]\n"
    "v_max_f32 v102, v102, v106\n"
    "v_mul_f32 v128, v37, v128\n"
    "v_sub_f32 v88, 0xc1400000, v102\n"
    "v_mul_f32 v129, v37, v129\n"
    "v_sub_f32 v89, 0xc1400000, v102\n"
    "v_mul_f32 v130, v37, v130\n"
    "v_mfma_f32_16x16x32_bf16 v[84:87], a[204:207], v[60:63], v[84:87]\n"
    "v_sub_f32 v90, 0xc1400000, v102\n"
    "v_mul_f32 v131, v37, v131\n"
    "v_sub_f32 v91, 0xc1400000, v102\n"
    "v_mul_f32 v132, v37, v132\n"
    "v_mul_f32 v133, v37, v133\n"
    "v_mul_f32 v134, v37, v134\n"
    "v_mul_f32 v135, v37, v135\n"
    "v_cvt_pk_bf16_f32 v56, v128, v129\n"
    "v_cvt_pk_bf16_f32 v57, v130, v131\n"
    "v_cvt_pk_bf16_f32 v58, v132, v133\n"
    "v_cvt_pk_bf16_f32 v59, v134, v135\n"
    "v_max3_f32 v101, v84, v85, v86\n"
    "v_max_f32 v101, v101, v87\n"
    "v_mfma_f32_16x16x32_bf16 v[80:83], a[192:195], a[128:131], 0\n"
    "v_mov_b32 v105, v101\n"
    "s_nop 1\n"
    "v_permlane16_swap_b32 v101, v105\n"
    "v_mfma_f32_16x16x32_bf16 v[80:83], a[196:199], a[144:147], v[80:83]\n"
    "v_max_f32 v101, v101, v105\n"
    "v_mov_b32 v105, v101\n"
    "v_mfma_f32_16x16x32_bf16 v[80:83], a[200:203], a[176:179], v[80:83]\n"
    "s_nop 0\n"
    "v_permlane32_swap_b32 v101, v105\n"
    "v_max_f32 v101, v101, v105\n"
    "v_mfma_f32_16x16x32_bf16 v[80:83], a[204:207], v[56:59], v[80:83]\n"
    "v_sub_f32 v84, 0xc1400000, v101\n"
    "v_sub_f32 v85, 0xc1400000, v101\n"
    "v_sub_f32 v86, 0xc1400000, v101\n"
    "v_sub_f32 v87, 0xc1400000, v101\n"
    "s_nop 3\n"
    "v_max3_f32 v100, v80, v81, v82\n"
    "v_max_f32 v100, v100, v83\n"
    "v_mov_b32 v104, v100\n"
    "s_nop 1\n"
    "v_permlane16_swap_b32 v100, v104\n"
    "v_max_f32 v100, v100, v104\n"
    "v_mov_b32 v104, v100\n"
    "s_nop 1\n"
    "v_permlane32_swap_b32 v100, v104\n"
    "v_max_f32 v100, v100, v104\n"
    "v_sub_f32 v80, 0xc1400000, v100\n"
    "v_sub_f32 v81, 0xc1400000, v100\n"
    "v_sub_f32 v82, 0xc1400000, v100\n"
    "v_sub_f32 v83, 0xc1400000, v100\n"
    "L_go%=:\n"
    "s_waitcnt lgkmcnt(0)\n"
    "s_nop 1\n"
    "v_mfma_f32_16x16x32_bf16 v[96:99], a[192:195], a[128:131], v[80:83]\n"
    "v_mfma_f32_16x16x32_bf16 v[96:99], a[196:199], a[144:147], v[96:99]\n"
    "v_mfma_f32_16x16x32_bf16 v[96:99], a[200:203], a[176:179], v[96:99]\n"
    "ds_read_b128 a[224:227], v4 offset:8192\n"
    "v_mfma_f32_16x16x32_bf16 v[96:99], a[204:207], v[56:59], v[96:99]\n"
    "s_add_u32 m0, s66, 0x8000\n"
    "v_mfma_f32_16x16x32_bf16 v[100:103], a[220:223], v[56:59], v[80:83]\n"
    "buffer_load_dwordx4 v14, s[12:15], s67 offen offset:0 lds\n"
    "v_mfma_f32_16x16x32_bf16 v[100:103], a[216:219], a[176:179], v[100:103]\n"
    "v_mfma_f32_16x16x32_bf16 v[100:103], a[212:215], a[144:147], v[100:103]\n"
    "ds_read_b128 a[228:231], v5 offset:8192\n"
    "v_mfma_f32_16x16x32_bf16 v[100:103], a[208:211], a[128:131], v[100:103]\n"
    "s_nop 0\n"
    "v_exp_f32 v96, v96\n"
    "ds_read_b128 a[232:235], v6 offset:8192\n"
    "v_exp_f32 v97, v97\n"
    "s_nop 0\n"
    "v_cvt_pk_bf16_f32 v40, v96, v97\n"
    "v_exp_f32 v98, v98\n"
    "ds_read_b128 a[236:239], v7 offset:8192\n"
    "v_exp_f32 v99, v99\n"
    "s_nop 0\n"
    "v_cvt_pk_bf16_f32 v41, v98, v99\n"
    "v_exp_f32 v100, v100\n"
    "ds_read_b128 a[240:243], v4 offset:12288\n"
    "v_mfma_f32_16x16x32_bf16 v[104:107], a[192:195], a[132:135], v[84:87]\n"
    "ds_read_b64_tr_b16 a[0:1], v8 offset:0\n"
    "ds_read_b64_tr_b16 a[2:3], v8 offset:4096\n"
    "v_mfma_f32_16x16x32_bf16 v[104:107], a[196:199], a[148:151], v[104:107]\n"
    "v_exp_f32 v101, v101\n"
    "v_mfma_f32_16x16x32_bf16 v[104:107], a[200:203], a[180:183], v[104:107]\n"
    "ds_read_b64_tr_b16 a[4:5], v9 offset:0\n"
    "v_cvt_pk_bf16_f32 v42, v100, v101\n"
    "v_mfma_f32_16x16x32_bf16 v[104:107], a[204:207], v[60:63], v[104:107]\n"
    "v_exp_f32 v102, v102\n"
    "v_mfma_f32_16x16x32_bf16 v[108:111], a[220:223], v[60:63], v[84:87]\n"
    "buffer_load_dwordx4 v15, s[12:15], s67 offen offset:1024 lds\n"
    "v_mfma_f32_16x16x32_bf16 v[108:111], a[216:219], a[180:183], v[108:111]\n"
    "v_exp_f32 v103, v103\n"
    "v_mfma_f32_16x16x32_bf16 v[108:111], a[212:215], a[148:151], v[108:111]\n"
    "ds_read_b64_tr_b16 a[6:7], v9 offset:4096\n"
    "v_cvt_pk_bf16_f32 v43, v102, v103\n"
    "v_mfma_f32_16x16x32_bf16 v[108:111], a[208:211], a[132:135], v[108:111]\n"
    "v_exp_f32 v104, v104\n"
    "ds_read_b64_tr_b16 a[8:9], v8 offset:512\n"
    "ds_read_b64_tr_b16 a[10:11], v8 offset:4608\n"
    "v_exp_f32 v105, v105\n"
    "s_waitcnt vmcnt(6)\n"
    "s_or_b32 s68, s64, s74\n"
    "s_cbranch_scc1 L_gok%=\n"
    "s_mov_b32 s86, 0\n"
    "L_grt%=:\n"
    "v_cmp_ne_u32 vcc, s87, v27\n"
    "s_mov_b32 s68, vcc_lo\n"
    "v_cmp_ne_u32 vcc, v26, v33\n"
    "s_or_b32 s68, s68, vcc_lo\n"
    "s_cbranch_scc0 L_gok%=\n"
    "v_mov_b32 v33, v26\n"
    "buffer_store_dwordx2 v[32:33], v22, s[24:27], 0 offen offset:0 sc1\n"
    "s_sleep 2\n"
    "buffer_load_dwordx2 v[26:27], v23, s[24:27], s51 offen offset:256 sc1\n"
    "s_waitcnt vmcnt(0)\n"
    "s_add_u32 s86, s86, 1\n"
    "s_cmp_lt_u32 s86, 0x100000\n"
    "s_cbranch_scc1 L_grt%=\n"
    "L_gok%=:\n"
    "v_cvt_pk_bf16_f32 v112, v104, v105\n"
    "v_exp_f32 v106, v106\n"
    "ds_read_b64_tr_b16 a[12:13], v9 offset:512\n"
    "ds_read_b64_tr_b16 a[14:15], v9 offset:4608\n"
    "v_exp_f32 v107, v107\n"
    "v_mov_b32 v32, s90\n"
    "v_mov_b32 v33, s91\n"
    "v_cvt_pk_bf16_f32 v113, v106, v107\n"
    "v_exp_f32 v108, v108\n"
    "buffer_store_dwordx2 v[32:33], v3, s[24:27], s49 offen offset:0 sc1\n"
    "v_mfma_f32_16x16x32_bf16 v[96:99], a[192:195], a[136:139], v[88:91]\n"
    "ds_read_b64_tr_b16 a[16:17], v8 offset:1024\n"
    "ds_read_b64_tr_b16 a[18:19], v8 offset:5120\n"
    "v_mfma_f32_16x16x32_bf16 v[96:99], a[196:199], a[152:155], v[96:99]\n"
    "v_exp_f32 v109, v109\n"
    "v_mfma_f32_16x16x32_bf16 v[96:99], a[200:203], a[184:187], v[96:99]\n"
    "ds_read_b64_tr_b16 a[20:21], v9 offset:1024\n"
    "v_cvt_pk_bf16_f32 v114, v108, v109\n"
    "v_mfma_f32_16x16x32_bf16 v[96:99], a[204:207], v[124:127], v[96:99]\n"
    "v_exp_f32 v110, v110\n"
    "v_mfma_f32_16x16x32_bf16 v[100:103], a[220:223], v[124:127], v[88:91]\n"
    "buffer_load_dwordx4 v14, s[12:15], s67 offen offset:2048 lds\n"
    "v_mfma_f32_16x16x32_bf16 v[100:103], a[216:219], a[184:187], v[100:103]\n"
    "v_exp_f32 v111, v111\n"
    "v_mfma_f32_16x16x32_bf16 v[100:103], a[212:215], a[152:155], v[100:103]\n"
    "ds_read_b64_tr_b16 a[22:23], v9 offset:5120\n"
    "v_cvt_pk_bf16_f32 v115, v110, v111\n"
    "v_mfma_f32_16x16x32_bf16 v[100:103], a[208:211], a[136:139], v[100:103]\n"
    "s_waitcnt lgkmcnt(4)\n"
    "v_exp_f32 v96, v96\n"
    "ds_read_b64_tr_b16 a[24:25], v8 offset:1536\n"
    "ds_read_b64_tr_b16 a[26:27], v8 offset:5632\n"
    "v_mfma_f32_16x16x32_bf16 v[160:163], a[0:3], v[112:115], 0\n"
    "v_exp_f32 v97, v97\n"
    "v_mov_b32 v22, -1\n"
    "v_mov_b32 v23, -1\n"
    "v_cvt_pk_bf16_f32 v48, v96, v97\n"
    "v_mfma_f32_16x16x32_bf16 v[164:167], a[4:7], v[112:115], 0\n"
    "v_exp_f32 v98, v98\n"
    "ds_read_b64_tr_b16 a[28:29], v9 offset:1536\n"
    "ds_read_b64_tr_b16 a[30:31], v9 offset:5632\n"
    "v_mfma_f32_16x16x32_bf16 v[168:171], a[8:11], v[112:115], 0\n"
    "v_exp_f32 v99, v99\n"
    "v_mov_b32 v24, -1\n"
    "v_mov_b32 v25, -1\n"
    "v_cvt_pk_bf16_f32 v49, v98, v99\n"
    "v_mfma_f32_16x16x32_bf16 v[172:175], a[12:15], v[112:115], 0\n"
    "v_exp_f32 v100, v100\n"
    "v_bfe_u32 v37, v1, 1, 3\n"
    "v_mfma_f32_16x16x32_bf16 v[104:107], a[192:195], a[140:143], v[92:95]\n"
    "ds_read_b128 a[244:247], v5 offset:12288\n"
    "v_and_b32 v36, 1, v1\n"
    "v_mfma_f32_16x16x32_bf16 v[104:107], a[196:199], a[156:159], v[104:107]\n"
    "v_exp_f32 v101, v101\n"
    "v_mfma_f32_16x16x32_bf16 v[104:107], a[200:203], a[188:191], v[104:107]\n"
    "ds_read_b128 a[248:251], v6 offset:12288\n"
    "v_cvt_pk_bf16_f32 v50, v100, v101\n"
    "v_mfma_f32_16x16x32_bf16 v[104:107], a[204:207], v[28:31], v[104:107]\n"
    "v_exp_f32 v102, v102\n"
    "v_mfma_f32_16x16x32_bf16 v[108:111], a[220:223], v[28:31], v[92:95]\n"
    "buffer_load_dwordx4 v15, s[12:15], s67 offen offset:3072 lds\n"
    "v_mfma_f32_16x16x32_bf16 v[108:111], a[216:219], a[188:191], v[108:111]\n"
    "s_add_u32 s67, s67, 0x4000\n"
    "v_exp_f32 v103, v103\n"
    "v_mfma_f32_16x16x32_bf16 v[108:111], a[212:215], a[156:159], v[108:111]\n"
    "ds_read_b128 a[252:255], v7 offset:12288\n"
    "v_cvt_pk_bf16_f32 v51, v102, v103\n"
    "v_mfma_f32_16x16x32_bf16 v[108:111], a[208:211], a[140:143], v[108:111]\n"
    "s_waitcnt lgkmcnt(3)\n"
    "v_exp_f32 v104, v104\n"
    "v_lshlrev_b32 v36, 7, v36\n"
    "v_mfma_f32_16x16x32_bf16 v[176:179], a[16:19], v[112:115], 0\n"
    "v_exp_f32 v105, v105\n"
    "v_lshl_add_u32 v37, v37, 10, v36\n"
    "v_cvt_pk_bf16_f32 v116, v104, v105\n"
    "v_mfma_f32_16x16x32_bf16 v[180:183], a[20:23], v[112:115], 0\n"
    "v_exp_f32 v106, v106\n"
    "v_and_b32 v36, 15, v1\n"
    "v_mfma_f32_16x16x32_bf16 v[184:187], a[24:27], v[112:115], 0\n"
    "v_exp_f32 v107, v107\n"
    "v_lshlrev_b32 v36, 7, v36\n"
    "v_cvt_pk_bf16_f32 v117, v106, v107\n"
    "v_mfma_f32_16x16x32_bf16 v[188:191], a[28:31], v[112:115], 0\n"
    "s_waitcnt lgkmcnt(0)\n"
    "v_exp_f32 v108, v108\n"
    "v_mfma_f32_16x16x32_bf16 v[68:71], v[120:123], v[112:115], 0\n"
    "s_lshr_b32 s69, s65, 1\n"
    "s_add_u32 s69, s69, s55\n"
    "v_mfma_f32_16x16x32_bf16 v[96:99], a[224:227], a[128:131], v[80:83]\n"
    "s_nop 0\n"
    "s_lshr_b32 s88, s57, 2\n"
    "s_add_u32 s88, s88, 0x20000\n"
    "v_mfma_f32_16x16x32_bf16 v[96:99], a[228:231], a[144:147], v[96:99]\n"
    "s_barrier\n"
    "v_exp_f32 v109, v109\n"
    "v_mfma_f32_16x16x32_bf16 v[96:99], a[232:235], a[176:179], v[96:99]\n"
    "ds_read_b128 a[192:195], v4 offset:16384\n"
    "v_cvt_pk_bf16_f32 v118, v108, v109\n"
    "v_mfma_f32_16x16x32_bf16 v[96:99], a[236:239], v[56:59], v[96:99]\n"
    "s_add_u32 m0, s66, 0x4000\n"
    "v_exp_f32 v110, v110\n"
    "v_mfma_f32_16x16x32_bf16 v[100:103], a[252:255], v[56:59], v[80:83]\n"
    "buffer_load_dwordx4 v10, s[8:11], s67 offen offset:0 lds\n"
    "v_mfma_f32_16x16x32_bf16 v[100:103], a[248:251], a[176:179], v[100:103]\n"
    "v_exp_f32 v111, v111\n"
    "v_mfma_f32_16x16x32_bf16 v[100:103], a[244:247], a[144:147], v[100:103]\n"
    "ds_read_b128 a[196:199], v5 offset:16384\n"
    "v_cvt_pk_bf16_f32 v119, v110, v111\n"
    "v_mfma_f32_16x16x32_bf16 v[100:103], a[240:243], a[128:131], v[100:103]\n"
    "v_exp_f32 v96, v96\n"
    "ds_read_b128 a[200:203], v6 offset:16384\n"
    "buffer_load_dwordx2 v[34:35], v21, s[24:27], s51 offen offset:128 sc1\n"
    "v_mfma_f32_16x16x32_bf16 v[224:227], a[0:3], v[116:119], 0\n"
    "v_exp_f32 v97, v97\n"
    "s_nop 0\n"
    "v_cvt_pk_bf16_f32 v112, v96, v97\n"
    "v_mfma_f32_16x16x32_bf16 v[228:231], a[4:7], v[116:119], 0\n"
    "v_exp_f32 v98, v98\n"
    "ds_read_b128 a[204:207], v7 offset:16384\n"
    "v_mfma_f32_16x16x32_bf16 v[232:235], a[8:11], v[116:119], 0\n"
    "v_exp_f32 v99, v99\n"
    "s_nop 0\n"
    "v_cvt_pk_bf16_f32 v113, v98, v99\n"
    "v_mfma_f32_16x16x32_bf16 v[236:239], a[12:15], v[116:119], 0\n"
    "v_exp_f32 v100, v100\n"
    "ds_read_b128 a[208:211], v4 offset:20480\n"
    "v_mfma_f32_16x16x32_bf16 v[104:107], a[224:227], a[132:135], v[84:87]\n"
    "ds_read_b64_tr_b16 a[32:33], v8 offset:8192\n"
    "ds_read_b64_tr_b16 a[34:35], v8 offset:12288\n"
    "v_mfma_f32_16x16x32_bf16 v[104:107], a[228:231], a[148:151], v[104:107]\n"
    "v_exp_f32 v101, v101\n"
    "v_mfma_f32_16x16x32_bf16 v[104:107], a[232:235], a[180:183], v[104:107]\n"
    "ds_read_b64_tr_b16 a[36:37], v9 offset:8192\n"
    "v_cvt_pk_bf16_f32 v114, v100, v101\n"
    "v_mfma_f32_16x16x32_bf16 v[104:107], a[236:239], v[60:63], v[104:107]\n"
    "v_exp_f32 v102, v102\n"
    "v_mfma_f32_16x16x32_bf16 v[108:111], a[252:255], v[60:63], v[84:87]\n"
    "buffer_load_dwordx4 v11, s[8:11], s67 offen offset:1024 lds\n"
    "v_mfma_f32_16x16x32_bf16 v[108:111], a[248:251], a[180:183], v[108:111]\n"
    "v_exp_f32 v103, v103\n"
    "v_mfma_f32_16x16x32_bf16 v[108:111], a[244:247], a[148:151], v[108:111]\n"
    "ds_read_b64_tr_b16 a[38:39], v9 offset:12288\n"
    "v_cvt_pk_bf16_f32 v115, v102, v103\n"
    "v_mfma_f32_16x16x32_bf16 v[108:111], a[240:243], a[132:135], v[108:111]\n"
    "v_exp_f32 v104, v104\n"
    "ds_read_b64_tr_b16 a[40:41], v8 offset:8704\n"
    "ds_read_b64_tr_b16 a[42:43], v8 offset:12800\n"
    "v_mfma_f32_16x16x32_bf16 v[240:243], a[16:19], v[116:119], 0\n"
    "v_exp_f32 v105, v105\n"
    "s_nop 0\n"
    "v_cvt_pk_bf16_f32 v44, v104, v105\n"
    "v_mfma_f32_16x16x32_bf16 v[244:247], a[20:23], v[116:119], 0\n"
    "v_exp_f32 v106, v106\n"
    "ds_read_b64_tr_b16 a[44:45], v9 offset:8704\n"
    "ds_read_b64_tr_b16 a[46:47], v9 offset:12800\n"
    "v_mfma_f32_16x16x32_bf16 v[248:251], a[24:27], v[116:119], 0\n"
    "v_exp_f32 v107, v107\n"
    "s_nop 0\n"
    "v_cvt_pk_bf16_f32 v45, v106, v107\n"
    "v_mfma_f32_16x16x32_bf16 v[252:255], a[28:31], v[116:119], 0\n"
    "v_exp_f32 v108, v108\n"
    "v_mfma_f32_16x16x32_bf16 v[76:79], v[120:123], v[116:119], 0\n"
    "s_add_u32 s68, s69, 0x6000\n"
    "buffer_store_dwordx4 v[22:25], v2, s[24:27], s68 offen offset:0 sc1\n"
    "v_mfma_f32_16x16x32_bf16 v[96:99], a[224:227], a[136:139], v[88:91]\n"
    "ds_read_b64_tr_b16 a[48:49], v8 offset:9216\n"
    "ds_read_b64_tr_b16 a[50:51], v8 offset:13312\n"
    "v_mfma_f32_16x16x32_bf16 v[96:99], a[228:231], a[152:155], v[96:99]\n"
    "v_exp_f32 v109, v109\n"
    "v_mfma_f32_16x16x32_bf16 v[96:99], a[232:235], a[184:187], v[96:99]\n"
    "ds_read_b64_tr_b16 a[52:53], v9 offset:9216\n"
    "v_cvt_pk_bf16_f32 v46, v108, v109\n"
    "v_mfma_f32_16x16x32_bf16 v[96:99], a[236:239], v[124:127], v[96:99]\n"
    "v_exp_f32 v110, v110\n"
    "v_mfma_f32_16x16x32_bf16 v[100:103], a[252:255], v[124:127], v[88:91]\n"
    "buffer_load_dwordx4 v12, s[8:11], s67 offen offset:2048 lds\n"
    "v_mfma_f32_16x16x32_bf16 v[100:103], a[248:251], a[184:187], v[100:103]\n"
    "v_exp_f32 v111, v111\n"
    "v_mfma_f32_16x16x32_bf16 v[100:103], a[244:247], a[152:155], v[100:103]\n"
    "ds_read_b64_tr_b16 a[54:55], v9 offset:13312\n"
    "v_cvt_pk_bf16_f32 v47, v110, v111\n"
    "v_mfma_f32_16x16x32_bf16 v[100:103], a[240:243], a[136:139], v[100:103]\n"
    "s_waitcnt lgkmcnt(4)\n"
    "v_exp_f32 v96, v96\n"
    "v_mfma_f32_16x16x32_bf16 v[128:131], a[0:3], v[40:43], 0\n"
    "ds_read_b64_tr_b16 a[56:57], v8 offset:9728\n"
    "ds_read_b64_tr_b16 a[58:59], v8 offset:13824\n"
    "v_mfma_f32_16x16x32_bf16 v[128:131], a[32:35], v[112:115], v[128:131]\n"
    "v_exp_f32 v97, v97\n"
    "v_mfma_f32_16x16x32_bf16 v[132:135], a[36:39], v[112:115], 0\n"
    "v_cvt_pk_bf16_f32 v116, v96, v97\n"
    "v_mfma_f32_16x16x32_bf16 v[132:135], a[4:7], v[40:43], v[132:135]\n"
    "v_exp_f32 v98, v98\n"
    "v_mfma_f32_16x16x32_bf16 v[136:139], a[8:11], v[40:43], 0\n"
    "ds_read_b64_tr_b16 a[60:61], v9 offset:9728\n"
    "ds_read_b64_tr_b16 a[62:63], v9 offset:13824\n"
    "v_mfma_f32_16x16x32_bf16 v[136:139], a[40:43], v[112:115], v[136:139]\n"
    "v_exp_f32 v99, v99\n"
    "v_mfma_f32_16x16x32_bf16 v[140:143], a[44:47], v[112:115], 0\n"
    "v_cvt_pk_bf16_f32 v117, v98, v99\n"
    "v_mfma_f32_16x16x32_bf16 v[140:143], a[12:15], v[40:43], v[140:143]\n"
    "v_exp_f32 v100, v100\n"
    "v_mfma_f32_16x16x32_bf16 v[64:67], v[120:123], v[40:43], 0\n"
    "v_mfma_f32_16x16x32_bf16 v[104:107], a[224:227], a[140:143], v[92:95]\n"
    "ds_read_b128 a[212:215], v5 offset:20480\n"
    "v_mfma_f32_16x16x32_bf16 v[104:107], a[228:231], a[156:159], v[104:107]\n"
    "v_exp_f32 v101, v101\n"
    "v_mfma_f32_16x16x32_bf16 v[104:107], a[232:235], a[188:191], v[104:107]\n"
    "ds_read_b128 a[216:219], v6 offset:20480\n"
    "v_cvt_pk_bf16_f32 v118, v100, v101\n"
    "v_mfma_f32_16x16x32_bf16 v[104:107], a[236:239], v[28:31], v[104:107]\n"
    "v_exp_f32 v102, v102\n"
    "v_mfma_f32_16x16x32_bf16 v[108:111], a[252:255], v[28:31], v[92:95]\n"
    "buffer_load_dwordx4 v13, s[8:11], s67 offen offset:3072 lds\n"
    "v_mfma_f32_16x16x32_bf16 v[108:111], a[248:251], a[188:191], v[108:111]\n"
    "v_exp_f32 v103, v103\n"
    "v_mfma_f32_16x16x32_bf16 v[108:111], a[244:247], a[156:159], v[108:111]\n"
    "ds_read_b128 a[220:223], v7 offset:20480\n"
    "v_cvt_pk_bf16_f32 v119, v102, v103\n"
    "v_mfma_f32_16x16x32_bf16 v[108:111], a[240:243], a[140:143], v[108:111]\n"
    "s_waitcnt lgkmcnt(3)\n"
    "v_exp_f32 v104, v104\n"
    "v_mfma_f32_16x16x32_bf16 v[144:147], a[48:51], v[112:115], 0\n"
    "buffer_store_dwordx4 v[22:25], v2, s[24:27], s68 offen offset:1024 sc1\n"
    "v_mfma_f32_16x16x32_bf16 v[144:147], a[16:19], v[40:43], v[144:147]\n"
    "v_exp_f32 v105, v105\n"
    "v_mfma_f32_16x16x32_bf16 v[148:151], a[20:23], v[40:43], 0\n"
    "v_cvt_pk_bf16_f32 v52, v104, v105\n"
    "v_mfma_f32_16x16x32_bf16 v[148:151], a[52:55], v[112:115], v[148:151]\n"
    "v_exp_f32 v106, v106\n"
    "v_mfma_f32_16x16x32_bf16 v[152:155], a[56:59], v[112:115], 0\n"
    "v_mfma_f32_16x16x32_bf16 v[152:155], a[24:27], v[40:43], v[152:155]\n"
    "v_exp_f32 v107, v107\n"
    "v_mfma_f32_16x16x32_bf16 v[156:159], a[28:31], v[40:43], 0\n"
    "v_cvt_pk_bf16_f32 v53, v106, v107\n"
    "v_mfma_f32_16x16x32_bf16 v[156:159], a[60:63], v[112:115], v[156:159]\n"
    "s_waitcnt lgkmcnt(0)\n"
    "v_exp_f32 v108, v108\n"
    "v_mfma_f32_16x16x32_bf16 v[64:67], v[120:123], v[112:115], v[64:67]\n"
    "v_mfma_f32_16x16x32_bf16 v[96:99], a[192:195], a[128:131], v[80:83]\n"
    "v_mfma_f32_16x16x32_bf16 v[96:99], a[196:199], a[144:147], v[96:99]\n"
    "v_exp_f32 v109, v109\n"
    "v_mfma_f32_16x16x32_bf16 v[96:99], a[200:203], a[176:179], v[96:99]\n"
    "ds_read_b128 a[224:227], v4 offset:24576\n"
    "v_cvt_pk_bf16_f32 v54, v108, v109\n"
    "v_mfma_f32_16x16x32_bf16 v[96:99], a[204:207], v[56:59], v[96:99]\n"
    "s_add_u32 m0, s66, 0xc000\n"
    "v_exp_f32 v110, v110\n"
    "v_mfma_f32_16x16x32_bf16 v[100:103], a[220:223], v[56:59], v[80:83]\n"
    "buffer_load_dwordx4 v14, s[12:15], s67 offen offset:0 lds\n"
    "v_mfma_f32_16x16x32_bf16 v[100:103], a[216:219], a[176:179], v[100:103]\n"
    "v_exp_f32 v111, v111\n"
    "v_mfma_f32_16x16x32_bf16 v[100:103], a[212:215], a[144:147], v[100:103]\n"
    "ds_read_b128 a[228:231], v5 offset:24576\n"
    "v_cvt_pk_bf16_f32 v55, v110, v111\n"
    "v_mfma_f32_16x16x32_bf16 v[100:103], a[208:211], a[128:131], v[100:103]\n"
    "v_exp_f32 v96, v96\n"
    "v_mfma_f32_16x16x32_bf16 v[192:195], a[0:3], v[48:51], 0\n"
    "ds_read_b128 a[232:235], v6 offset:24576\n"
    "v_mfma_f32_16x16x32_bf16 v[192:195], a[32:35], v[116:119], v[192:195]\n"
    "v_exp_f32 v97, v97\n"
    "v_mfma_f32_16x16x32_bf16 v[196:199], a[36:39], v[116:119], 0\n"
    "v_cvt_pk_bf16_f32 v40, v96, v97\n"
    "v_mfma_f32_16x16x32_bf16 v[196:199], a[4:7], v[48:51], v[196:199]\n"
    "v_exp_f32 v98, v98\n"
    "v_mfma_f32_16x16x32_bf16 v[200:203], a[8:11], v[48:51], 0\n"
    "ds_read_b128 a[236:239], v7 offset:24576\n"
    "v_mfma_f32_16x16x32_bf16 v[200:203], a[40:43], v[116:119], v[200:203]\n"
    "v_exp_f32 v99, v99\n"
    "v_mfma_f32_16x16x32_bf16 v[204:207], a[44:47], v[116:119], 0\n"
    "v_cvt_pk_bf16_f32 v41, v98, v99\n"
    "v_mfma_f32_16x16x32_bf16 v[204:207], a[12:15], v[48:51], v[204:207]\n"
    "v_exp_f32 v100, v100\n"
    "v_mfma_f32_16x16x32_bf16 v[72:75], v[120:123], v[48:51], 0\n"
    "ds_read_b128 a[240:243], v4 offset:28672\n"
    "s_add_u32 s68, s69, 0x8000\n"
    "buffer_store_dwordx4 v[22:25], v2, s[24:27], s68 offen offset:0 sc1\n"
    "v_mfma_f32_16x16x32_bf16 v[104:107], a[192:195], a[132:135], v[84:87]\n"
    "ds_read_b64_tr_b16 a[0:1], v8 offset:16384\n"
    "ds_read_b64_tr_b16 a[2:3], v8 offset:20480\n"
    "v_mfma_f32_16x16x32_bf16 v[104:107], a[196:199], a[148:151], v[104:107]\n"
    "v_exp_f32 v101, v101\n"
    "v_mfma_f32_16x16x32_bf16 v[104:107], a[200:203], a[180:183], v[104:107]\n"
    "ds_read_b64_tr_b16 a[4:5], v9 offset:16384\n"
    "v_cvt_pk_bf16_f32 v42, v100, v101\n"
    "v_mfma_f32_16x16x32_bf16 v[104:107], a[204:207], v[60:63], v[104:107]\n"
    "v_exp_f32 v102, v102\n"
    "v_mfma_f32_16x16x32_bf16 v[108:111], a[220:223], v[60:63], v[84:87]\n"
    "buffer_load_dwordx4 v15, s[12:15], s67 offen offset:1024 lds\n"
    "v_mfma_f32_16x16x32_bf16 v[108:111], a[216:219], a[180:183], v[108:111]\n"
    "v_exp_f32 v103, v103\n"
    "v_mfma_f32_16x16x32_bf16 v[108:111], a[212:215], a[148:151], v[108:111]\n"
    "ds_read_b64_tr_b16 a[6:7], v9 offset:20480\n"
    "v_cvt_pk_bf16_f32 v43, v102, v103\n"
    "v_mfma_f32_16x16x32_bf16 v[108:111], a[208:211], a[132:135], v[108:111]\n"
    "v_exp_f32 v104, v104\n"
    "v_mfma_f32_16x16x32_bf16 v[208:211], a[48:51], v[116:119], 0\n"
    "ds_read_b64_tr_b16 a[8:9], v8 offset:16896\n"
    "ds_read_b64_tr_b16 a[10:11], v8 offset:20992\n"
    "v_mfma_f32_16x16x32_bf16 v[208:211], a[16:19], v[48:51], v[208:211]\n"
    "v_exp_f32 v105, v105\n"
    "v_mfma_f32_16x16x32_bf16 v[212:215], a[20:23], v[48:51], 0\n"
    "v_cvt_pk_bf16_f32 v112, v104, v105\n"
    "v_mfma_f32_16x16x32_bf16 v[212:215], a[52:55], v[116:119], v[212:215]\n"
    "v_exp_f32 v106, v106\n"
    "v_mfma_f32_16x16x32_bf16 v[216:219], a[56:59], v[116:119], 0\n"
    "ds_read_b64_tr_b16 a[12:13], v9 offset:16896\n"
    "ds_read_b64_tr_b16 a[14:15], v9 offset:20992\n"
    "v_mfma_f32_16x16x32_bf16 v[216:219], a[24:27], v[48:51], v[216:219]\n"
    "v_exp_f32 v107, v107\n"
    "v_mfma_f32_16x16x32_bf16 v[220:223], a[28:31], v[48:51], 0\n"
    "v_cvt_pk_bf16_f32 v113, v106, v107\n"
    "v_mfma_f32_16x16x32_bf16 v[220:223], a[60:63], v[116:119], v[220:223]\n"
    "v_exp_f32 v108, v108\n"
    "v_mfma_f32_16x16x32_bf16 v[72:75], v[120:123], v[116:119], v[72:75]\n"
    "v_mfma_f32_16x16x32_bf16 v[96:99], a[192:195], a[136:139], v[88:91]\n"
    "ds_read_b64_tr_b16 a[16:17], v8 offset:17408\n"
    "ds_read_b64_tr_b16 a[18:19], v8 offset:21504\n"
    "v_mfma_f32_16x16x32_bf16 v[96:99], a[196:199], a[152:155], v[96:99]\n"
    "v_exp_f32 v109, v109\n"
    "v_mfma_f32_16x16x32_bf16 v[96:99], a[200:203], a[184:187], v[96:99]\n"
    "ds_read_b64_tr_b16 a[20:21], v9 offset:17408\n"
    "v_cvt_pk_bf16_f32 v114, v108, v109\n"
    "v_mfma_f32_16x16x32_bf16 v[96:99], a[204:207], v[124:127], v[96:99]\n"
    "v_exp_f32 v110, v110\n"
    "v_mfma_f32_16x16x32_bf16 v[100:103], a[220:223], v[124:127], v[88:91]\n"
    "buffer_load_dwordx4 v14, s[12:15], s67 offen offset:2048 lds\n"
    "v_mfma_f32_16x16x32_bf16 v[100:103], a[216:219], a[184:187], v[100:103]\n"
    "v_exp_f32 v111, v111\n"
    "v_mfma_f32_16x16x32_bf16 v[100:103], a[212:215], a[152:155], v[100:103]\n"
    "ds_read_b64_tr_b16 a[22:23], v9 offset:21504\n"
    "v_cvt_pk_bf16_f32 v115, v110, v111\n"
    "v_mfma_f32_16x16x32_bf16 v[100:103], a[208:211], a[136:139], v[100:103]\n"
    "s_waitcnt lgkmcnt(4)\n"
    "v_exp_f32 v96, v96\n"
    "v_mfma_f32_16x16x32_bf16 v[160:163], a[32:35], v[44:47], v[160:163]\n"
    "ds_read_b64_tr_b16 a[24:25], v8 offset:17920\n"
    "ds_read_b64_tr_b16 a[26:27], v8 offset:22016\n"
    "v_mfma_f32_16x16x32_bf16 v[160:163], a[0:3], v[112:115], v[160:163]\n"
    "v_exp_f32 v97, v97\n"
    "v_mfma_f32_16x16x32_bf16 v[164:167], a[4:7], v[112:115], v[164:167]\n"
    "buffer_store_dwordx4 v[22:25], v2, s[24:27], s68 offen offset:1024 sc1\n"
    "v_cvt_pk_bf16_f32 v48, v96, v97\n"
    "v_mfma_f32_16x16x32_bf16 v[164:167], a[36:39], v[44:47], v[164:167]\n"
    "v_exp_f32 v98, v98\n"
    "v_mfma_f32_16x16x32_bf16 v[168:171], a[40:43], v[44:47], v[168:171]\n"
    "ds_read_b64_tr_b16 a[28:29], v9 offset:17920\n"
    "ds_read_b64_tr_b16 a[30:31], v9 offset:22016\n"
    "v_mfma_f32_16x16x32_bf16 v[168:171], a[8:11], v[112:115], v[168:171]\n"
    "v_exp_f32 v99, v99\n"
    "v_mfma_f32_16x16x32_bf16 v[172:175], a[12:15], v[112:115], v[172:175]\n"
    "v_cvt_pk_bf16_f32 v49, v98, v99\n"
    "v_mfma_f32_16x16x32_bf16 v[172:175], a[44:47], v[44:47], v[172:175]\n"
    "v_exp_f32 v100, v100\n"
    "v_mfma_f32_16x16x32_bf16 v[68:71], v[120:123], v[44:47], v[68:71]\n"
    "v_mfma_f32_16x16x32_bf16 v[104:107], a[192:195], a[140:143], v[92:95]\n"
    "ds_read_b128 a[244:247], v5 offset:28672\n"
    "v_mfma_f32_16x16x32_bf16 v[104:107], a[196:199], a[156:159], v[104:107]\n"
    "v_exp_f32 v101, v101\n"
    "v_mfma_f32_16x16x32_bf16 v[104:107], a[200:203], a[188:191], v[104:107]\n"
    "ds_read_b128 a[248:251], v6 offset:28672\n"
    "v_cvt_pk_bf16_f32 v50, v100, v101\n"
    "v_mfma_f32_16x16x32_bf16 v[104:107], a[204:207], v[28:31], v[104:107]\n"
    "v_exp_f32 v102, v102\n"
    "v_mfma_f32_16x16x32_bf16 v[108:111], a[220:223], v[28:31], v[92:95]\n"
    "buffer_load_dwordx4 v15, s[12:15], s67 offen offset:3072 lds\n"
    "v_mfma_f32_16x16x32_bf16 v[108:111], a[216:219], a[188:191], v[108:111]\n"
    "s_add_u32 s67, s67, 0x4000\n"
    "v_exp_f32 v103, v103\n"
    "v_mfma_f32_16x16x32_bf16 v[108:111], a[212:215], a[156:159], v[108:111]\n"
    "ds_read_b128 a[252:255], v7 offset:28672\n"
    "v_cvt_pk_bf16_f32 v51, v102, v103\n"
    "v_mfma_f32_16x16x32_bf16 v[108:111], a[208:211], a[140:143], v[108:111]\n"
    "s_waitcnt lgkmcnt(3)\n"
    "v_exp_f32 v104, v104\n"
    "v_mfma_f32_16x16x32_bf16 v[176:179], a[16:19], v[112:115], v[176:179]\n"
    "v_xor_b32 v4, 0x10000, v4\n"
    "v_xor_b32 v5, 0x10000, v5\n"
    "v_mfma_f32_16x16x32_bf16 v[176:179], a[48:51], v[44:47], v[176:179]\n"
    "s_xor_b32 s66, s66, 0x10000\n"
    "v_exp_f32 v105, v105\n"
    "v_mfma_f32_16x16x32_bf16 v[180:183], a[52:55], v[44:47], v[180:183]\n"
    "v_cvt_pk_bf16_f32 v116, v104, v105\n"
    "v_mfma_f32_16x16x32_bf16 v[180:183], a[20:23], v[112:115], v[180:183]\n"
    "v_exp_f32 v106, v106\n"
    "v_mfma_f32_16x16x32_bf16 v[184:187], a[24:27], v[112:115], v[184:187]\n"
    "v_xor_b32 v6, 0x10000, v6\n"
    "v_xor_b32 v7, 0x10000, v7\n"
    "v_mfma_f32_16x16x32_bf16 v[184:187], a[56:59], v[44:47], v[184:187]\n"
    "v_exp_f32 v107, v107\n"
    "v_mfma_f32_16x16x32_bf16 v[188:191], a[60:63], v[44:47], v[188:191]\n"
    "v_cvt_pk_bf16_f32 v117, v106, v107\n"
    "v_mfma_f32_16x16x32_bf16 v[188:191], a[28:31], v[112:115], v[188:191]\n"
    "s_waitcnt lgkmcnt(0)\n"
    "v_exp_f32 v108, v108\n"
    "v_mfma_f32_16x16x32_bf16 v[68:71], v[120:123], v[112:115], v[68:71]\n"
    "v_mfma_f32_16x16x32_bf16 v[96:99], a[224:227], a[128:131], v[80:83]\n"
    "s_waitcnt vmcnt(13)\n"
    "s_add_u32 s68, s69, 0xa000\n"
    "buffer_store_dwordx4 v[22:25], v2, s[24:27], s68 offen offset:0 sc1\n"
    "v_mfma_f32_16x16x32_bf16 v[96:99], a[228:231], a[144:147], v[96:99]\n"
    "s_barrier\n"
    "v_exp_f32 v109, v109\n"
    "v_mfma_f32_16x16x32_bf16 v[96:99], a[232:235], a[176:179], v[96:99]\n"
    "ds_read_b128 a[192:195], v4 offset:0\n"
    "v_cvt_pk_bf16_f32 v118, v108, v109\n"
    "v_mfma_f32_16x16x32_bf16 v[96:99], a[236:239], v[56:59], v[96:99]\n"
    "s_add_u32 m0, s66, 0\n"
    "v_exp_f32 v110, v110\n"
    "v_mfma_f32_16x16x32_bf16 v[100:103], a[252:255], v[56:59], v[80:83]\n"
    "buffer_load_dwordx4 v10, s[8:11], s67 offen offset:0 lds\n"
    "v_mfma_f32_16x16x32_bf16 v[100:103], a[248:251], a[176:179], v[100:103]\n"
    "v_exp_f32 v111, v111\n"
    "v_mfma_f32_16x16x32_bf16 v[100:103], a[244:247], a[144:147], v[100:103]\n"
    "ds_read_b128 a[196:199], v5 offset:0\n"
    "v_cvt_pk_bf16_f32 v119, v110, v111\n"
    "v_mfma_f32_16x16x32_bf16 v[100:103], a[240:243], a[128:131], v[100:103]\n"
    "v_exp_f32 v96, v96\n"
    "v_mfma_f32_16x16x32_bf16 v[224:227], a[32:35], v[52:55], v[224:227]\n"
    "ds_read_b128 a[200:203], v6 offset:0\n"
    "v_mfma_f32_16x16x32_bf16 v[224:227], a[0:3], v[116:119], v[224:227]\n"
    "v_exp_f32 v97, v97\n"
    "v_mfma_f32_16x16x32_bf16 v[228:231], a[4:7], v[116:119], v[228:231]\n"
    "v_cvt_pk_bf16_f32 v112, v96, v97\n"
    "v_mfma_f32_16x16x32_bf16 v[228:231], a[36:39], v[52:55], v[228:231]\n"
    "v_exp_f32 v98, v98\n"
    "v_mfma_f32_16x16x32_bf16 v[232:235], a[40:43], v[52:55], v[232:235]\n"
    "ds_read_b128 a[204:207], v7 offset:0\n"
    "v_mfma_f32_16x16x32_bf16 v[232:235], a[8:11], v[116:119], v[232:235]\n"
    "v_exp_f32 v99, v99\n"
    "v_mfma_f32_16x16x32_bf16 v[236:239], a[12:15], v[116:119], v[236:239]\n"
    "v_cvt_pk_bf16_f32 v113, v98, v99\n"
    "v_mfma_f32_16x16x32_bf16 v[236:239], a[44:47], v[52:55], v[236:239]\n"
    "v_exp_f32 v100, v100\n"
    "v_mfma_f32_16x16x32_bf16 v[76:79], v[120:123], v[52:55], v[76:79]\n"
    "ds_read_b128 a[208:211], v4 offset:4096\n"
    "v_mfma_f32_16x16x32_bf16 v[104:107], a[224:227], a[132:135], v[84:87]\n"
    "ds_read_b64_tr_b16 a[32:33], v8 offset:24576\n"
    "ds_read_b64_tr_b16 a[34:35], v8 offset:28672\n"
    "v_mfma_f32_16x16x32_bf16 v[104:107], a[228:231], a[148:151], v[104:107]\n"
    "v_exp_f32 v101, v101\n"
    "v_mfma_f32_16x16x32_bf16 v[104:107], a[232:235], a[180:183], v[104:107]\n"
    "ds_read_b64_tr_b16 a[36:37], v9 offset:24576\n"
    "v_cvt_pk_bf16_f32 v114, v100, v101\n"
    "v_mfma_f32_16x16x32_bf16 v[104:107], a[236:239], v[60:63], v[104:107]\n"
    "v_exp_f32 v102, v102\n"
    "v_mfma_f32_16x16x32_bf16 v[108:111], a[252:255], v[60:63], v[84:87]\n"
    "buffer_load_dwordx4 v11, s[8:11], s67 offen offset:1024 lds\n"
    "v_mfma_f32_16x16x32_bf16 v[108:111], a[248:251], a[180:183], v[108:111]\n"
    "v_exp_f32 v103, v103\n"
    "v_mfma_f32_16x16x32_bf16 v[108:111], a[244:247], a[148:151], v[108:111]\n"
    "ds_read_b64_tr_b16 a[38:39], v9 offset:28672\n"
    "v_cvt_pk_bf16_f32 v115, v102, v103\n"
    "v_mfma_f32_16x16x32_bf16 v[108:111], a[240:243], a[132:135], v[108:111]\n"
    "v_exp_f32 v104, v104\n"
    "v_mfma_f32_16x16x32_bf16 v[240:243], a[16:19], v[116:119], v[240:243]\n"
    "ds_read_b64_tr_b16 a[40:41], v8 offset:25088\n"
    "ds_read_b64_tr_b16 a[42:43], v8 offset:29184\n"
    "v_mfma_f32_16x16x32_bf16 v[240:243], a[48:51], v[52:55], v[240:243]\n"
    "v_exp_f32 v105, v105\n"
    "v_mfma_f32_16x16x32_bf16 v[244:247], a[52:55], v[52:55], v[244:247]\n"
    "buffer_store_dwordx4 v[22:25], v2, s[24:27], s68 offen offset:1024 sc1\n"
    "v_cvt_pk_bf16_f32 v44, v104, v105\n"
    "v_mfma_f32_16x16x32_bf16 v[244:247], a[20:23], v[116:119], v[244:247]\n"
    "v_exp_f32 v106, v106\n"
    "v_mfma_f32_16x16x32_bf16 v[248:251], a[24:27], v[116:119], v[248:251]\n"
    "ds_read_b64_tr_b16 a[44:45], v9 offset:25088\n"
    "ds_read_b64_tr_b16 a[46:47], v9 offset:29184\n"
    "v_mfma_f32_16x16x32_bf16 v[248:251], a[56:59], v[52:55], v[248:251]\n"
    "v_exp_f32 v107, v107\n"
    "v_mfma_f32_16x16x32_bf16 v[252:255], a[60:63], v[52:55], v[252:255]\n"
    "v_cvt_pk_bf16_f32 v45, v106, v107\n"
    "v_mfma_f32_16x16x32_bf16 v[252:255], a[28:31], v[116:119], v[252:255]\n"
    "v_exp_f32 v108, v108\n"
    "v_mfma_f32_16x16x32_bf16 v[76:79], v[120:123], v[116:119], v[76:79]\n"
    "v_mfma_f32_16x16x32_bf16 v[96:99], a[224:227], a[136:139], v[88:91]\n"
    "ds_read_b64_tr_b16 a[48:49], v8 offset:25600\n"
    "ds_read_b64_tr_b16 a[50:51], v8 offset:29696\n"
    "v_mfma_f32_16x16x32_bf16 v[96:99], a[228:231], a[152:155], v[96:99]\n"
    "v_exp_f32 v109, v109\n"
    "v_mfma_f32_16x16x32_bf16 v[96:99], a[232:235], a[184:187], v[96:99]\n"
    "ds_read_b64_tr_b16 a[52:53], v9 offset:25600\n"
    "v_cvt_pk_bf16_f32 v46, v108, v109\n"
    "v_mfma_f32_16x16x32_bf16 v[96:99], a[236:239], v[124:127], v[96:99]\n"
    "v_exp_f32 v110, v110\n"
    "v_mfma_f32_16x16x32_bf16 v[100:103], a[252:255], v[124:127], v[88:91]\n"
    "buffer_load_dwordx4 v12, s[8:11], s67 offen offset:2048 lds\n"
    "v_mfma_f32_16x16x32_bf16 v[100:103], a[248:251], a[184:187], v[100:103]\n"
    "v_exp_f32 v111, v111\n"
    "v_mfma_f32_16x16x32_bf16 v[100:103], a[244:247], a[152:155], v[100:103]\n"
    "ds_read_b64_tr_b16 a[54:55], v9 offset:29696\n"
    "v_cvt_pk_bf16_f32 v47, v110, v111\n"
    "v_mfma_f32_16x16x32_bf16 v[100:103], a[240:243], a[136:139], v[100:103]\n"
    "s_waitcnt lgkmcnt(4)\n"
    "v_exp_f32 v96, v96\n"
    "v_mfma_f32_16x16x32_bf16 v[128:131], a[0:3], v[40:43], v[128:131]\n"
    "ds_read_b64_tr_b16 a[56:57], v8 offset:26112\n"
    "ds_read_b64_tr_b16 a[58:59], v8 offset:30208\n"
    "v_mfma_f32_16x16x32_bf16 v[128:131], a[32:35], v[112:115], v[128:131]\n"
    "v_exp_f32 v97, v97\n"
    "v_mfma_f32_16x16x32_bf16 v[132:135], a[36:39], v[112:115], v[132:135]\n"
    "v_cvt_pk_bf16_f32 v116, v96, v97\n"
    "v_mfma_f32_16x16x32_bf16 v[132:135], a[4:7], v[40:43], v[132:135]\n"
    "v_exp_f32 v98, v98\n"
    "v_mfma_f32_16x16x32_bf16 v[136:139], a[8:11], v[40:43], v[136:139]\n"
    "ds_read_b64_tr_b16 a[60:61], v9 offset:26112\n"
    "ds_read_b64_tr_b16 a[62:63], v9 offset:30208\n"
    "v_mfma_f32_16x16x32_bf16 v[136:139], a[40:43], v[112:115], v[136:139]\n"
    "v_exp_f32 v99, v99\n"
    "v_mfma_f32_16x16x32_bf16 v[140:143], a[44:47], v[112:115], v[140:143]\n"
    "v_cvt_pk_bf16_f32 v117, v98, v99\n"
    "v_mfma_f32_16x16x32_bf16 v[140:143], a[12:15], v[40:43], v[140:143]\n"
    "v_exp_f32 v100, v100\n"
    "v_mfma_f32_16x16x32_bf16 v[64:67], v[120:123], v[40:43], v[64:67]\n"
    "v_mfma_f32_16x16x32_bf16 v[104:107], a[224:227], a[140:143], v[92:95]\n"
    "ds_read_b128 a[212:215], v5 offset:4096\n"
    "v_mfma_f32_16x16x32_bf16 v[104:107], a[228:231], a[156:159], v[104:107]\n"
    "v_exp_f32 v101, v101\n"
    "v_mfma_f32_16x16x32_bf16 v[104:107], a[232:235], a[188:191], v[104:107]\n"
    "ds_read_b128 a[216:219], v6 offset:4096\n"
    "v_cvt_pk_bf16_f32 v118, v100, v101\n"
    "v_mfma_f32_16x16x32_bf16 v[104:107], a[236:239], v[28:31], v[104:107]\n"
    "v_exp_f32 v102, v102\n"
    "v_mfma_f32_16x16x32_bf16 v[108:111], a[252:255], v[28:31], v[92:95]\n"
    "buffer_load_dwordx4 v13, s[8:11], s67 offen offset:3072 lds\n"
    "v_mfma_f32_16x16x32_bf16 v[108:111], a[248:251], a[188:191], v[108:111]\n"
    "v_exp_f32 v103, v103\n"
    "v_mfma_f32_16x16x32_bf16 v[108:111], a[244:247], a[156:159], v[108:111]\n"
    "ds_read_b128 a[220:223], v7 offset:4096\n"
    "v_cvt_pk_bf16_f32 v119, v102, v103\n"
    "v_mfma_f32_16x16x32_bf16 v[108:111], a[240:243], a[140:143], v[108:111]\n"
    "s_waitcnt lgkmcnt(3)\n"
    "v_exp_f32 v104, v104\n"
    "v_mfma_f32_16x16x32_bf16 v[144:147], a[48:51], v[112:115], v[144:147]\n"
    "v_xor_b32 v8, 0x10000, v8\n"
    "v_xor_b32 v9, 0x10000, v9\n"
    "v_mfma_f32_16x16x32_bf16 v[144:147], a[16:19], v[40:43], v[144:147]\n"
    "v_exp_f32 v105, v105\n"
    "v_mfma_f32_16x16x32_bf16 v[148:151], a[20:23], v[40:43], v[148:151]\n"
    "v_cvt_pk_bf16_f32 v52, v104, v105\n"
    "v_mfma_f32_16x16x32_bf16 v[148:151], a[52:55], v[112:115], v[148:151]\n"
    "v_exp_f32 v106, v106\n"
    "v_mfma_f32_16x16x32_bf16 v[152:155], a[56:59], v[112:115], v[152:155]\n"
    "v_mfma_f32_16x16x32_bf16 v[152:155], a[24:27], v[40:43], v[152:155]\n"
    "v_exp_f32 v107, v107\n"
    "v_mfma_f32_16x16x32_bf16 v[156:159], a[28:31], v[40:43], v[156:159]\n"
    "v_cvt_pk_bf16_f32 v53, v106, v107\n"
    "v_mfma_f32_16x16x32_bf16 v[156:159], a[60:63], v[112:115], v[156:159]\n"
    "s_waitcnt lgkmcnt(0)\n"
    "v_exp_f32 v108, v108\n"
    "v_mfma_f32_16x16x32_bf16 v[64:67], v[120:123], v[112:115], v[64:67]\n"
    ".p2align 6\n"
    "L_loop%=:\n"
    "v_mfma_f32_16x16x32_bf16 v[96:99], a[192:195], a[128:131], v[80:83]\n"
    "v_mfma_f32_16x16x32_bf16 v[96:99], a[196:199], a[144:147], v[96:99]\n"
    "v_exp_f32 v109, v109\n"
    "v_mfma_f32_16x16x32_bf16 v[96:99], a[200:203], a[176:179], v[96:99]\n"
    "ds_read_b128 a[224:227], v4 offset:8192\n"
    "v_cvt_pk_bf16_f32 v54, v108, v109\n"
    "v_mfma_f32_16x16x32_bf16 v[96:99], a[204:207], v[56:59], v[96:99]\n"
    "s_add_u32 m0, s66, 0x8000\n"
    "v_exp_f32 v110, v110\n"
    "v_mfma_f32_16x16x32_bf16 v[100:103], a[220:223], v[56:59], v[80:83]\n"
    "buffer_load_dwordx4 v14, s[12:15], s67 offen offset:0 lds\n"
    "v_mfma_f32_16x16x32_bf16 v[100:103], a[216:219], a[176:179], v[100:103]\n"
    "v_exp_f32 v111, v111\n"
    "v_mfma_f32_16x16x32_bf16 v[100:103], a[212:215], a[144:147], v[100:103]\n"
    "ds_read_b128 a[228:231], v5 offset:8192\n"
    "v_cvt_pk_bf16_f32 v55, v110, v111\n"
    "v_mfma_f32_16x16x32_bf16 v[100:103], a[208:211], a[128:131], v[100:103]\n"
    "v_exp_f32 v96, v96\n"
    "v_mfma_f32_16x16x32_bf16 v[192:195], a[0:3], v[48:51], v[192:195]\n"
    "ds_read_b128 a[232:235], v6 offset:8192\n"
    "v_mfma_f32_16x16x32_bf16 v[192:195], a[32:35], v[116:119], v[192:195]\n"
    "v_exp_f32 v97, v97\n"
    "v_mfma_f32_16x16x32_bf16 v[196:199], a[36:39], v[116:119], v[196:199]\n"
    "v_cvt_pk_bf16_f32 v40, v96, v97\n"
    "v_mfma_f32_16x16x32_bf16 v[196:199], a[4:7], v[48:51], v[196:199]\n"
    "v_exp_f32 v98, v98\n"
    "v_mfma_f32_16x16x32_bf16 v[200:203], a[8:11], v[48:51], v[200:203]\n"
    "ds_read_b128 a[236:239], v7 offset:8192\n"
    "v_mfma_f32_16x16x32_bf16 v[200:203], a[40:43], v[116:119], v[200:203]\n"
    "v_exp_f32 v99, v99\n"
    "v_mfma_f32_16x16x32_bf16 v[204:207], a[44:47], v[116:119], v[204:207]\n"
    "v_cvt_pk_bf16_f32 v41, v98, v99\n"
    "v_mfma_f32_16x16x32_bf16 v[204:207], a[12:15], v[48:51], v[204:207]\n"
    "v_exp_f32 v100, v100\n"
    "v_mfma_f32_16x16x32_bf16 v[72:75], v[120:123], v[48:51], v[72:75]\n"
    "ds_read_b128 a[240:243], v4 offset:12288\n"
    "v_mfma_f32_16x16x32_bf16 v[104:107], a[192:195], a[132:135], v[84:87]\n"
    "ds_read_b64_tr_b16 a[0:1], v8 offset:0\n"
    "ds_read_b64_tr_b16 a[2:3], v8 offset:4096\n"
    "v_mfma_f32_16x16x32_bf16 v[104:107], a[196:199], a[148:151], v[104:107]\n"
    "v_exp_f32 v101, v101\n"
    "v_mfma_f32_16x16x32_bf16 v[104:107], a[200:203], a[180:183], v[104:107]\n"
    "ds_read_b64_tr_b16 a[4:5], v9 offset:0\n"
    "v_cvt_pk_bf16_f32 v42, v100, v101\n"
    "v_mfma_f32_16x16x32_bf16 v[104:107], a[204:207], v[60:63], v[104:107]\n"
    "v_exp_f32 v102, v102\n"
    "v_mfma_f32_16x16x32_bf16 v[108:111], a[220:223], v[60:63], v[84:87]\n"
    "buffer_load_dwordx4 v15, s[12:15], s67 offen offset:1024 lds\n"
    "v_mfma_f32_16x16x32_bf16 v[108:111], a[216:219], a[180:183], v[108:111]\n"
    "v_exp_f32 v103, v103\n"
    "v_mfma_f32_16x16x32_bf16 v[108:111], a[212:215], a[148:151], v[108:111]\n"
    "ds_read_b64_tr_b16 a[6:7], v9 offset:4096\n"
    "v_cvt_pk_bf16_f32 v43, v102, v103\n"
    "v_mfma_f32_16x16x32_bf16 v[108:111], a[208:211], a[132:135], v[108:111]\n"
    "v_exp_f32 v104, v104\n"
    "v_mfma_f32_16x16x32_bf16 v[208:211], a[48:51], v[116:119], v[208:211]\n"
    "ds_read_b64_tr_b16 a[8:9], v8 offset:512\n"
    "ds_read_b64_tr_b16 a[10:11], v8 offset:4608\n"
    "v_mfma_f32_16x16x32_bf16 v[208:211], a[16:19], v[48:51], v[208:211]\n"
    "s_and_b32 s87, s67, 0x18000\n"
    "v_exp_f32 v105, v105\n"
    "v_mfma_f32_16x16x32_bf16 v[212:215], a[20:23], v[48:51], v[212:215]\n"
    "v_cvt_pk_bf16_f32 v112, v104, v105\n"
    "v_mfma_f32_16x16x32_bf16 v[212:215], a[52:55], v[116:119], v[212:215]\n"
    "s_lshr_b32 s87, s87, 2\n"
    "v_exp_f32 v106, v106\n"
    "v_mfma_f32_16x16x32_bf16 v[216:219], a[56:59], v[116:119], v[216:219]\n"
    "ds_read_b64_tr_b16 a[12:13], v9 offset:512\n"
    "ds_read_b64_tr_b16 a[14:15], v9 offset:4608\n"
    "v_mfma_f32_16x16x32_bf16 v[216:219], a[24:27], v[48:51], v[216:219]\n"
    "s_add_u32 s87, s87, s57\n"
    "v_exp_f32 v107, v107\n"
    "v_mfma_f32_16x16x32_bf16 v[220:223], a[28:31], v[48:51], v[220:223]\n"
    "v_cvt_pk_bf16_f32 v113, v106, v107\n"
    "v_mfma_f32_16x16x32_bf16 v[220:223], a[60:63], v[116:119], v[220:223]\n"
    "v_exp_f32 v108, v108\n"
    "v_mfma_f32_16x16x32_bf16 v[72:75], v[120:123], v[116:119], v[72:75]\n"
    "v_mfma_f32_16x16x32_bf16 v[96:99], a[192:195], a[136:139], v[88:91]\n"
    "ds_read_b64_tr_b16 a[16:17], v8 offset:1024\n"
    "ds_read_b64_tr_b16 a[18:19], v8 offset:5120\n"
    "v_mfma_f32_16x16x32_bf16 v[96:99], a[196:199], a[152:155], v[96:99]\n"
    "v_exp_f32 v109, v109\n"
    "v_mfma_f32_16x16x32_bf16 v[96:99], a[200:203], a[184:187], v[96:99]\n"
    "ds_read_b64_tr_b16 a[20:21], v9 offset:1024\n"
    "v_cvt_pk_bf16_f32 v114, v108, v109\n"
    "v_mfma_f32_16x16x32_bf16 v[96:99], a[204:207], v[124:127], v[96:99]\n"
    "v_exp_f32 v110, v110\n"
    "v_mfma_f32_16x16x32_bf16 v[100:103], a[220:223], v[124:127], v[88:91]\n"
    "buffer_load_dwordx4 v14, s[12:15], s67 offen offset:2048 lds\n"
    "v_mfma_f32_16x16x32_bf16 v[100:103], a[216:219], a[184:187], v[100:103]\n"
    "v_exp_f32 v111, v111\n"
    "v_mfma_f32_16x16x32_bf16 v[100:103], a[212:215], a[152:155], v[100:103]\n"
    "ds_read_b64_tr_b16 a[22:23], v9 offset:5120\n"
    "v_cvt_pk_bf16_f32 v115, v110, v111\n"
    "v_mfma_f32_16x16x32_bf16 v[100:103], a[208:211], a[136:139], v[100:103]\n"
    "s_waitcnt lgkmcnt(4)\n"
    "v_exp_f32 v96, v96\n"
    "v_mfma_f32_16x16x32_bf16 v[160:163], a[32:35], v[44:47], v[160:163]\n"
    "ds_read_b64_tr_b16 a[24:25], v8 offset:1536\n"
    "ds_read_b64_tr_b16 a[26:27], v8 offset:5632\n"
    "v_mfma_f32_16x16x32_bf16 v[160:163], a[0:3], v[112:115], v[160:163]\n"
    "v_exp_f32 v97, v97\n"
    "v_mfma_f32_16x16x32_bf16 v[164:167], a[4:7], v[112:115], v[164:167]\n"
    "v_cvt_pk_bf16_f32 v48, v96, v97\n"
    "v_mfma_f32_16x16x32_bf16 v[164:167], a[36:39], v[44:47], v[164:167]\n"
    "v_exp_f32 v98, v98\n"
    "v_mfma_f32_16x16x32_bf16 v[168:171], a[40:43], v[44:47], v[168:171]\n"
    "ds_read_b64_tr_b16 a[28:29], v9 offset:1536\n"
    "ds_read_b64_tr_b16 a[30:31], v9 offset:5632\n"
    "v_mfma_f32_16x16x32_bf16 v[168:171], a[8:11], v[112:115], v[168:171]\n"
    "v_exp_f32 v99, v99\n"
    "v_mfma_f32_16x16x32_bf16 v[172:175], a[12:15], v[112:115], v[172:175]\n"
    "v_cvt_pk_bf16_f32 v49, v98, v99\n"
    "v_mfma_f32_16x16x32_bf16 v[172:175], a[44:47], v[44:47], v[172:175]\n"
    "v_exp_f32 v100, v100\n"
    "v_mfma_f32_16x16x32_bf16 v[68:71], v[120:123], v[44:47], v[68:71]\n"
    "v_mfma_f32_16x16x32_bf16 v[104:107], a[192:195], a[140:143], v[92:95]\n"
    "ds_read_b128 a[244:247], v5 offset:12288\n"
    "v_mfma_f32_16x16x32_bf16 v[104:107], a[196:199], a[156:159], v[104:107]\n"
    "v_exp_f32 v101, v101\n"
    "v_mfma_f32_16x16x32_bf16 v[104:107], a[200:203], a[188:191], v[104:107]\n"
    "ds_read_b128 a[248:251], v6 offset:12288\n"
    "v_cvt_pk_bf16_f32 v50, v100, v101\n"
    "v_mfma_f32_16x16x32_bf16 v[104:107], a[204:207], v[28:31], v[104:107]\n"
    "v_exp_f32 v102, v102\n"
    "v_mfma_f32_16x16x32_bf16 v[108:111], a[220:223], v[28:31], v[92:95]\n"
    "buffer_load_dwordx4 v15, s[12:15], s67 offen offset:3072 lds\n"
    "v_mfma_f32_16x16x32_bf16 v[108:111], a[216:219], a[188:191], v[108:111]\n"
    "s_add_u32 s67, s67, 0x4000\n"
    "v_exp_f32 v103, v103\n"
    "v_mfma_f32_16x16x32_bf16 v[108:111], a[212:215], a[156:159], v[108:111]\n"
    "ds_read_b128 a[252:255], v7 offset:12288\n"
    "v_cvt_pk_bf16_f32 v51, v102, v103\n"
    "v_mfma_f32_16x16x32_bf16 v[108:111], a[208:211], a[140:143], v[108:111]\n"
    "s_waitcnt lgkmcnt(3)\n"
    "v_exp_f32 v104, v104\n"
    "v_mfma_f32_16x16x32_bf16 v[176:179], a[16:19], v[112:115], v[176:179]\n"
    "v_mfma_f32_16x16x32_bf16 v[176:179], a[48:51], v[44:47], v[176:179]\n"
    "v_exp_f32 v105, v105\n"
    "v_mfma_f32_16x16x32_bf16 v[180:183], a[52:55], v[44:47], v[180:183]\n"
    "v_cvt_pk_bf16_f32 v116, v104, v105\n"
    "v_mfma_f32_16x16x32_bf16 v[180:183], a[20:23], v[112:115], v[180:183]\n"
    "v_exp_f32 v106, v106\n"
    "v_mfma_f32_16x16x32_bf16 v[184:187], a[24:27], v[112:115], v[184:187]\n"
    "buffer_load_dword v34, v37, s[32:35], s87 offen offset:1024\n"
    "v_mfma_f32_16x16x32_bf16 v[184:187], a[56:59], v[44:47], v[184:187]\n"
    "v_exp_f32 v107, v107\n"
    "v_mfma_f32_16x16x32_bf16 v[188:191], a[60:63], v[44:47], v[188:191]\n"
    "v_cvt_pk_bf16_f32 v117, v106, v107\n"
    "v_mfma_f32_16x16x32_bf16 v[188:191], a[28:31], v[112:115], v[188:191]\n"
    "s_waitcnt lgkmcnt(0)\n"
    "v_exp_f32 v108, v108\n"
    "v_mfma_f32_16x16x32_bf16 v[68:71], v[120:123], v[112:115], v[68:71]\n"
    "v_mfma_f32_16x16x32_bf16 v[96:99], a[224:227], a[128:131], v[80:83]\n"
    "s_waitcnt vmcnt(9)\n"
    "v_mfma_f32_16x16x32_bf16 v[96:99], a[228:231], a[144:147], v[96:99]\n"
    "s_barrier\n"
    "v_exp_f32 v109, v109\n"
    "v_mfma_f32_16x16x32_bf16 v[96:99], a[232:235], a[176:179], v[96:99]\n"
    "ds_read_b128 a[192:195], v4 offset:16384\n"
    "v_cvt_pk_bf16_f32 v118, v108, v109\n"
    "v_mfma_f32_16x16x32_bf16 v[96:99], a[236:239], v[56:59], v[96:99]\n"
    "s_add_u32 m0, s66, 0x4000\n"
    "v_exp_f32 v110, v110\n"
    "v_mfma_f32_16x16x32_bf16 v[100:103], a[252:255], v[56:59], v[80:83]\n"
    "buffer_load_dwordx4 v10, s[8:11], s67 offen offset:0 lds\n"
    "v_mfma_f32_16x16x32_bf16 v[100:103], a[248:251], a[176:179], v[100:103]\n"
    "v_exp_f32 v111, v111\n"
    "v_mfma_f32_16x16x32_bf16 v[100:103], a[244:247], a[144:147], v[100:103]\n"
    "ds_read_b128 a[196:199], v5 offset:16384\n"
    "v_cvt_pk_bf16_f32 v119, v110, v111\n"
    "v_mfma_f32_16x16x32_bf16 v[100:103], a[240:243], a[128:131], v[100:103]\n"
    "v_exp_f32 v96, v96\n"
    "v_mfma_f32_16x16x32_bf16 v[224:227], a[32:35], v[52:55], v[224:227]\n"
    "ds_read_b128 a[200:203], v6 offset:16384\n"
    "v_mfma_f32_16x16x32_bf16 v[224:227], a[0:3], v[116:119], v[224:227]\n"
    "v_exp_f32 v97, v97\n"
    "v_mfma_f32_16x16x32_bf16 v[228:231], a[4:7], v[116:119], v[228:231]\n"
    "v_cvt_pk_bf16_f32 v112, v96, v97\n"
    "v_mfma_f32_16x16x32_bf16 v[228:231], a[36:39], v[52:55], v[228:231]\n"
    "v_exp_f32 v98, v98\n"
    "v_mfma_f32_16x16x32_bf16 v[232:235], a[40:43], v[52:55], v[232:235]\n"
    "ds_read_b128 a[204:207], v7 offset:16384\n"
    "v_mfma_f32_16x16x32_bf16 v[232:235], a[8:11], v[116:119], v[232:235]\n"
    "v_exp_f32 v99, v99\n"
    "v_mfma_f32_16x16x32_bf16 v[236:239], a[12:15], v[116:119], v[236:239]\n"
    "v_cvt_pk_bf16_f32 v113, v98, v99\n"
    "v_mfma_f32_16x16x32_bf16 v[236:239], a[44:47], v[52:55], v[236:239]\n"
    "v_exp_f32 v100, v100\n"
    "v_mfma_f32_16x16x32_bf16 v[76:79], v[120:123], v[52:55], v[76:79]\n"
    "ds_read_b128 a[208:211], v4 offset:20480\n"
    "v_mfma_f32_16x16x32_bf16 v[104:107], a[224:227], a[132:135], v[84:87]\n"
    "ds_read_b64_tr_b16 a[32:33], v8 offset:8192\n"
    "ds_read_b64_tr_b16 a[34:35], v8 offset:12288\n"
    "v_mfma_f32_16x16x32_bf16 v[104:107], a[228:231], a[148:151], v[104:107]\n"
    "v_exp_f32 v101, v101\n"
    "v_mfma_f32_16x16x32_bf16 v[104:107], a[232:235], a[180:183], v[104:107]\n"
    "ds_read_b64_tr_b16 a[36:37], v9 offset:8192\n"
    "v_cvt_pk_bf16_f32 v114, v100, v101\n"
    "v_mfma_f32_16x16x32_bf16 v[104:107], a[236:239], v[60:63], v[104:107]\n"
    "v_exp_f32 v102, v102\n"
    "v_mfma_f32_16x16x32_bf16 v[108:111], a[252:255], v[60:63], v[84:87]\n"
    "buffer_load_dwordx4 v11, s[8:11], s67 offen offset:1024 lds\n"
    "v_mfma_f32_16x16x32_bf16 v[108:111], a[248:251], a[180:183], v[108:111]\n"
    "v_exp_f32 v103, v103\n"
    "v_mfma_f32_16x16x32_bf16 v[108:111], a[244:247], a[148:151], v[108:111]\n"
    "ds_read_b64_tr_b16 a[38:39], v9 offset:12288\n"
    "v_cvt_pk_bf16_f32 v115, v102, v103\n"
    "v_mfma_f32_16x16x32_bf16 v[108:111], a[240:243], a[132:135], v[108:111]\n"
    "v_exp_f32 v104, v104\n"
    "v_mfma_f32_16x16x32_bf16 v[240:243], a[16:19], v[116:119], v[240:243]\n"
    "ds_read_b64_tr_b16 a[40:41], v8 offset:8704\n"
    "ds_read_b64_tr_b16 a[42:43], v8 offset:12800\n"
    "v_mfma_f32_16x16x32_bf16 v[240:243], a[48:51], v[52:55], v[240:243]\n"
    "v_exp_f32 v105, v105\n"
    "v_mfma_f32_16x16x32_bf16 v[244:247], a[52:55], v[52:55], v[244:247]\n"
    "v_cvt_pk_bf16_f32 v44, v104, v105\n"
    "v_mfma_f32_16x16x32_bf16 v[244:247], a[20:23], v[116:119], v[244:247]\n"
    "v_exp_f32 v106, v106\n"
    "v_mfma_f32_16x16x32_bf16 v[248:251], a[24:27], v[116:119], v[248:251]\n"
    "ds_read_b64_tr_b16 a[44:45], v9 offset:8704\n"
    "ds_read_b64_tr_b16 a[46:47], v9 offset:12800\n"
    "v_mfma_f32_16x16x32_bf16 v[248:251], a[56:59], v[52:55], v[248:251]\n"
    "v_exp_f32 v107, v107\n"
    "v_mfma_f32_16x16x32_bf16 v[252:255], a[60:63], v[52:55], v[252:255]\n"
    "v_cvt_pk_bf16_f32 v45, v106, v107\n"
    "v_mfma_f32_16x16x32_bf16 v[252:255], a[28:31], v[116:119], v[252:255]\n"
    "v_exp_f32 v108, v108\n"
    "v_mfma_f32_16x16x32_bf16 v[76:79], v[120:123], v[116:119], v[76:79]\n"
    "buffer_load_dwordx2 v[26:27], v21, s[24:27], s51 offen offset:0 sc1\n"
    "v_mfma_f32_16x16x32_bf16 v[96:99], a[224:227], a[136:139], v[88:91]\n"
    "ds_read_b64_tr_b16 a[48:49], v8 offset:9216\n"
    "ds_read_b64_tr_b16 a[50:51], v8 offset:13312\n"
    "v_mfma_f32_16x16x32_bf16 v[96:99], a[228:231], a[152:155], v[96:99]\n"
    "v_exp_f32 v109, v109\n"
    "v_mfma_f32_16x16x32_bf16 v[96:99], a[232:235], a[184:187], v[96:99]\n"
    "ds_read_b64_tr_b16 a[52:53], v9 offset:9216\n"
    "v_cvt_pk_bf16_f32 v46, v108, v109\n"
    "v_mfma_f32_16x16x32_bf16 v[96:99], a[236:239], v[124:127], v[96:99]\n"
    "v_exp_f32 v110, v110\n"
    "v_mfma_f32_16x16x32_bf16 v[100:103], a[252:255], v[124:127], v[88:91]\n"
    "buffer_load_dwordx4 v12, s[8:11], s67 offen offset:2048 lds\n"
    "v_mfma_f32_16x16x32_bf16 v[100:103], a[248:251], a[184:187], v[100:103]\n"
    "v_exp_f32 v111, v111\n"
    "v_mfma_f32_16x16x32_bf16 v[100:103], a[244:247], a[152:155], v[100:103]\n"
    "ds_read_b64_tr_b16 a[54:55], v9 offset:13312\n"
    "v_cvt_pk_bf16_f32 v47, v110, v111\n"
    "v_mfma_f32_16x16x32_bf16 v[100:103], a[240:243], a[136:139], v[100:103]\n"
    "s_waitcnt lgkmcnt(4)\n"
    "v_exp_f32 v96, v96\n"
    "v_mfma_f32_16x16x32_bf16 v[128:131], a[0:3], v[40:43], v[128:131]\n"
    "ds_read_b64_tr_b16 a[56:57], v8 offset:9728\n"
    "ds_read_b64_tr_b16 a[58:59], v8 offset:13824\n"
    "v_mfma_f32_16x16x32_bf16 v[128:131], a[32:35], v[112:115], v[128:131]\n"
    "v_exp_f32 v97, v97\n"
    "v_mfma_f32_16x16x32_bf16 v[132:135], a[36:39], v[112:115], v[132:135]\n"
    "v_cvt_pk_bf16_f32 v116, v96, v97\n"
    "v_mfma_f32_16x16x32_bf16 v[132:135], a[4:7], v[40:43], v[132:135]\n"
    "v_exp_f32 v98, v98\n"
    "v_mfma_f32_16x16x32_bf16 v[136:139], a[8:11], v[40:43], v[136:139]\n"
    "ds_read_b64_tr_b16 a[60:61], v9 offset:9728\n"
    "ds_read_b64_tr_b16 a[62:63], v9 offset:13824\n"
    "v_mfma_f32_16x16x32_bf16 v[136:139], a[40:43], v[112:115], v[136:139]\n"
    "v_exp_f32 v99, v99\n"
    "v_mfma_f32_16x16x32_bf16 v[140:143], a[44:47], v[112:115], v[140:143]\n"
    "v_cvt_pk_bf16_f32 v117, v98, v99\n"
    "v_mfma_f32_16x16x32_bf16 v[140:143], a[12:15], v[40:43], v[140:143]\n"
    "v_exp_f32 v100, v100\n"
    "v_mfma_f32_16x16x32_bf16 v[64:67], v[120:123], v[40:43], v[64:67]\n"
    "v_mfma_f32_16x16x32_bf16 v[104:107], a[224:227], a[140:143], v[92:95]\n"
    "ds_read_b128 a[212:215], v5 offset:20480\n"
    "v_mfma_f32_16x16x32_bf16 v[104:107], a[228:231], a[156:159], v[104:107]\n"
    "v_exp_f32 v101, v101\n"
    "v_mfma_f32_16x16x32_bf16 v[104:107], a[232:235], a[188:191], v[104:107]\n"
    "ds_read_b128 a[216:219], v6 offset:20480\n"
    "v_cvt_pk_bf16_f32 v118, v100, v101\n"
    "v_mfma_f32_16x16x32_bf16 v[104:107], a[236:239], v[28:31], v[104:107]\n"
    "v_exp_f32 v102, v102\n"
    "v_mfma_f32_16x16x32_bf16 v[108:111], a[252:255], v[28:31], v[92:95]\n"
    "buffer_load_dwordx4 v13, s[8:11], s67 offen offset:3072 lds\n"
    "v_mfma_f32_16x16x32_bf16 v[108:111], a[248:251], a[188:191], v[108:111]\n"
    "v_exp_f32 v103, v103\n"
    "v_mfma_f32_16x16x32_bf16 v[108:111], a[244:247], a[156:159], v[108:111]\n"
    "ds_read_b128 a[220:223], v7 offset:20480\n"
    "v_cvt_pk_bf16_f32 v119, v102, v103\n"
    "v_mfma_f32_16x16x32_bf16 v[108:111], a[240:243], a[140:143], v[108:111]\n"
    "s_waitcnt lgkmcnt(3)\n"
    "v_exp_f32 v104, v104\n"
    "v_mfma_f32_16x16x32_bf16 v[144:147], a[48:51], v[112:115], v[144:147]\n"
    "v_mfma_f32_16x16x32_bf16 v[144:147], a[16:19], v[40:43], v[144:147]\n"
    "v_exp_f32 v105, v105\n"
    "v_mfma_f32_16x16x32_bf16 v[148:151], a[20:23], v[40:43], v[148:151]\n"
    "v_cvt_pk_bf16_f32 v52, v104, v105\n"
    "v_mfma_f32_16x16x32_bf16 v[148:151], a[52:55], v[112:115], v[148:151]\n"
    "v_exp_f32 v106, v106\n"
    "v_mfma_f32_16x16x32_bf16 v[152:155], a[56:59], v[112:115], v[152:155]\n"
    "v_mfma_f32_16x16x32_bf16 v[152:155], a[24:27], v[40:43], v[152:155]\n"
    "v_exp_f32 v107, v107\n"
    "v_mfma_f32_16x16x32_bf16 v[156:159], a[28:31], v[40:43], v[156:159]\n"
    "v_cvt_pk_bf16_f32 v53, v106, v107\n"
    "v_mfma_f32_16x16x32_bf16 v[156:159], a[60:63], v[112:115], v[156:159]\n"
    "s_waitcnt lgkmcnt(0)\n"
    "v_exp_f32 v108, v108\n"
    "v_mfma_f32_16x16x32_bf16 v[64:67], v[120:123], v[112:115], v[64:67]\n"
    "v_mfma_f32_16x16x32_bf16 v[96:99], a[192:195], a[128:131], v[80:83]\n"
    "v_mfma_f32_16x16x32_bf16 v[96:99], a[196:199], a[144:147], v[96:99]\n"
    "v_exp_f32 v109, v109\n"
    "v_mfma_f32_16x16x32_bf16 v[96:99], a[200:203], a[176:179], v[96:99]\n"
    "ds_read_b128 a[224:227], v4 offset:24576\n"
    "v_cvt_pk_bf16_f32 v54, v108, v109\n"
    "v_mfma_f32_16x16x32_bf16 v[96:99], a[204:207], v[56:59], v[96:99]\n"
    "s_add_u32 m0, s66, 0xc000\n"
    "v_exp_f32 v110, v110\n"
    "v_mfma_f32_16x16x32_bf16 v[100:103], a[220:223], v[56:59], v[80:83]\n"
    "buffer_load_dwordx4 v14, s[12:15], s67 offen offset:0 lds\n"
    "v_mfma_f32_16x16x32_bf16 v[100:103], a[216:219], a[176:179], v[100:103]\n"
    "v_exp_f32 v111, v111\n"
    "v_mfma_f32_16x16x32_bf16 v[100:103], a[212:215], a[144:147], v[100:103]\n"
    "ds_read_b128 a[228:231], v5 offset:24576\n"
    "v_cvt_pk_bf16_f32 v55, v110, v111\n"
    "v_mfma_f32_16x16x32_bf16 v[100:103], a[208:211], a[128:131], v[100:103]\n"
    "v_exp_f32 v96, v96\n"
    "v_mfma_f32_16x16x32_bf16 v[192:195], a[0:3], v[48:51], v[192:195]\n"
    "ds_read_b128 a[232:235], v6 offset:24576\n"
    "v_mfma_f32_16x16x32_bf16 v[192:195], a[32:35], v[116:119], v[192:195]\n"
    "v_exp_f32 v97, v97\n"
    "v_mfma_f32_16x16x32_bf16 v[196:199], a[36:39], v[116:119], v[196:199]\n"
    "v_cvt_pk_bf16_f32 v40, v96, v97\n"
    "v_mfma_f32_16x16x32_bf16 v[196:199], a[4:7], v[48:51], v[196:199]\n"
    "v_exp_f32 v98, v98\n"
    "v_mfma_f32_16x16x32_bf16 v[200:203], a[8:11], v[48:51], v[200:203]\n"
    "ds_read_b128 a[236:239], v7 offset:24576\n"
    "v_mfma_f32_16x16x32_bf16 v[200:203], a[40:43], v[116:119], v[200:203]\n"
    "v_exp_f32 v99, v99\n"
    "v_mfma_f32_16x16x32_bf16 v[204:207], a[44:47], v[116:119], v[204:207]\n"
    "v_cvt_pk_bf16_f32 v41, v98, v99\n"
    "v_mfma_f32_16x16x32_bf16 v[204:207], a[12:15], v[48:51], v[204:207]\n"
    "v_exp_f32 v100, v100\n"
    "v_mfma_f32_16x16x32_bf16 v[72:75], v[120:123], v[48:51], v[72:75]\n"
    "ds_read_b128 a[240:243], v4 offset:28672\n"
    "v_mfma_f32_16x16x32_bf16 v[104:107], a[192:195], a[132:135], v[84:87]\n"
    "ds_read_b64_tr_b16 a[0:1], v8 offset:16384\n"
    "ds_read_b64_tr_b16 a[2:3], v8 offset:20480\n"
    "v_mfma_f32_16x16x32_bf16 v[104:107], a[196:199], a[148:151], v[104:107]\n"
    "v_exp_f32 v101, v101\n"
    "v_mfma_f32_16x16x32_bf16 v[104:107], a[200:203], a[180:183], v[104:107]\n"
    "ds_read_b64_tr_b16 a[4:5], v9 offset:16384\n"
    "v_cvt_pk_bf16_f32 v42, v100, v101\n"
    "v_mfma_f32_16x16x32_bf16 v[104:107], a[204:207], v[60:63], v[104:107]\n"
    "v_exp_f32 v102, v102\n"
    "v_mfma_f32_16x16x32_bf16 v[108:111], a[220:223], v[60:63], v[84:87]\n"
    "buffer_load_dwordx4 v15, s[12:15], s67 offen offset:1024 lds\n"
    "v_mfma_f32_16x16x32_bf16 v[108:111], a[216:219], a[180:183], v[108:111]\n"
    "v_exp_f32 v103, v103\n"
    "v_mfma_f32_16x16x32_bf16 v[108:111], a[212:215], a[148:151], v[108:111]\n"
    "ds_read_b64_tr_b16 a[6:7], v9 offset:20480\n"
    "v_cvt_pk_bf16_f32 v43, v102, v103\n"
    "v_mfma_f32_16x16x32_bf16 v[108:111], a[208:211], a[132:135], v[108:111]\n"
    "v_exp_f32 v104, v104\n"
    "v_mfma_f32_16x16x32_bf16 v[208:211], a[48:51], v[116:119], v[208:211]\n"
    "ds_read_b64_tr_b16 a[8:9], v8 offset:16896\n"
    "ds_read_b64_tr_b16 a[10:11], v8 offset:20992\n"
    "v_mfma_f32_16x16x32_bf16 v[208:211], a[16:19], v[48:51], v[208:211]\n"
    "v_exp_f32 v105, v105\n"
    "v_mfma_f32_16x16x32_bf16 v[212:215], a[20:23], v[48:51], v[212:215]\n"
    "v_cvt_pk_bf16_f32 v112, v104, v105\n"
    "v_mfma_f32_16x16x32_bf16 v[212:215], a[52:55], v[116:119], v[212:215]\n"
    "v_exp_f32 v106, v106\n"
    "v_mfma_f32_16x16x32_bf16 v[216:219], a[56:59], v[116:119], v[216:219]\n"
    "ds_read_b64_tr_b16 a[12:13], v9 offset:16896\n"
    "ds_read_b64_tr_b16 a[14:15], v9 offset:20992\n"
    "v_mfma_f32_16x16x32_bf16 v[216:219], a[24:27], v[48:51], v[216:219]\n"
    "v_exp_f32 v107, v107\n"
    "v_mfma_f32_16x16x32_bf16 v[220:223], a[28:31], v[48:51], v[220:223]\n"
    "v_cvt_pk_bf16_f32 v113, v106, v107\n"
    "v_mfma_f32_16x16x32_bf16 v[220:223], a[60:63], v[116:119], v[220:223]\n"
    "v_exp_f32 v108, v108\n"
    "v_mfma_f32_16x16x32_bf16 v[72:75], v[120:123], v[116:119], v[72:75]\n"
    "v_mfma_f32_16x16x32_bf16 v[96:99], a[192:195], a[136:139], v[88:91]\n"
    "ds_read_b64_tr_b16 a[16:17], v8 offset:17408\n"
    "ds_read_b64_tr_b16 a[18:19], v8 offset:21504\n"
    "v_mfma_f32_16x16x32_bf16 v[96:99], a[196:199], a[152:155], v[96:99]\n"
    "v_exp_f32 v109, v109\n"
    "v_mfma_f32_16x16x32_bf16 v[96:99], a[200:203], a[184:187], v[96:99]\n"
    "ds_read_b64_tr_b16 a[20:21], v9 offset:17408\n"
    "v_cvt_pk_bf16_f32 v114, v108, v109\n"
    "v_mfma_f32_16x16x32_bf16 v[96:99], a[204:207], v[124:127], v[96:99]\n"
    "v_exp_f32 v110, v110\n"
    "v_mfma_f32_16x16x32_bf16 v[100:103], a[220:223], v[124:127], v[88:91]\n"
    "buffer_load_dwordx4 v14, s[12:15], s67 offen offset:2048 lds\n"
    "v_mfma_f32_16x16x32_bf16 v[100:103], a[216:219], a[184:187], v[100:103]\n"
    "v_exp_f32 v111, v111\n"
    "v_mfma_f32_16x16x32_bf16 v[100:103], a[212:215], a[152:155], v[100:103]\n"
    "ds_read_b64_tr_b16 a[22:23], v9 offset:21504\n"
    "v_cvt_pk_bf16_f32 v115, v110, v111\n"
    "v_mfma_f32_16x16x32_bf16 v[100:103], a[208:211], a[136:139], v[100:103]\n"
    "s_waitcnt lgkmcnt(4)\n"
    "v_exp_f32 v96, v96\n"
    "v_mfma_f32_16x16x32_bf16 v[160:163], a[32:35], v[44:47], v[160:163]\n"
    "ds_read_b64_tr_b16 a[24:25], v8 offset:17920\n"
    "ds_read_b64_tr_b16 a[26:27], v8 offset:22016\n"
    "v_mfma_f32_16x16x32_bf16 v[160:163], a[0:3], v[112:115], v[160:163]\n"
    "v_exp_f32 v97, v97\n"
    "v_mfma_f32_16x16x32_bf16 v[164:167], a[4:7], v[112:115], v[164:167]\n"
    "v_cvt_pk_bf16_f32 v48, v96, v97\n"
    "v_mfma_f32_16x16x32_bf16 v[164:167], a[36:39], v[44:47], v[164:167]\n"
    "v_exp_f32 v98, v98\n"
    "v_mfma_f32_16x16x32_bf16 v[168:171], a[40:43], v[44:47], v[168:171]\n"
    "ds_read_b64_tr_b16 a[28:29], v9 offset:17920\n"
    "ds_read_b64_tr_b16 a[30:31], v9 offset:22016\n"
    "v_mfma_f32_16x16x32_bf16 v[168:171], a[8:11], v[112:115], v[168:171]\n"
    "v_exp_f32 v99, v99\n"
    "v_mfma_f32_16x16x32_bf16 v[172:175], a[12:15], v[112:115], v[172:175]\n"
    "v_cvt_pk_bf16_f32 v49, v98, v99\n"
    "v_mfma_f32_16x16x32_bf16 v[172:175], a[44:47], v[44:47], v[172:175]\n"
    "v_exp_f32 v100, v100\n"
    "v_mfma_f32_16x16x32_bf16 v[68:71], v[120:123], v[44:47], v[68:71]\n"
    "v_mfma_f32_16x16x32_bf16 v[104:107], a[192:195], a[140:143], v[92:95]\n"
    "ds_read_b128 a[244:247], v5 offset:28672\n"
    "v_mfma_f32_16x16x32_bf16 v[104:107], a[196:199], a[156:159], v[104:107]\n"
    "v_exp_f32 v101, v101\n"
    "v_mfma_f32_16x16x32_bf16 v[104:107], a[200:203], a[188:191], v[104:107]\n"
    "ds_read_b128 a[248:251], v6 offset:28672\n"
    "v_cvt_pk_bf16_f32 v50, v100, v101\n"
    "v_mfma_f32_16x16x32_bf16 v[104:107], a[204:207], v[28:31], v[104:107]\n"
    "v_exp_f32 v102, v102\n"
    "v_mfma_f32_16x16x32_bf16 v[108:111], a[220:223], v[28:31], v[92:95]\n"
    "buffer_load_dwordx4 v15, s[12:15], s67 offen offset:3072 lds\n"
    "v_mfma_f32_16x16x32_bf16 v[108:111], a[216:219], a[188:191], v[108:111]\n"
    "s_add_u32 s67, s67, 0x4000\n"
    "v_exp_f32 v103, v103\n"
    "v_mfma_f32_16x16x32_bf16 v[108:111], a[212:215], a[156:159], v[108:111]\n"
    "ds_read_b128 a[252:255], v7 offset:28672\n"
    "v_cvt_pk_bf16_f32 v51, v102, v103\n"
    "v_mfma_f32_16x16x32_bf16 v[108:111], a[208:211], a[140:143], v[108:111]\n"
    "s_waitcnt lgkmcnt(3)\n"
    "v_exp_f32 v104, v104\n"
    "v_mfma_f32_16x16x32_bf16 v[176:179], a[16:19], v[112:115], v[176:179]\n"
    "v_xor_b32 v4, 0x10000, v4\n"
    "v_xor_b32 v5, 0x10000, v5\n"
    "v_mfma_f32_16x16x32_bf16 v[176:179], a[48:51], v[44:47], v[176:179]\n"
    "s_xor_b32 s66, s66, 0x10000\n"
    "v_exp_f32 v105, v105\n"
    "v_mfma_f32_16x16x32_bf16 v[180:183], a[52:55], v[44:47], v[180:183]\n"
    "v_cvt_pk_bf16_f32 v116, v104, v105\n"
    "v_mfma_f32_16x16x32_bf16 v[180:183], a[20:23], v[112:115], v[180:183]\n"
    "v_exp_f32 v106, v106\n"
    "v_mfma_f32_16x16x32_bf16 v[184:187], a[24:27], v[112:115], v[184:187]\n"
    "v_xor_b32 v6, 0x10000, v6\n"
    "v_xor_b32 v7, 0x10000, v7\n"
    "v_mfma_f32_16x16x32_bf16 v[184:187], a[56:59], v[44:47], v[184:187]\n"
    "v_exp_f32 v107, v107\n"
    "v_mfma_f32_16x16x32_bf16 v[188:191], a[60:63], v[44:47], v[188:191]\n"
    "v_cvt_pk_bf16_f32 v117, v106, v107\n"
    "v_mfma_f32_16x16x32_bf16 v[188:191], a[28:31], v[112:115], v[188:191]\n"
    "s_waitcnt lgkmcnt(0)\n"
    "v_exp_f32 v108, v108\n"
    "v_mfma_f32_16x16x32_bf16 v[68:71], v[120:123], v[112:115], v[68:71]\n"
    "buffer_load_dword v34, v37, s[28:31], s88 offen offset:1024\n"
    "v_mfma_f32_16x16x32_bf16 v[96:99], a[224:227], a[128:131], v[80:83]\n"
    "s_waitcnt vmcnt(10)\n"
    "v_mfma_f32_16x16x32_bf16 v[96:99], a[228:231], a[144:147], v[96:99]\n"
    "s_barrier\n"
    "v_exp_f32 v109, v109\n"
    "v_mfma_f32_16x16x32_bf16 v[96:99], a[232:235], a[176:179], v[96:99]\n"
    "ds_read_b128 a[192:195], v4 offset:0\n"
    "v_cvt_pk_bf16_f32 v118, v108, v109\n"
    "v_mfma_f32_16x16x32_bf16 v[96:99], a[236:239], v[56:59], v[96:99]\n"
    "s_add_u32 m0, s66, 0\n"
    "v_exp_f32 v110, v110\n"
    "v_mfma_f32_16x16x32_bf16 v[100:103], a[252:255], v[56:59], v[80:83]\n"
    "buffer_load_dwordx4 v10, s[8:11], s67 offen offset:0 lds\n"
    "v_mfma_f32_16x16x32_bf16 v[100:103], a[248:251], a[176:179], v[100:103]\n"
    "v_exp_f32 v111, v111\n"
    "v_mfma_f32_16x16x32_bf16 v[100:103], a[244:247], a[144:147], v[100:103]\n"
    "ds_read_b128 a[196:199], v5 offset:0\n"
    "v_cvt_pk_bf16_f32 v119, v110, v111\n"
    "v_mfma_f32_16x16x32_bf16 v[100:103], a[240:243], a[128:131], v[100:103]\n"
    "v_exp_f32 v96, v96\n"
    "v_mfma_f32_16x16x32_bf16 v[224:227], a[32:35], v[52:55], v[224:227]\n"
    "ds_read_b128 a[200:203], v6 offset:0\n"
    "v_mfma_f32_16x16x32_bf16 v[224:227], a[0:3], v[116:119], v[224:227]\n"
    "v_exp_f32 v97, v97\n"
    "v_mfma_f32_16x16x32_bf16 v[228:231], a[4:7], v[116:119], v[228:231]\n"
    "v_cvt_pk_bf16_f32 v112, v96, v97\n"
    "v_mfma_f32_16x16x32_bf16 v[228:231], a[36:39], v[52:55], v[228:231]\n"
    "v_exp_f32 v98, v98\n"
    "v_mfma_f32_16x16x32_bf16 v[232:235], a[40:43], v[52:55], v[232:235]\n"
    "ds_read_b128 a[204:207], v7 offset:0\n"
    "v_mfma_f32_16x16x32_bf16 v[232:235], a[8:11], v[116:119], v[232:235]\n"
    "v_exp_f32 v99, v99\n"
    "v_mfma_f32_16x16x32_bf16 v[236:239], a[12:15], v[116:119], v[236:239]\n"
    "v_cvt_pk_bf16_f32 v113, v98, v99\n"
    "v_mfma_f32_16x16x32_bf16 v[236:239], a[44:47], v[52:55], v[236:239]\n"
    "v_exp_f32 v100, v100\n"
    "v_mfma_f32_16x16x32_bf16 v[76:79], v[120:123], v[52:55], v[76:79]\n"
    "ds_read_b128 a[208:211], v4 offset:4096\n"
    "v_mfma_f32_16x16x32_bf16 v[104:107], a[224:227], a[132:135], v[84:87]\n"
    "ds_read_b64_tr_b16 a[32:33], v8 offset:24576\n"
    "ds_read_b64_tr_b16 a[34:35], v8 offset:28672\n"
    "v_mfma_f32_16x16x32_bf16 v[104:107], a[228:231], a[148:151], v[104:107]\n"
    "v_exp_f32 v101, v101\n"
    "v_mfma_f32_16x16x32_bf16 v[104:107], a[232:235], a[180:183], v[104:107]\n"
    "ds_read_b64_tr_b16 a[36:37], v9 offset:24576\n"
    "v_cvt_pk_bf16_f32 v114, v100, v101\n"
    "v_mfma_f32_16x16x32_bf16 v[104:107], a[236:239], v[60:63], v[104:107]\n"
    "v_exp_f32 v102, v102\n"
    "v_mfma_f32_16x16x32_bf16 v[108:111], a[252:255], v[60:63], v[84:87]\n"
    "buffer_load_dwordx4 v11, s[8:11], s67 offen offset:1024 lds\n"
    "v_mfma_f32_16x16x32_bf16 v[108:111], a[248:251], a[180:183], v[108:111]\n"
    "v_exp_f32 v103, v103\n"
    "v_mfma_f32_16x16x32_bf16 v[108:111], a[244:247], a[148:151], v[108:111]\n"
    "ds_read_b64_tr_b16 a[38:39], v9 offset:28672\n"
    "v_cvt_pk_bf16_f32 v115, v102, v103\n"
    "v_mfma_f32_16x16x32_bf16 v[108:111], a[240:243], a[132:135], v[108:111]\n"
    "v_exp_f32 v104, v104\n"
    "v_mfma_f32_16x16x32_bf16 v[240:243], a[16:19], v[116:119], v[240:243]\n"
    "ds_read_b64_tr_b16 a[40:41], v8 offset:25088\n"
    "ds_read_b64_tr_b16 a[42:43], v8 offset:29184\n"
    "v_mfma_f32_16x16x32_bf16 v[240:243], a[48:51], v[52:55], v[240:243]\n"
    "v_exp_f32 v105, v105\n"
    "v_mfma_f32_16x16x32_bf16 v[244:247], a[52:55], v[52:55], v[244:247]\n"
    "v_cvt_pk_bf16_f32 v44, v104, v105\n"
    "v_mfma_f32_16x16x32_bf16 v[244:247], a[20:23], v[116:119], v[244:247]\n"
    "v_exp_f32 v106, v106\n"
    "v_mfma_f32_16x16x32_bf16 v[248:251], a[24:27], v[116:119], v[248:251]\n"
    "ds_read_b64_tr_b16 a[44:45], v9 offset:25088\n"
    "ds_read_b64_tr_b16 a[46:47], v9 offset:29184\n"
    "v_mfma_f32_16x16x32_bf16 v[248:251], a[56:59], v[52:55], v[248:251]\n"
    "v_exp_f32 v107, v107\n"
    "v_mfma_f32_16x16x32_bf16 v[252:255], a[60:63], v[52:55], v[252:255]\n"
    "v_cvt_pk_bf16_f32 v45, v106, v107\n"
    "v_mfma_f32_16x16x32_bf16 v[252:255], a[28:31], v[116:119], v[252:255]\n"
    "v_exp_f32 v108, v108\n"
    "v_mfma_f32_16x16x32_bf16 v[76:79], v[120:123], v[116:119], v[76:79]\n"
    "v_mfma_f32_16x16x32_bf16 v[96:99], a[224:227], a[136:139], v[88:91]\n"
    "ds_read_b64_tr_b16 a[48:49], v8 offset:25600\n"
    "ds_read_b64_tr_b16 a[50:51], v8 offset:29696\n"
    "v_mfma_f32_16x16x32_bf16 v[96:99], a[228:231], a[152:155], v[96:99]\n"
    "v_exp_f32 v109, v109\n"
    "v_mfma_f32_16x16x32_bf16 v[96:99], a[232:235], a[184:187], v[96:99]\n"
    "ds_read_b64_tr_b16 a[52:53], v9 offset:25600\n"
    "v_cvt_pk_bf16_f32 v46, v108, v109\n"
    "v_mfma_f32_16x16x32_bf16 v[96:99], a[236:239], v[124:127], v[96:99]\n"
    "v_exp_f32 v110, v110\n"
    "v_mfma_f32_16x16x32_bf16 v[100:103], a[252:255], v[124:127], v[88:91]\n"
    "buffer_load_dwordx4 v12, s[8:11], s67 offen offset:2048 lds\n"
    "v_mfma_f32_16x16x32_bf16 v[100:103], a[248:251], a[184:187], v[100:103]\n"
    "v_exp_f32 v111, v111\n"
    "v_mfma_f32_16x16x32_bf16 v[100:103], a[244:247], a[152:155], v[100:103]\n"
    "ds_read_b64_tr_b16 a[54:55], v9 offset:29696\n"
    "v_cvt_pk_bf16_f32 v47, v110, v111\n"
    "v_mfma_f32_16x16x32_bf16 v[100:103], a[240:243], a[136:139], v[100:103]\n"
    "s_waitcnt lgkmcnt(4)\n"
    "v_exp_f32 v96, v96\n"
    "v_mfma_f32_16x16x32_bf16 v[128:131], a[0:3], v[40:43], v[128:131]\n"
    "ds_read_b64_tr_b16 a[56:57], v8 offset:26112\n"
    "ds_read_b64_tr_b16 a[58:59], v8 offset:30208\n"
    "v_mfma_f32_16x16x32_bf16 v[128:131], a[32:35], v[112:115], v[128:131]\n"
    "v_exp_f32 v97, v97\n"
    "v_mfma_f32_16x16x32_bf16 v[132:135], a[36:39], v[112:115], v[132:135]\n"
    "v_cvt_pk_bf16_f32 v116, v96, v97\n"
    "v_mfma_f32_16x16x32_bf16 v[132:135], a[4:7], v[40:43], v[132:135]\n"
    "v_exp_f32 v98, v98\n"
    "v_mfma_f32_16x16x32_bf16 v[136:139], a[8:11], v[40:43], v[136:139]\n"
    "ds_read_b64_tr_b16 a[60:61], v9 offset:26112\n"
    "ds_read_b64_tr_b16 a[62:63], v9 offset:30208\n"
    "v_mfma_f32_16x16x32_bf16 v[136:139], a[40:43], v[112:115], v[136:139]\n"
    "v_exp_f32 v99, v99\n"
    "v_mfma_f32_16x16x32_bf16 v[140:143], a[44:47], v[112:115], v[140:143]\n"
    "v_cvt_pk_bf16_f32 v117, v98, v99\n"
    "v_mfma_f32_16x16x32_bf16 v[140:143], a[12:15], v[40:43], v[140:143]\n"
    "v_exp_f32 v100, v100\n"
    "v_mfma_f32_16x16x32_bf16 v[64:67], v[120:123], v[40:43], v[64:67]\n"
    "v_mfma_f32_16x16x32_bf16 v[104:107], a[224:227], a[140:143], v[92:95]\n"
    "ds_read_b128 a[212:215], v5 offset:4096\n"
    "v_mfma_f32_16x16x32_bf16 v[104:107], a[228:231], a[156:159], v[104:107]\n"
    "v_exp_f32 v101, v101\n"
    "v_mfma_f32_16x16x32_bf16 v[104:107], a[232:235], a[188:191], v[104:107]\n"
    "ds_read_b128 a[216:219], v6 offset:4096\n"
    "v_cvt_pk_bf16_f32 v118, v100, v101\n"
    "v_mfma_f32_16x16x32_bf16 v[104:107], a[236:239], v[28:31], v[104:107]\n"
    "v_exp_f32 v102, v102\n"
    "v_mfma_f32_16x16x32_bf16 v[108:111], a[252:255], v[28:31], v[92:95]\n"
    "buffer_load_dwordx4 v13, s[8:11], s67 offen offset:3072 lds\n"
    "v_mfma_f32_16x16x32_bf16 v[108:111], a[248:251], a[188:191], v[108:111]\n"
    "v_exp_f32 v103, v103\n"
    "v_mfma_f32_16x16x32_bf16 v[108:111], a[244:247], a[156:159], v[108:111]\n"
    "ds_read_b128 a[220:223], v7 offset:4096\n"
    "v_cvt_pk_bf16_f32 v119, v102, v103\n"
    "v_mfma_f32_16x16x32_bf16 v[108:111], a[240:243], a[140:143], v[108:111]\n"
    "s_waitcnt lgkmcnt(3)\n"
    "v_exp_f32 v104, v104\n"
    "v_mfma_f32_16x16x32_bf16 v[144:147], a[48:51], v[112:115], v[144:147]\n"
    "v_xor_b32 v8, 0x10000, v8\n"
    "v_xor_b32 v9, 0x10000, v9\n"
    "v_mfma_f32_16x16x32_bf16 v[144:147], a[16:19], v[40:43], v[144:147]\n"
    "v_exp_f32 v105, v105\n"
    "v_mfma_f32_16x16x32_bf16 v[148:151], a[20:23], v[40:43], v[148:151]\n"
    "v_cvt_pk_bf16_f32 v52, v104, v105\n"
    "v_mfma_f32_16x16x32_bf16 v[148:151], a[52:55], v[112:115], v[148:151]\n"
    "v_exp_f32 v106, v106\n"
    "v_mfma_f32_16x16x32_bf16 v[152:155], a[56:59], v[112:115], v[152:155]\n"
    "v_mfma_f32_16x16x32_bf16 v[152:155], a[24:27], v[40:43], v[152:155]\n"
    "s_cmp_lt_u32 s67, 0x80000\n"
    "v_exp_f32 v107, v107\n"
    "v_mfma_f32_16x16x32_bf16 v[156:159], a[28:31], v[40:43], v[156:159]\n"
    "v_cvt_pk_bf16_f32 v53, v106, v107\n"
    "v_mfma_f32_16x16x32_bf16 v[156:159], a[60:63], v[112:115], v[156:159]\n"
    "s_waitcnt lgkmcnt(0)\n"
    "v_exp_f32 v108, v108\n"
    "v_mfma_f32_16x16x32_bf16 v[64:67], v[120:123], v[112:115], v[64:67]\n"
    "s_cbranch_scc1 L_loop%=\n"
    "v_mfma_f32_16x16x32_bf16 v[96:99], a[192:195], a[128:131], v[80:83]\n"
    "s_waitcnt vmcnt(11)\n"
    "v_cmp_ne_u64 vcc, s[90:91], v[26:27]\n"
    "s_and_b32 s68, vcc_lo, 0xffffffff\n"
    "s_cbranch_scc0 L_phok%=\n"
    "s_mov_b32 s86, 0\n"
    "buffer_load_dwordx2 v[26:27], v21, s[24:27], s51 offen offset:0 sc1\n"
    "s_sleep 1\n"
    "L_ph%=:\n"
    "s_waitcnt vmcnt(0)\n"
    "v_cmp_ne_u64 vcc, s[90:91], v[26:27]\n"
    "s_and_b32 s68, vcc_lo, 0xffffffff\n"
    "s_cbranch_scc0 L_phok%=\n"
    "buffer_load_dwordx2 v[26:27], v21, s[24:27], s51 offen offset:0 sc1\n"
    "s_add_u32 s86, s86, 1\n"
    "s_cmp_lt_u32 s86, 0x100000\n"
    "s_cbranch_scc1 L_ph%=\n"
    "L_phok%=:\n"
    "v_mfma_f32_16x16x32_bf16 v[96:99], a[196:199], a[144:147], v[96:99]\n"
    "v_exp_f32 v109, v109\n"
    "v_mfma_f32_16x16x32_bf16 v[96:99], a[200:203], a[176:179], v[96:99]\n"
    "ds_read_b128 a[224:227], v4 offset:8192\n"
    "v_cvt_pk_bf16_f32 v54, v108, v109\n"
    "v_mfma_f32_16x16x32_bf16 v[96:99], a[204:207], v[56:59], v[96:99]\n"
    "v_exp_f32 v110, v110\n"
    "v_mfma_f32_16x16x32_bf16 v[100:103], a[220:223], v[56:59], v[80:83]\n"
    "v_and_b32 v37, 7, v1\n"
    "v_mfma_f32_16x16x32_bf16 v[100:103], a[216:219], a[176:179], v[100:103]\n"
    "v_exp_f32 v111, v111\n"
    "v_mfma_f32_16x16x32_bf16 v[100:103], a[212:215], a[144:147], v[100:103]\n"
    "ds_read_b128 a[228:231], v5 offset:8192\n"
    "v_cvt_pk_bf16_f32 v55, v110, v111\n"
    "v_mfma_f32_16x16x32_bf16 v[100:103], a[208:211], a[128:131], v[100:103]\n"
    "v_exp_f32 v96, v96\n"
    "v_mfma_f32_16x16x32_bf16 v[192:195], a[0:3], v[48:51], v[192:195]\n"
    "ds_read_b128 a[232:235], v6 offset:8192\n"
    "v_lshlrev_b32 v37, 7, v37\n"
    "v_mfma_f32_16x16x32_bf16 v[192:195], a[32:35], v[116:119], v[192:195]\n"
    "v_exp_f32 v97, v97\n"
    "v_mfma_f32_16x16x32_bf16 v[196:199], a[36:39], v[116:119], v[196:199]\n"
    "buffer_load_dword v35, v37, s[92:95], s85 offen offset:0\n"
    "v_cvt_pk_bf16_f32 v40, v96, v97\n"
    "v_mfma_f32_16x16x32_bf16 v[196:199], a[4:7], v[48:51], v[196:199]\n"
    "v_exp_f32 v98, v98\n"
    "v_mfma_f32_16x16x32_bf16 v[200:203], a[8:11], v[48:51], v[200:203]\n"
    "ds_read_b128 a[236:239], v7 offset:8192\n"
    "s_add_u32 s87, s47, 0\n"
    "buffer_load_dwordx4 a[64:67], v2, s[24:27], s87 offen offset:0 sc1\n"
    "v_mfma_f32_16x16x32_bf16 v[200:203], a[40:43], v[116:119], v[200:203]\n"
    "v_exp_f32 v99, v99\n"
    "v_mfma_f32_16x16x32_bf16 v[204:207], a[44:47], v[116:119], v[204:207]\n"
    "buffer_load_dwordx4 a[68:71], v2, s[24:27], s87 offen offset:1024 sc1\n"
    "v_cvt_pk_bf16_f32 v41, v98, v99\n"
    "v_mfma_f32_16x16x32_bf16 v[204:207], a[12:15], v[48:51], v[204:207]\n"
    "v_exp_f32 v100, v100\n"
    "v_mfma_f32_16x16x32_bf16 v[72:75], v[120:123], v[48:51], v[72:75]\n"
    "ds_read_b128 a[240:243], v4 offset:12288\n"
    "s_add_u32 s88, s87, 0x2c000\n"
    "buffer_load_dwordx4 a[72:75], v2, s[24:27], s88 offen offset:0 sc1\n"
    "v_mfma_f32_16x16x32_bf16 v[104:107], a[192:195], a[132:135], v[84:87]\n"
    "ds_read_b64_tr_b16 a[0:1], v8 offset:0\n"
    "ds_read_b64_tr_b16 a[2:3], v8 offset:4096\n"
    "v_mfma_f32_16x16x32_bf16 v[104:107], a[196:199], a[148:151], v[104:107]\n"
    "v_exp_f32 v101, v101\n"
    "v_mfma_f32_16x16x32_bf16 v[104:107], a[200:203], a[180:183], v[104:107]\n"
    "ds_read_b64_tr_b16 a[4:5], v9 offset:0\n"
    "v_cvt_pk_bf16_f32 v42, v100, v101\n"
    "v_mfma_f32_16x16x32_bf16 v[104:107], a[204:207], v[60:63], v[104:107]\n"
    "v_exp_f32 v102, v102\n"
    "v_mfma_f32_16x16x32_bf16 v[108:111], a[220:223], v[60:63], v[84:87]\n"
    "buffer_load_dwordx4 a[76:79], v2, s[24:27], s88 offen offset:1024 sc1\n"
    "v_mfma_f32_16x16x32_bf16 v[108:111], a[216:219], a[180:183], v[108:111]\n"
    "v_exp_f32 v103, v103\n"
    "v_mfma_f32_16x16x32_bf16 v[108:111], a[212:215], a[148:151], v[108:111]\n"
    "ds_read_b64_tr_b16 a[6:7], v9 offset:4096\n"
    "v_cvt_pk_bf16_f32 v43, v102, v103\n"
    "v_mfma_f32_16x16x32_bf16 v[108:111], a[208:211], a[132:135], v[108:111]\n"
    "v_exp_f32 v104, v104\n"
    "v_mfma_f32_16x16x32_bf16 v[208:211], a[48:51], v[116:119], v[208:211]\n"
    "ds_read_b64_tr_b16 a[8:9], v8 offset:512\n"
    "ds_read_b64_tr_b16 a[10:11], v8 offset:4608\n"
    "v_mfma_f32_16x16x32_bf16 v[208:211], a[16:19], v[48:51], v[208:211]\n"
    "v_exp_f32 v105, v105\n"
    "v_mfma_f32_16x16x32_bf16 v[212:215], a[20:23], v[48:51], v[212:215]\n"
    "s_add_u32 m0, s65, 0x8000\n"
    "s_mov_b32 s69, 0x20000\n"
    "buffer_load_dwordx4 v16, s[28:31], s69 offen offset:0 lds\n"
    "v_cvt_pk_bf16_f32 v112, v104, v105\n"
    "v_mfma_f32_16x16x32_bf16 v[212:215], a[52:55], v[116:119], v[212:215]\n"
    "v_exp_f32 v106, v106\n"
    "v_mfma_f32_16x16x32_bf16 v[216:219], a[56:59], v[116:119], v[216:219]\n"
    "ds_read_b64_tr_b16 a[12:13], v9 offset:512\n"
    "ds_read_b64_tr_b16 a[14:15], v9 offset:4608\n"
    "v_mfma_f32_16x16x32_bf16 v[216:219], a[24:27], v[48:51], v[216:219]\n"
    "v_exp_f32 v107, v107\n"
    "v_mfma_f32_16x16x32_bf16 v[220:223], a[28:31], v[48:51], v[220:223]\n"
    "buffer_load_dwordx4 v17, s[28:31], s69 offen offset:1024 lds\n"
    "v_cvt_pk_bf16_f32 v113, v106, v107\n"
    "v_mfma_f32_16x16x32_bf16 v[220:223], a[60:63], v[116:119], v[220:223]\n"
    "v_exp_f32 v108, v108\n"
    "v_mfma_f32_16x16x32_bf16 v[72:75], v[120:123], v[116:119], v[72:75]\n"
    "buffer_load_dwordx4 v18, s[28:31], s69 offen offset:2048 lds\n"
    "v_mfma_f32_16x16x32_bf16 v[96:99], a[192:195], a[136:139], v[88:91]\n"
    "ds_read_b64_tr_b16 a[16:17], v8 offset:1024\n"
    "ds_read_b64_tr_b16 a[18:19], v8 offset:5120\n"
    "v_mfma_f32_16x16x32_bf16 v[96:99], a[196:199], a[152:155], v[96:99]\n"
    "v_exp_f32 v109, v109\n"
    "v_mfma_f32_16x16x32_bf16 v[96:99], a[200:203], a[184:187], v[96:99]\n"
    "ds_read_b64_tr_b16 a[20:21], v9 offset:1024\n"
    "v_cvt_pk_bf16_f32 v114, v108, v109\n"
    "v_mfma_f32_16x16x32_bf16 v[96:99], a[204:207], v[124:127], v[96:99]\n"
    "v_exp_f32 v110, v110\n"
    "v_mfma_f32_16x16x32_bf16 v[100:103], a[220:223], v[124:127], v[88:91]\n"
    "buffer_load_dwordx4 v19, s[28:31], s69 offen offset:3072 lds\n"
    "v_mfma_f32_16x16x32_bf16 v[100:103], a[216:219], a[184:187], v[100:103]\n"
    "v_exp_f32 v111, v111\n"
    "v_mfma_f32_16x16x32_bf16 v[100:103], a[212:215], a[152:155], v[100:103]\n"
    "ds_read_b64_tr_b16 a[22:23], v9 offset:5120\n"
    "v_cvt_pk_bf16_f32 v115, v110, v111\n"
    "v_mfma_f32_16x16x32_bf16 v[100:103], a[208:211], a[136:139], v[100:103]\n"
    "s_waitcnt lgkmcnt(4)\n"
    "v_exp_f32 v96, v96\n"
    "v_mfma_f32_16x16x32_bf16 v[160:163], a[32:35], v[44:47], v[160:163]\n"
    "ds_read_b64_tr_b16 a[24:25], v8 offset:1536\n"
    "ds_read_b64_tr_b16 a[26:27], v8 offset:5632\n"
    "v_mfma_f32_16x16x32_bf16 v[160:163], a[0:3], v[112:115], v[160:163]\n"
    "v_exp_f32 v97, v97\n"
    "v_mfma_f32_16x16x32_bf16 v[164:167], a[4:7], v[112:115], v[164:167]\n"
    "s_add_u32 s87, s47, 0x800\n"
    "buffer_load_dwordx4 a[80:83], v2, s[24:27], s87 offen offset:0 sc1\n"
    "v_cvt_pk_bf16_f32 v48, v96, v97\n"
    "v_mfma_f32_16x16x32_bf16 v[164:167], a[36:39], v[44:47], v[164:167]\n"
    "v_exp_f32 v98, v98\n"
    "v_mfma_f32_16x16x32_bf16 v[168:171], a[40:43], v[44:47], v[168:171]\n"
    "ds_read_b64_tr_b16 a[28:29], v9 offset:1536\n"
    "ds_read_b64_tr_b16 a[30:31], v9 offset:5632\n"
    "v_mfma_f32_16x16x32_bf16 v[168:171], a[8:11], v[112:115], v[168:171]\n"
    "v_exp_f32 v99, v99\n"
    "v_mfma_f32_16x16x32_bf16 v[172:175], a[12:15], v[112:115], v[172:175]\n"
    "buffer_load_dwordx4 a[84:87], v2, s[24:27], s87 offen offset:1024 sc1\n"
    "v_cvt_pk_bf16_f32 v49, v98, v99\n"
    "v_mfma_f32_16x16x32_bf16 v[172:175], a[44:47], v[44:47], v[172:175]\n"
    "v_exp_f32 v100, v100\n"
    "v_mfma_f32_16x16x32_bf16 v[68:71], v[120:123], v[44:47], v[68:71]\n"
    "s_add_u32 s88, s87, 0x2c000\n"
    "buffer_load_dwordx4 a[88:91], v2, s[24:27], s88 offen offset:0 sc1\n"
    "v_mfma_f32_16x16x32_bf16 v[104:107], a[192:195], a[140:143], v[92:95]\n"
    "ds_read_b128 a[244:247], v5 offset:12288\n"
    "v_mfma_f32_16x16x32_bf16 v[104:107], a[196:199], a[156:159], v[104:107]\n"
    "v_exp_f32 v101, v101\n"
    "v_mfma_f32_16x16x32_bf16 v[104:107], a[200:203], a[188:191], v[104:107]\n"
    "ds_read_b128 a[248:251], v6 offset:12288\n"
    "v_cvt_pk_bf16_f32 v50, v100, v101\n"
    "v_mfma_f32_16x16x32_bf16 v[104:107], a[204:207], v[28:31], v[104:107]\n"
    "v_exp_f32 v102, v102\n"
    "v_mfma_f32_16x16x32_bf16 v[108:111], a[220:223], v[28:31], v[92:95]\n"
    "buffer_load_dwordx4 a[92:95], v2, s[24:27], s88 offen offset:1024 sc1\n"
    "v_mfma_f32_16x16x32_bf16 v[108:111], a[216:219], a[188:191], v[108:111]\n"
    "v_exp_f32 v103, v103\n"
    "v_mfma_f32_16x16x32_bf16 v[108:111], a[212:215], a[156:159], v[108:111]\n"
    "ds_read_b128 a[252:255], v7 offset:12288\n"
    "v_cvt_pk_bf16_f32 v51, v102, v103\n"
    "v_mfma_f32_16x16x32_bf16 v[108:111], a[208:211], a[140:143], v[108:111]\n"
    "s_waitcnt lgkmcnt(3)\n"
    "v_exp_f32 v104, v104\n"
    "v_mfma_f32_16x16x32_bf16 v[176:179], a[16:19], v[112:115], v[176:179]\n"
    "s_add_u32 m0, s65, 0x4000\n"
    "s_mov_b32 s69, 0x30000\n"
    "buffer_load_dwordx4 v16, s[28:31], s69 offen offset:0 lds\n"
    "v_mfma_f32_16x16x32_bf16 v[176:179], a[48:51], v[44:47], v[176:179]\n"
    "v_exp_f32 v105, v105\n"
    "v_mfma_f32_16x16x32_bf16 v[180:183], a[52:55], v[44:47], v[180:183]\n"
    "buffer_load_dwordx4 v17, s[28:31], s69 offen offset:1024 lds\n"
    "v_cvt_pk_bf16_f32 v116, v104, v105\n"
    "v_mfma_f32_16x16x32_bf16 v[180:183], a[20:23], v[112:115], v[180:183]\n"
    "v_exp_f32 v106, v106\n"
    "v_mfma_f32_16x16x32_bf16 v[184:187], a[24:27], v[112:115], v[184:187]\n"
    "buffer_load_dwordx4 v18, s[28:31], s69 offen offset:2048 lds\n"
    "v_mfma_f32_16x16x32_bf16 v[184:187], a[56:59], v[44:47], v[184:187]\n"
    "v_exp_f32 v107, v107\n"
    "v_mfma_f32_16x16x32_bf16 v[188:191], a[60:63], v[44:47], v[188:191]\n"
    "buffer_load_dwordx4 v19, s[28:31], s69 offen offset:3072 lds\n"
    "v_cvt_pk_bf16_f32 v117, v106, v107\n"
    "v_mfma_f32_16x16x32_bf16 v[188:191], a[28:31], v[112:115], v[188:191]\n"
    "s_waitcnt lgkmcnt(0)\n"
    "v_exp_f32 v108, v108\n"
    "v_mfma_f32_16x16x32_bf16 v[68:71], v[120:123], v[112:115], v[68:71]\n"
    "s_add_u32 s87, s47, 0x1000\n"
    "buffer_load_dwordx4 a[96:99], v2, s[24:27], s87 offen offset:0 sc1\n"
    "v_mfma_f32_16x16x32_bf16 v[96:99], a[224:227], a[128:131], v[80:83]\n"
    "s_waitcnt vmcnt(23)\n"
    "v_mfma_f32_16x16x32_bf16 v[96:99], a[228:231], a[144:147], v[96:99]\n"
    "s_barrier\n"
    "v_exp_f32 v109, v109\n"
    "v_mfma_f32_16x16x32_bf16 v[96:99], a[232:235], a[176:179], v[96:99]\n"
    "ds_read_b128 a[192:195], v4 offset:16384\n"
    "v_cvt_pk_bf16_f32 v118, v108, v109\n"
    "v_mfma_f32_16x16x32_bf16 v[96:99], a[236:239], v[56:59], v[96:99]\n"
    "v_exp_f32 v110, v110\n"
    "v_mfma_f32_16x16x32_bf16 v[100:103], a[252:255], v[56:59], v[80:83]\n"
    "buffer_load_dwordx4 a[100:103], v2, s[24:27], s87 offen offset:1024 sc1\n"
    "v_mfma_f32_16x16x32_bf16 v[100:103], a[248:251], a[176:179], v[100:103]\n"
    "v_exp_f32 v111, v111\n"
    "v_mfma_f32_16x16x32_bf16 v[100:103], a[244:247], a[144:147], v[100:103]\n"
    "ds_read_b128 a[196:199], v5 offset:16384\n"
    "v_cvt_pk_bf16_f32 v119, v110, v111\n"
    "v_mfma_f32_16x16x32_bf16 v[100:103], a[240:243], a[128:131], v[100:103]\n"
    "v_exp_f32 v96, v96\n"
    "v_mfma_f32_16x16x32_bf16 v[224:227], a[32:35], v[52:55], v[224:227]\n"
    "ds_read_b128 a[200:203], v6 offset:16384\n"
    "s_add_u32 s88, s87, 0x2c000\n"
    "buffer_load_dwordx4 a[104:107], v2, s[24:27], s88 offen offset:0 sc1\n"
    "v_mfma_f32_16x16x32_bf16 v[224:227], a[0:3], v[116:119], v[224:227]\n"
    "v_exp_f32 v97, v97\n"
    "v_mfma_f32_16x16x32_bf16 v[228:231], a[4:7], v[116:119], v[228:231]\n"
    "buffer_load_dwordx4 a[108:111], v2, s[24:27], s88 offen offset:1024 sc1\n"
    "v_cvt_pk_bf16_f32 v112, v96, v97\n"
    "v_mfma_f32_16x16x32_bf16 v[228:231], a[36:39], v[52:55], v[228:231]\n"
    "v_exp_f32 v98, v98\n"
    "v_mfma_f32_16x16x32_bf16 v[232:235], a[40:43], v[52:55], v[232:235]\n"
    "ds_read_b128 a[204:207], v7 offset:16384\n"
    "s_add_u32 m0, s65, 0xc000\n"
    "s_mov_b32 s69, 0x40000\n"
    "buffer_load_dwordx4 v16, s[28:31], s69 offen offset:0 lds\n"
    "v_mfma_f32_16x16x32_bf16 v[232:235], a[8:11], v[116:119], v[232:235]\n"
    "v_exp_f32 v99, v99\n"
    "v_mfma_f32_16x16x32_bf16 v[236:239], a[12:15], v[116:119], v[236:239]\n"
    "buffer_load_dwordx4 v17, s[28:31], s69 offen offset:1024 lds\n"
    "v_cvt_pk_bf16_f32 v113, v98, v99\n"
    "v_mfma_f32_16x16x32_bf16 v[236:239], a[44:47], v[52:55], v[236:239]\n"
    "v_exp_f32 v100, v100\n"
    "v_mfma_f32_16x16x32_bf16 v[76:79], v[120:123], v[52:55], v[76:79]\n"
    "ds_read_b128 a[208:211], v4 offset:20480\n"
    "buffer_load_dwordx4 v18, s[28:31], s69 offen offset:2048 lds\n"
    "v_mfma_f32_16x16x32_bf16 v[104:107], a[224:227], a[132:135], v[84:87]\n"
    "ds_read_b64_tr_b16 a[32:33], v8 offset:8192\n"
    "ds_read_b64_tr_b16 a[34:35], v8 offset:12288\n"
    "v_mfma_f32_16x16x32_bf16 v[104:107], a[228:231], a[148:151], v[104:107]\n"
    "v_exp_f32 v101, v101\n"
    "v_mfma_f32_16x16x32_bf16 v[104:107], a[232:235], a[180:183], v[104:107]\n"
    "ds_read_b64_tr_b16 a[36:37], v9 offset:8192\n"
    "v_cvt_pk_bf16_f32 v114, v100, v101\n"
    "v_mfma_f32_16x16x32_bf16 v[104:107], a[236:239], v[60:63], v[104:107]\n"
    "v_exp_f32 v102, v102\n"
    "v_mfma_f32_16x16x32_bf16 v[108:111], a[252:255], v[60:63], v[84:87]\n"
    "buffer_load_dwordx4 v19, s[28:31], s69 offen offset:3072 lds\n"
    "v_mfma_f32_16x16x32_bf16 v[108:111], a[248:251], a[180:183], v[108:111]\n"
    "v_exp_f32 v103, v103\n"
    "v_mfma_f32_16x16x32_bf16 v[108:111], a[244:247], a[148:151], v[108:111]\n"
    "ds_read_b64_tr_b16 a[38:39], v9 offset:12288\n"
    "v_cvt_pk_bf16_f32 v115, v102, v103\n"
    "v_mfma_f32_16x16x32_bf16 v[108:111], a[240:243], a[132:135], v[108:111]\n"
    "v_exp_f32 v104, v104\n"
    "v_mfma_f32_16x16x32_bf16 v[240:243], a[16:19], v[116:119], v[240:243]\n"
    "ds_read_b64_tr_b16 a[40:41], v8 offset:8704\n"
    "ds_read_b64_tr_b16 a[42:43], v8 offset:12800\n"
    "v_mfma_f32_16x16x32_bf16 v[240:243], a[48:51], v[52:55], v[240:243]\n"
    "v_exp_f32 v105, v105\n"
    "v_mfma_f32_16x16x32_bf16 v[244:247], a[52:55], v[52:55], v[244:247]\n"
    "s_add_u32 s87, s47, 0x1800\n"
    "buffer_load_dwordx4 a[112:115], v2, s[24:27], s87 offen offset:0 sc1\n"
    "v_cvt_pk_bf16_f32 v44, v104, v105\n"
    "v_mfma_f32_16x16x32_bf16 v[244:247], a[20:23], v[116:119], v[244:247]\n"
    "v_exp_f32 v106, v106\n"
    "v_mfma_f32_16x16x32_bf16 v[248:251], a[24:27], v[116:119], v[248:251]\n"
    "ds_read_b64_tr_b16 a[44:45], v9 offset:8704\n"
    "ds_read_b64_tr_b16 a[46:47], v9 offset:12800\n"
    "v_mfma_f32_16x16x32_bf16 v[248:251], a[56:59], v[52:55], v[248:251]\n"
    "v_exp_f32 v107, v107\n"
    "v_mfma_f32_16x16x32_bf16 v[252:255], a[60:63], v[52:55], v[252:255]\n"
    "buffer_load_dwordx4 a[116:119], v2, s[24:27], s87 offen offset:1024 sc1\n"
    "v_cvt_pk_bf16_f32 v45, v106, v107\n"
    "v_mfma_f32_16x16x32_bf16 v[252:255], a[28:31], v[116:119], v[252:255]\n"
    "v_exp_f32 v108, v108\n"
    "v_mfma_f32_16x16x32_bf16 v[76:79], v[120:123], v[116:119], v[76:79]\n"
    "s_add_u32 s88, s87, 0x2c000\n"
    "buffer_load_dwordx4 a[120:123], v2, s[24:27], s88 offen offset:0 sc1\n"
    "v_mfma_f32_16x16x32_bf16 v[96:99], a[224:227], a[136:139], v[88:91]\n"
    "ds_read_b64_tr_b16 a[48:49], v8 offset:9216\n"
    "ds_read_b64_tr_b16 a[50:51], v8 offset:13312\n"
    "v_mfma_f32_16x16x32_bf16 v[96:99], a[228:231], a[152:155], v[96:99]\n"
    "v_exp_f32 v109, v109\n"
    "v_mfma_f32_16x16x32_bf16 v[96:99], a[232:235], a[184:187], v[96:99]\n"
    "ds_read_b64_tr_b16 a[52:53], v9 offset:9216\n"
    "v_cvt_pk_bf16_f32 v46, v108, v109\n"
    "v_mfma_f32_16x16x32_bf16 v[96:99], a[236:239], v[124:127], v[96:99]\n"
    "v_exp_f32 v110, v110\n"
    "v_mfma_f32_16x16x32_bf16 v[100:103], a[252:255], v[124:127], v[88:91]\n"
    "buffer_load_dwordx4 a[124:127], v2, s[24:27], s88 offen offset:1024 sc1\n"
    "v_mfma_f32_16x16x32_bf16 v[100:103], a[248:251], a[184:187], v[100:103]\n"
    "v_exp_f32 v111, v111\n"
    "v_mfma_f32_16x16x32_bf16 v[100:103], a[244:247], a[152:155], v[100:103]\n"
    "ds_read_b64_tr_b16 a[54:55], v9 offset:13312\n"
    "v_cvt_pk_bf16_f32 v47, v110, v111\n"
    "v_mfma_f32_16x16x32_bf16 v[100:103], a[240:243], a[136:139], v[100:103]\n"
    "s_waitcnt lgkmcnt(4)\n"
    "v_exp_f32 v96, v96\n"
    "v_mfma_f32_16x16x32_bf16 v[128:131], a[0:3], v[40:43], v[128:131]\n"
    "ds_read_b64_tr_b16 a[56:57], v8 offset:9728\n"
    "ds_read_b64_tr_b16 a[58:59], v8 offset:13824\n"
    "v_mfma_f32_16x16x32_bf16 v[128:131], a[32:35], v[112:115], v[128:131]\n"
    "v_exp_f32 v97, v97\n"
    "v_mfma_f32_16x16x32_bf16 v[132:135], a[36:39], v[112:115], v[132:135]\n"
    "s_add_u32 m0, s65, 0x10000\n"
    "s_mov_b32 s69, 0x50000\n"
    "buffer_load_dwordx4 v16, s[28:31], s69 offen offset:0 lds\n"
    "v_cvt_pk_bf16_f32 v116, v96, v97\n"
    "v_mfma_f32_16x16x32_bf16 v[132:135], a[4:7], v[40:43], v[132:135]\n"
    "v_exp_f32 v98, v98\n"
    "v_mfma_f32_16x16x32_bf16 v[136:139], a[8:11], v[40:43], v[136:139]\n"
    "ds_read_b64_tr_b16 a[60:61], v9 offset:9728\n"
    "ds_read_b64_tr_b16 a[62:63], v9 offset:13824\n"
    "v_mfma_f32_16x16x32_bf16 v[136:139], a[40:43], v[112:115], v[136:139]\n"
    "v_exp_f32 v99, v99\n"
    "v_mfma_f32_16x16x32_bf16 v[140:143], a[44:47], v[112:115], v[140:143]\n"
    "buffer_load_dwordx4 v17, s[28:31], s69 offen offset:1024 lds\n"
    "v_cvt_pk_bf16_f32 v117, v98, v99\n"
    "v_mfma_f32_16x16x32_bf16 v[140:143], a[12:15], v[40:43], v[140:143]\n"
    "v_exp_f32 v100, v100\n"
    "v_mfma_f32_16x16x32_bf16 v[64:67], v[120:123], v[40:43], v[64:67]\n"
    "buffer_load_dwordx4 v18, s[28:31], s69 offen offset:2048 lds\n"
    "v_mfma_f32_16x16x32_bf16 v[104:107], a[224:227], a[140:143], v[92:95]\n"
    "ds_read_b128 a[212:215], v5 offset:20480\n"
    "v_mfma_f32_16x16x32_bf16 v[104:107], a[228:231], a[156:159], v[104:107]\n"
    "v_exp_f32 v101, v101\n"
    "v_mfma_f32_16x16x32_bf16 v[104:107], a[232:235], a[188:191], v[104:107]\n"
    "ds_read_b128 a[216:219], v6 offset:20480\n"
    "v_cvt_pk_bf16_f32 v118, v100, v101\n"
    "v_mfma_f32_16x16x32_bf16 v[104:107], a[236:239], v[28:31], v[104:107]\n"
    "v_exp_f32 v102, v102\n"
    "v_mfma_f32_16x16x32_bf16 v[108:111], a[252:255], v[28:31], v[92:95]\n"
    "buffer_load_dwordx4 v19, s[28:31], s69 offen offset:3072 lds\n"
    "v_mfma_f32_16x16x32_bf16 v[108:111], a[248:251], a[188:191], v[108:111]\n"
    "v_exp_f32 v103, v103\n"
    "v_mfma_f32_16x16x32_bf16 v[108:111], a[244:247], a[156:159], v[108:111]\n"
    "ds_read_b128 a[220:223], v7 offset:20480\n"
    "v_cvt_pk_bf16_f32 v119, v102, v103\n"
    "v_mfma_f32_16x16x32_bf16 v[108:111], a[240:243], a[140:143], v[108:111]\n"
    "s_waitcnt lgkmcnt(3)\n"
    "v_exp_f32 v104, v104\n"
    "v_mfma_f32_16x16x32_bf16 v[144:147], a[48:51], v[112:115], v[144:147]\n"
    "s_add_u32 s87, s47, 0x2000\n"
    "buffer_load_dwordx4 a[160:163], v2, s[24:27], s87 offen offset:0 sc1\n"
    "v_mfma_f32_16x16x32_bf16 v[144:147], a[16:19], v[40:43], v[144:147]\n"
    "v_exp_f32 v105, v105\n"
    "v_mfma_f32_16x16x32_bf16 v[148:151], a[20:23], v[40:43], v[148:151]\n"
    "buffer_load_dwordx4 a[164:167], v2, s[24:27], s87 offen offset:1024 sc1\n"
    "v_cvt_pk_bf16_f32 v52, v104, v105\n"
    "v_mfma_f32_16x16x32_bf16 v[148:151], a[52:55], v[112:115], v[148:151]\n"
    "v_exp_f32 v106, v106\n"
    "v_mfma_f32_16x16x32_bf16 v[152:155], a[56:59], v[112:115], v[152:155]\n"
    "s_add_u32 s88, s87, 0x2c000\n"
    "buffer_load_dwordx4 a[168:171], v2, s[24:27], s88 offen offset:0 sc1\n"
    "v_mfma_f32_16x16x32_bf16 v[152:155], a[24:27], v[40:43], v[152:155]\n"
    "v_exp_f32 v107, v107\n"
    "v_mfma_f32_16x16x32_bf16 v[156:159], a[28:31], v[40:43], v[156:159]\n"
    "buffer_load_dwordx4 a[172:175], v2, s[24:27], s88 offen offset:1024 sc1\n"
    "v_cvt_pk_bf16_f32 v53, v106, v107\n"
    "v_mfma_f32_16x16x32_bf16 v[156:159], a[60:63], v[112:115], v[156:159]\n"
    "s_waitcnt lgkmcnt(0)\n"
    "v_exp_f32 v108, v108\n"
    "v_mfma_f32_16x16x32_bf16 v[64:67], v[120:123], v[112:115], v[64:67]\n"
    "s_add_u32 m0, s65, 0\n"
    "s_xor_b32 s69, s89, 0\n"
    "buffer_load_dwordx4 v16, s[32:35], s69 offen offset:0 lds\n"
    "v_mfma_f32_16x16x32_bf16 v[96:99], a[192:195], a[128:131], v[80:83]\n"
    "ds_read_b128 a[224:227], v4 offset:24576\n"
    "ds_read_b128 a[228:231], v5 offset:24576\n"
    "v_mfma_f32_16x16x32_bf16 v[96:99], a[196:199], a[144:147], v[96:99]\n"
    "v_exp_f32 v109, v109\n"
    "v_mfma_f32_16x16x32_bf16 v[96:99], a[200:203], a[176:179], v[96:99]\n"
    "ds_read_b128 a[232:235], v6 offset:24576\n"
    "v_cvt_pk_bf16_f32 v54, v108, v109\n"
    "v_mfma_f32_16x16x32_bf16 v[96:99], a[204:207], v[56:59], v[96:99]\n"
    "v_exp_f32 v110, v110\n"
    "v_mfma_f32_16x16x32_bf16 v[100:103], a[220:223], v[56:59], v[80:83]\n"
    "ds_read_b128 a[236:239], v7 offset:24576\n"
    "ds_read_b128 a[240:243], v4 offset:28672\n"
    "v_mfma_f32_16x16x32_bf16 v[100:103], a[216:219], a[176:179], v[100:103]\n"
    "v_exp_f32 v111, v111\n"
    "v_mfma_f32_16x16x32_bf16 v[100:103], a[212:215], a[144:147], v[100:103]\n"
    "ds_read_b128 a[244:247], v5 offset:28672\n"
    "v_cvt_pk_bf16_f32 v55, v110, v111\n"
    "v_mfma_f32_16x16x32_bf16 v[100:103], a[208:211], a[128:131], v[100:103]\n"
    "v_exp_f32 v96, v96\n"
    "v_mfma_f32_16x16x32_bf16 v[192:195], a[0:3], v[48:51], v[192:195]\n"
    "ds_read_b128 a[248:251], v6 offset:28672\n"
    "ds_read_b128 a[252:255], v7 offset:28672\n"
    "v_mfma_f32_16x16x32_bf16 v[192:195], a[32:35], v[116:119], v[192:195]\n"
    "v_exp_f32 v97, v97\n"
    "v_mfma_f32_16x16x32_bf16 v[196:199], a[36:39], v[116:119], v[196:199]\n"
    "buffer_load_dwordx4 v17, s[32:35], s69 offen offset:1024 lds\n"
    "v_cvt_pk_bf16_f32 v112, v96, v97\n"
    "v_mfma_f32_16x16x32_bf16 v[196:199], a[4:7], v[48:51], v[196:199]\n"
    "v_exp_f32 v98, v98\n"
    "v_mfma_f32_16x16x32_bf16 v[200:203], a[8:11], v[48:51], v[200:203]\n"
    "buffer_load_dwordx4 v18, s[32:35], s69 offen offset:2048 lds\n"
    "v_mfma_f32_16x16x32_bf16 v[200:203], a[40:43], v[116:119], v[200:203]\n"
    "v_exp_f32 v99, v99\n"
    "v_mfma_f32_16x16x32_bf16 v[204:207], a[44:47], v[116:119], v[204:207]\n"
    "buffer_load_dwordx4 v19, s[32:35], s69 offen offset:3072 lds\n"
    "v_cvt_pk_bf16_f32 v113, v98, v99\n"
    "v_mfma_f32_16x16x32_bf16 v[204:207], a[12:15], v[48:51], v[204:207]\n"
    "v_exp_f32 v100, v100\n"
    "v_mfma_f32_16x16x32_bf16 v[72:75], v[120:123], v[48:51], v[72:75]\n"
    "v_mfma_f32_16x16x32_bf16 v[208:211], a[48:51], v[116:119], v[208:211]\n"
    "ds_read_b64_tr_b16 a[0:1], v8 offset:16384\n"
    "ds_read_b64_tr_b16 a[2:3], v8 offset:20480\n"
    "v_mfma_f32_16x16x32_bf16 v[208:211], a[16:19], v[48:51], v[208:211]\n"
    "ds_read_b64_tr_b16 a[4:5], v9 offset:16384\n"
    "ds_read_b64_tr_b16 a[6:7], v9 offset:20480\n"
    "v_mfma_f32_16x16x32_bf16 v[212:215], a[20:23], v[48:51], v[212:215]\n"
    "ds_read_b64_tr_b16 a[8:9], v8 offset:16896\n"
    "ds_read_b64_tr_b16 a[10:11], v8 offset:20992\n"
    "v_mfma_f32_16x16x32_bf16 v[212:215], a[52:55], v[116:119], v[212:215]\n"
    "ds_read_b64_tr_b16 a[12:13], v9 offset:16896\n"
    "ds_read_b64_tr_b16 a[14:15], v9 offset:20992\n"
    "v_mfma_f32_16x16x32_bf16 v[216:219], a[56:59], v[116:119], v[216:219]\n"
    "v_mfma_f32_16x16x32_bf16 v[216:219], a[24:27], v[48:51], v[216:219]\n"
    "v_mfma_f32_16x16x32_bf16 v[220:223], a[28:31], v[48:51], v[220:223]\n"
    "v_mfma_f32_16x16x32_bf16 v[220:223], a[60:63], v[116:119], v[220:223]\n"
    "v_mfma_f32_16x16x32_bf16 v[72:75], v[120:123], v[116:119], v[72:75]\n"
    "s_waitcnt lgkmcnt(8)\n"
    "v_mfma_f32_16x16x32_bf16 v[104:107], a[224:227], a[128:131], v[80:83]\n"
    "s_waitcnt vmcnt(20)\n"
    "ds_read_b64_tr_b16 a[16:17], v8 offset:17408\n"
    "ds_read_b64_tr_b16 a[18:19], v8 offset:21504\n"
    "v_mfma_f32_16x16x32_bf16 v[104:107], a[228:231], a[144:147], v[104:107]\n"
    "s_barrier\n"
    "v_exp_f32 v101, v101\n"
    "v_mfma_f32_16x16x32_bf16 v[104:107], a[232:235], a[176:179], v[104:107]\n"
    "ds_read_b64_tr_b16 a[20:21], v9 offset:17408\n"
    "v_cvt_pk_bf16_f32 v114, v100, v101\n"
    "v_mfma_f32_16x16x32_bf16 v[104:107], a[236:239], v[56:59], v[104:107]\n"
    "v_exp_f32 v102, v102\n"
    "v_mfma_f32_16x16x32_bf16 v[108:111], a[252:255], v[56:59], v[80:83]\n"
    "ds_read_b64_tr_b16 a[22:23], v9 offset:21504\n"
    "ds_read_b64_tr_b16 a[24:25], v8 offset:17920\n"
    "v_mfma_f32_16x16x32_bf16 v[108:111], a[248:251], a[176:179], v[108:111]\n"
    "v_exp_f32 v103, v103\n"
    "v_mfma_f32_16x16x32_bf16 v[108:111], a[244:247], a[144:147], v[108:111]\n"
    "ds_read_b64_tr_b16 a[26:27], v8 offset:22016\n"
    "v_cvt_pk_bf16_f32 v115, v102, v103\n"
    "v_mfma_f32_16x16x32_bf16 v[108:111], a[240:243], a[128:131], v[108:111]\n"
    "v_exp_f32 v104, v104\n"
    "v_mfma_f32_16x16x32_bf16 v[160:163], a[32:35], v[44:47], v[160:163]\n"
    "ds_read_b64_tr_b16 a[28:29], v9 offset:17920\n"
    "ds_read_b64_tr_b16 a[30:31], v9 offset:22016\n"
    "v_mfma_f32_16x16x32_bf16 v[224:227], a[32:35], v[52:55], v[224:227]\n"
    "v_exp_f32 v105, v105\n"
    "v_mfma_f32_16x16x32_bf16 v[228:231], a[36:39], v[52:55], v[228:231]\n"
    "s_add_u32 m0, s65, 0x14000\n"
    "s_xor_b32 s69, s89, 0x10000\n"
    "buffer_load_dwordx4 v16, s[32:35], s69 offen offset:0 lds\n"
    "v_cvt_pk_bf16_f32 v40, v104, v105\n"
    "v_mfma_f32_16x16x32_bf16 v[164:167], a[36:39], v[44:47], v[164:167]\n"
    "v_exp_f32 v106, v106\n"
    "v_mfma_f32_16x16x32_bf16 v[168:171], a[40:43], v[44:47], v[168:171]\n"
    "buffer_load_dwordx4 v17, s[32:35], s69 offen offset:1024 lds\n"
    "v_mfma_f32_16x16x32_bf16 v[232:235], a[40:43], v[52:55], v[232:235]\n"
    "v_exp_f32 v107, v107\n"
    "v_mfma_f32_16x16x32_bf16 v[236:239], a[44:47], v[52:55], v[236:239]\n"
    "buffer_load_dwordx4 v18, s[32:35], s69 offen offset:2048 lds\n"
    "v_cvt_pk_bf16_f32 v41, v106, v107\n"
    "v_mfma_f32_16x16x32_bf16 v[172:175], a[44:47], v[44:47], v[172:175]\n"
    "v_exp_f32 v108, v108\n"
    "v_mfma_f32_16x16x32_bf16 v[176:179], a[48:51], v[44:47], v[176:179]\n"
    "ds_read_b64_tr_b16 a[32:33], v8 offset:24576\n"
    "ds_read_b64_tr_b16 a[34:35], v8 offset:28672\n"
    "v_mfma_f32_16x16x32_bf16 v[240:243], a[48:51], v[52:55], v[240:243]\n"
    "ds_read_b64_tr_b16 a[36:37], v9 offset:24576\n"
    "ds_read_b64_tr_b16 a[38:39], v9 offset:28672\n"
    "v_mfma_f32_16x16x32_bf16 v[244:247], a[52:55], v[52:55], v[244:247]\n"
    "ds_read_b64_tr_b16 a[40:41], v8 offset:25088\n"
    "ds_read_b64_tr_b16 a[42:43], v8 offset:29184\n"
    "v_mfma_f32_16x16x32_bf16 v[180:183], a[52:55], v[44:47], v[180:183]\n"
    "ds_read_b64_tr_b16 a[44:45], v9 offset:25088\n"
    "ds_read_b64_tr_b16 a[46:47], v9 offset:29184\n"
    "v_mfma_f32_16x16x32_bf16 v[184:187], a[56:59], v[44:47], v[184:187]\n"
    "buffer_load_dwordx4 v19, s[32:35], s69 offen offset:3072 lds\n"
    "v_mfma_f32_16x16x32_bf16 v[248:251], a[56:59], v[52:55], v[248:251]\n"
    "v_mfma_f32_16x16x32_bf16 v[252:255], a[60:63], v[52:55], v[252:255]\n"
    "s_add_u32 m0, s65, 0x18000\n"
    "s_xor_b32 s69, s89, 0x20000\n"
    "buffer_load_dwordx4 v16, s[32:35], s69 offen offset:0 lds\n"
    "v_mfma_f32_16x16x32_bf16 v[188:191], a[60:63], v[44:47], v[188:191]\n"
    "v_mfma_f32_16x16x32_bf16 v[68:71], v[120:123], v[44:47], v[68:71]\n"
    "ds_read_b64_tr_b16 a[48:49], v8 offset:25600\n"
    "ds_read_b64_tr_b16 a[50:51], v8 offset:29696\n"
    "v_mfma_f32_16x16x32_bf16 v[76:79], v[120:123], v[52:55], v[76:79]\n"
    "ds_read_b64_tr_b16 a[52:53], v9 offset:25600\n"
    "ds_read_b64_tr_b16 a[54:55], v9 offset:29696\n"
    "v_mfma_f32_16x16x32_bf16 v[96:99], a[192:195], a[132:135], v[84:87]\n"
    "ds_read_b64_tr_b16 a[56:57], v8 offset:26112\n"
    "ds_read_b64_tr_b16 a[58:59], v8 offset:30208\n"
    "v_mfma_f32_16x16x32_bf16 v[96:99], a[196:199], a[148:151], v[96:99]\n"
    "v_exp_f32 v109, v109\n"
    "v_mfma_f32_16x16x32_bf16 v[96:99], a[200:203], a[180:183], v[96:99]\n"
    "ds_read_b64_tr_b16 a[60:61], v9 offset:26112\n"
    "v_cvt_pk_bf16_f32 v42, v108, v109\n"
    "v_mfma_f32_16x16x32_bf16 v[96:99], a[204:207], v[60:63], v[96:99]\n"
    "v_exp_f32 v110, v110\n"
    "v_mfma_f32_16x16x32_bf16 v[100:103], a[220:223], v[60:63], v[84:87]\n"
    "ds_read_b64_tr_b16 a[62:63], v9 offset:30208\n"
    "buffer_load_dwordx4 v17, s[32:35], s69 offen offset:1024 lds\n"
    "v_mfma_f32_16x16x32_bf16 v[100:103], a[216:219], a[180:183], v[100:103]\n"
    "v_exp_f32 v111, v111\n"
    "v_mfma_f32_16x16x32_bf16 v[100:103], a[212:215], a[148:151], v[100:103]\n"
    "buffer_load_dwordx4 v18, s[32:35], s69 offen offset:2048 lds\n"
    "v_cvt_pk_bf16_f32 v43, v110, v111\n"
    "v_mfma_f32_16x16x32_bf16 v[100:103], a[208:211], a[132:135], v[100:103]\n"
    "s_waitcnt lgkmcnt(8)\n"
    "v_exp_f32 v96, v96\n"
    "v_mfma_f32_16x16x32_bf16 v[128:131], a[0:3], v[112:115], v[128:131]\n"
    "buffer_load_dwordx4 v19, s[32:35], s69 offen offset:3072 lds\n"
    "v_mfma_f32_16x16x32_bf16 v[128:131], a[32:35], v[40:43], v[128:131]\n"
    "v_exp_f32 v97, v97\n"
    "v_mfma_f32_16x16x32_bf16 v[132:135], a[36:39], v[40:43], v[132:135]\n"
    "s_add_u32 s87, s47, 0x2800\n"
    "buffer_load_dwordx4 v[44:47], v2, s[24:27], s87 offen offset:0 sc1\n"
    "v_cvt_pk_bf16_f32 v116, v96, v97\n"
    "v_mfma_f32_16x16x32_bf16 v[132:135], a[4:7], v[112:115], v[132:135]\n"
    "v_exp_f32 v98, v98\n"
    "v_mfma_f32_16x16x32_bf16 v[136:139], a[8:11], v[112:115], v[136:139]\n"
    "buffer_load_dwordx4 v[48:51], v2, s[24:27], s87 offen offset:1024 sc1\n"
    "v_mfma_f32_16x16x32_bf16 v[136:139], a[40:43], v[40:43], v[136:139]\n"
    "v_exp_f32 v99, v99\n"
    "v_mfma_f32_16x16x32_bf16 v[140:143], a[44:47], v[40:43], v[140:143]\n"
    "s_add_u32 s88, s87, 0x2c000\n"
    "buffer_load_dwordx4 v[52:55], v2, s[24:27], s88 offen offset:0 sc1\n"
    "v_cvt_pk_bf16_f32 v117, v98, v99\n"
    "v_mfma_f32_16x16x32_bf16 v[140:143], a[12:15], v[112:115], v[140:143]\n"
    "s_waitcnt lgkmcnt(0)\n"
    "v_exp_f32 v100, v100\n"
    "v_mfma_f32_16x16x32_bf16 v[144:147], a[16:19], v[112:115], v[144:147]\n"
    "buffer_load_dwordx4 v[80:83], v2, s[24:27], s88 offen offset:1024 sc1\n"
    "v_mfma_f32_16x16x32_bf16 v[144:147], a[48:51], v[40:43], v[144:147]\n"
    "v_mfma_f32_16x16x32_bf16 v[148:151], a[52:55], v[40:43], v[148:151]\n"
    "v_mfma_f32_16x16x32_bf16 v[148:151], a[20:23], v[112:115], v[148:151]\n"
    "v_mfma_f32_16x16x32_bf16 v[152:155], a[24:27], v[112:115], v[152:155]\n"
    "v_xor_b32 v4, 0x10000, v4\n"
    "v_xor_b32 v5, 0x10000, v5\n"
    "v_mfma_f32_16x16x32_bf16 v[152:155], a[56:59], v[40:43], v[152:155]\n"
    "v_xor_b32 v6, 0x10000, v6\n"
    "v_xor_b32 v7, 0x10000, v7\n"
    "v_mfma_f32_16x16x32_bf16 v[156:159], a[60:63], v[40:43], v[156:159]\n"
    "v_xor_b32 v8, 0x10000, v8\n"
    "v_xor_b32 v9, 0x10000, v9\n"
    "v_mfma_f32_16x16x32_bf16 v[156:159], a[28:31], v[112:115], v[156:159]\n"
    "v_mfma_f32_16x16x32_bf16 v[64:67], v[120:123], v[112:115], v[64:67]\n"
    "v_mfma_f32_16x16x32_bf16 v[64:67], v[120:123], v[40:43], v[64:67]\n"
    "v_mfma_f32_16x16x32_bf16 v[104:107], a[224:227], a[132:135], v[84:87]\n"
    "v_mfma_f32_16x16x32_bf16 v[104:107], a[228:231], a[148:151], v[104:107]\n"
    "s_barrier\n"
    "v_exp_f32 v101, v101\n"
    "v_mfma_f32_16x16x32_bf16 v[104:107], a[232:235], a[180:183], v[104:107]\n"
    "v_cvt_pk_bf16_f32 v118, v100, v101\n"
    "v_mfma_f32_16x16x32_bf16 v[104:107], a[236:239], v[60:63], v[104:107]\n"
    "v_exp_f32 v102, v102\n"
    "v_rcp_f32 v26, v64\n"
    "v_mfma_f32_16x16x32_bf16 v[108:111], a[252:255], v[60:63], v[84:87]\n"
    "v_cmp_nlt_f32 s[76:77], v64, s73\n"
    "v_mul_f32 v128, v26, v128\n"
    "v_mul_f32 v129, v26, v129\n"
    "v_mfma_f32_16x16x32_bf16 v[108:111], a[248:251], a[180:183], v[108:111]\n"
    "v_exp_f32 v103, v103\n"
    "v_mul_f32 v130, v26, v130\n"
    "v_mfma_f32_16x16x32_bf16 v[108:111], a[244:247], a[148:151], v[108:111]\n"
    "v_cvt_pk_bf16_f32 v119, v102, v103\n"
    "v_mul_f32 v131, v26, v131\n"
    "v_mul_f32 v132, v26, v132\n"
    "v_mfma_f32_16x16x32_bf16 v[108:111], a[240:243], a[132:135], v[108:111]\n"
    "v_exp_f32 v104, v104\n"
    "v_mul_f32 v133, v26, v133\n"
    "v_mfma_f32_16x16x32_bf16 v[160:163], a[0:3], v[116:119], v[160:163]\n"
    "v_mul_f32 v134, v26, v134\n"
    "v_mul_f32 v135, v26, v135\n"
    "v_cvt_pk_bf16_f32 v128, v128, v129\n"
    "v_mfma_f32_16x16x32_bf16 v[164:167], a[4:7], v[116:119], v[164:167]\n"
    "v_exp_f32 v105, v105\n"
    "v_cvt_pk_bf16_f32 v129, v130, v131\n"
    "v_mfma_f32_16x16x32_bf16 v[168:171], a[8:11], v[116:119], v[168:171]\n"
    "v_cvt_pk_bf16_f32 v112, v104, v105\n"
    "v_cvt_pk_bf16_f32 v130, v132, v133\n"
    "v_cvt_pk_bf16_f32 v131, v134, v135\n"
    "v_mfma_f32_16x16x32_bf16 v[172:175], a[12:15], v[116:119], v[172:175]\n"
    "s_add_u32 s68, s41, s58\n"
    "buffer_store_dwordx4 v[128:131], v2, s[24:27], s68 offen offset:0 sc1\n"
    "v_exp_f32 v106, v106\n"
    "v_mfma_f32_16x16x32_bf16 v[176:179], a[16:19], v[116:119], v[176:179]\n"
    "v_mul_f32 v136, v26, v136\n"
    "v_mul_f32 v137, v26, v137\n"
    "v_mul_f32 v138, v26, v138\n"
    "v_mfma_f32_16x16x32_bf16 v[180:183], a[20:23], v[116:119], v[180:183]\n"
    "v_exp_f32 v107, v107\n"
    "v_mul_f32 v139, v26, v139\n"
    "v_mfma_f32_16x16x32_bf16 v[184:187], a[24:27], v[116:119], v[184:187]\n"
    "v_cvt_pk_bf16_f32 v113, v106, v107\n"
    "v_mul_f32 v140, v26, v140\n"
    "v_mul_f32 v141, v26, v141\n"
    "v_mfma_f32_16x16x32_bf16 v[188:191], a[28:31], v[116:119], v[188:191]\n"
    "v_exp_f32 v108, v108\n"
    "v_mul_f32 v142, v26, v142\n"
    "v_mfma_f32_16x16x32_bf16 v[68:71], v[120:123], v[116:119], v[68:71]\n"
    "v_mul_f32 v143, v26, v143\n"
    "v_cvt_pk_bf16_f32 v136, v136, v137\n"
    "v_cvt_pk_bf16_f32 v137, v138, v139\n"
    "v_mfma_f32_16x16x32_bf16 v[96:99], a[192:195], a[136:139], v[88:91]\n"
    "v_cvt_pk_bf16_f32 v138, v140, v141\n"
    "v_cvt_pk_bf16_f32 v139, v142, v143\n"
    "v_mfma_f32_16x16x32_bf16 v[96:99], a[196:199], a[152:155], v[96:99]\n"
    "buffer_store_dwordx4 v[136:139], v2, s[24:27], s68 offen offset:1024 sc1\n"
    "v_exp_f32 v109, v109\n"
    "v_mfma_f32_16x16x32_bf16 v[96:99], a[200:203], a[184:187], v[96:99]\n"
    "v_cvt_pk_bf16_f32 v114, v108, v109\n"
    "v_mul_f32 v144, v26, v144\n"
    "v_mul_f32 v145, v26, v145\n"
    "v_mfma_f32_16x16x32_bf16 v[96:99], a[204:207], v[124:127], v[96:99]\n"
    "v_exp_f32 v110, v110\n"
    "v_mul_f32 v146, v26, v146\n"
    "v_mfma_f32_16x16x32_bf16 v[100:103], a[220:223], v[124:127], v[88:91]\n"
    "v_mul_f32 v147, v26, v147\n"
    "v_mul_f32 v148, v26, v148\n"
    "v_mul_f32 v149, v26, v149\n"
    "v_mfma_f32_16x16x32_bf16 v[100:103], a[216:219], a[184:187], v[100:103]\n"
    "v_exp_f32 v111, v111\n"
    "v_mul_f32 v150, v26, v150\n"
    "v_mfma_f32_16x16x32_bf16 v[100:103], a[212:215], a[152:155], v[100:103]\n"
    "v_cvt_pk_bf16_f32 v115, v110, v111\n"
    "v_mul_f32 v151, v26, v151\n"
    "v_cvt_pk_bf16_f32 v144, v144, v145\n"
    "v_mfma_f32_16x16x32_bf16 v[100:103], a[208:211], a[136:139], v[100:103]\n"
    "v_exp_f32 v96, v96\n"
    "v_cvt_pk_bf16_f32 v145, v146, v147\n"
    "v_mfma_f32_16x16x32_bf16 v[160:163], a[32:35], v[112:115], v[160:163]\n"
    "v_cvt_pk_bf16_f32 v146, v148, v149\n"
    "v_cvt_pk_bf16_f32 v147, v150, v151\n"
    "v_mfma_f32_16x16x32_bf16 v[164:167], a[36:39], v[112:115], v[164:167]\n"
    "buffer_store_dwordx4 v[144:147], v2, s[24:27], s68 offen offset:2048 sc1\n"
    "v_exp_f32 v97, v97\n"
    "v_mfma_f32_16x16x32_bf16 v[168:171], a[40:43], v[112:115], v[168:171]\n"
    "v_cvt_pk_bf16_f32 v40, v96, v97\n"
    "v_mul_f32 v152, v26, v152\n"
    "v_mul_f32 v153, v26, v153\n"
    "v_mfma_f32_16x16x32_bf16 v[172:175], a[44:47], v[112:115], v[172:175]\n"
    "v_exp_f32 v98, v98\n"
    "v_mul_f32 v154, v26, v154\n"
    "v_mfma_f32_16x16x32_bf16 v[176:179], a[48:51], v[112:115], v[176:179]\n"
    "v_mul_f32 v155, v26, v155\n"
    "v_mul_f32 v156, v26, v156\n"
    "v_mul_f32 v157, v26, v157\n"
    "v_mfma_f32_16x16x32_bf16 v[180:183], a[52:55], v[112:115], v[180:183]\n"
    "v_exp_f32 v99, v99\n"
    "v_mul_f32 v158, v26, v158\n"
    "v_mfma_f32_16x16x32_bf16 v[184:187], a[56:59], v[112:115], v[184:187]\n"
    "v_cvt_pk_bf16_f32 v41, v98, v99\n"
    "v_mul_f32 v159, v26, v159\n"
    "v_cvt_pk_bf16_f32 v152, v152, v153\n"
    "v_mfma_f32_16x16x32_bf16 v[188:191], a[60:63], v[112:115], v[188:191]\n"
    "v_exp_f32 v100, v100\n"
    "v_cvt_pk_bf16_f32 v153, v154, v155\n"
    "v_mfma_f32_16x16x32_bf16 v[68:71], v[120:123], v[112:115], v[68:71]\n"
    "v_cvt_pk_bf16_f32 v154, v156, v157\n"
    "v_cvt_pk_bf16_f32 v155, v158, v159\n"
    "buffer_store_dwordx4 v[152:155], v2, s[24:27], s68 offen offset:3072 sc1\n"
    "v_mfma_f32_16x16x32_bf16 v[104:107], a[224:227], a[136:139], v[88:91]\n"
    "v_mfma_f32_16x16x32_bf16 v[104:107], a[228:231], a[152:155], v[104:107]\n"
    "v_exp_f32 v101, v101\n"
    "v_mfma_f32_16x16x32_bf16 v[104:107], a[232:235], a[184:187], v[104:107]\n"
    "v_cvt_pk_bf16_f32 v42, v100, v101\n"
    "v_mfma_f32_16x16x32_bf16 v[104:107], a[236:239], v[124:127], v[104:107]\n"
    "v_exp_f32 v102, v102\n"
    "v_rcp_f32 v27, v68\n"
    "v_mfma_f32_16x16x32_bf16 v[108:111], a[252:255], v[124:127], v[88:91]\n"
    "v_cmp_nlt_f32 s[78:79], v68, s73\n"
    "v_mul_f32 v160, v27, v160\n"
    "v_mul_f32 v161, v27, v161\n"
    "v_mfma_f32_16x16x32_bf16 v[108:111], a[248:251], a[184:187], v[108:111]\n"
    "v_exp_f32 v103, v103\n"
    "v_mul_f32 v162, v27, v162\n"
    "v_mfma_f32_16x16x32_bf16 v[108:111], a[244:247], a[152:155], v[108:111]\n"
    "v_cvt_pk_bf16_f32 v43, v102, v103\n"
    "v_mul_f32 v163, v27, v163\n"
    "v_mul_f32 v164, v27, v164\n"
    "v_mfma_f32_16x16x32_bf16 v[108:111], a[240:243], a[136:139], v[108:111]\n"
    "v_exp_f32 v104, v104\n"
    "v_mul_f32 v165, v27, v165\n"
    "v_mfma_f32_16x16x32_bf16 v[192:195], a[0:3], v[40:43], v[192:195]\n"
    "v_mul_f32 v166, v27, v166\n"
    "v_mul_f32 v167, v27, v167\n"
    "v_cvt_pk_bf16_f32 v160, v160, v161\n"
    "v_mfma_f32_16x16x32_bf16 v[196:199], a[4:7], v[40:43], v[196:199]\n"
    "v_exp_f32 v105, v105\n"
    "v_cvt_pk_bf16_f32 v161, v162, v163\n"
    "v_mfma_f32_16x16x32_bf16 v[200:203], a[8:11], v[40:43], v[200:203]\n"
    "v_cvt_pk_bf16_f32 v116, v104, v105\n"
    "v_cvt_pk_bf16_f32 v162, v164, v165\n"
    "v_cvt_pk_bf16_f32 v163, v166, v167\n"
    "v_mfma_f32_16x16x32_bf16 v[204:207], a[12:15], v[40:43], v[204:207]\n"
    "s_add_u32 s68, s41, s59\n"
    "buffer_store_dwordx4 v[160:163], v2, s[24:27], s68 offen offset:0 sc1\n"
    "v_exp_f32 v106, v106\n"
    "v_mfma_f32_16x16x32_bf16 v[208:211], a[16:19], v[40:43], v[208:211]\n"
    "v_mul_f32 v168, v27, v168\n"
    "v_mul_f32 v169, v27, v169\n"
    "v_mul_f32 v170, v27, v170\n"
    "v_mfma_f32_16x16x32_bf16 v[212:215], a[20:23], v[40:43], v[212:215]\n"
    "v_exp_f32 v107, v107\n"
    "v_mul_f32 v171, v27, v171\n"
    "v_mfma_f32_16x16x32_bf16 v[216:219], a[24:27], v[40:43], v[216:219]\n"
    "v_cvt_pk_bf16_f32 v117, v106, v107\n"
    "v_mul_f32 v172, v27, v172\n"
    "v_mul_f32 v173, v27, v173\n"
    "v_mfma_f32_16x16x32_bf16 v[220:223], a[28:31], v[40:43], v[220:223]\n"
    "v_exp_f32 v108, v108\n"
    "v_mul_f32 v174, v27, v174\n"
    "v_mfma_f32_16x16x32_bf16 v[72:75], v[120:123], v[40:43], v[72:75]\n"
    "v_mul_f32 v175, v27, v175\n"
    "v_cvt_pk_bf16_f32 v168, v168, v169\n"
    "v_cvt_pk_bf16_f32 v169, v170, v171\n"
    "v_mfma_f32_16x16x32_bf16 v[96:99], a[192:195], a[140:143], v[92:95]\n"
    "v_cvt_pk_bf16_f32 v170, v172, v173\n"
    "v_cvt_pk_bf16_f32 v171, v174, v175\n"
    "v_mfma_f32_16x16x32_bf16 v[96:99], a[196:199], a[156:159], v[96:99]\n"
    "buffer_store_dwordx4 v[168:171], v2, s[24:27], s68 offen offset:1024 sc1\n"
    "v_exp_f32 v109, v109\n"
    "v_mfma_f32_16x16x32_bf16 v[96:99], a[200:203], a[188:191], v[96:99]\n"
    "v_cvt_pk_bf16_f32 v118, v108, v109\n"
    "v_mul_f32 v176, v27, v176\n"
    "v_mul_f32 v177, v27, v177\n"
    "v_mfma_f32_16x16x32_bf16 v[96:99], a[204:207], v[28:31], v[96:99]\n"
    "v_exp_f32 v110, v110\n"
    "v_mul_f32 v178, v27, v178\n"
    "v_mfma_f32_16x16x32_bf16 v[100:103], a[220:223], v[28:31], v[92:95]\n"
    "v_mul_f32 v179, v27, v179\n"
    "v_mul_f32 v180, v27, v180\n"
    "v_mul_f32 v181, v27, v181\n"
    "v_mfma_f32_16x16x32_bf16 v[100:103], a[216:219], a[188:191], v[100:103]\n"
    "v_exp_f32 v111, v111\n"
    "v_mul_f32 v182, v27, v182\n"
    "v_mfma_f32_16x16x32_bf16 v[100:103], a[212:215], a[156:159], v[100:103]\n"
    "v_cvt_pk_bf16_f32 v119, v110, v111\n"
    "v_mul_f32 v183, v27, v183\n"
    "v_cvt_pk_bf16_f32 v176, v176, v177\n"
    "v_mfma_f32_16x16x32_bf16 v[100:103], a[208:211], a[140:143], v[100:103]\n"
    "v_exp_f32 v96, v96\n"
    "v_cvt_pk_bf16_f32 v177, v178, v179\n"
    "v_mfma_f32_16x16x32_bf16 v[192:195], a[32:35], v[116:119], v[192:195]\n"
    "v_cvt_pk_bf16_f32 v178, v180, v181\n"
    "v_cvt_pk_bf16_f32 v179, v182, v183\n"
    "v_mfma_f32_16x16x32_bf16 v[196:199], a[36:39], v[116:119], v[196:199]\n"
    "buffer_store_dwordx4 v[176:179], v2, s[24:27], s68 offen offset:2048 sc1\n"
    "v_exp_f32 v97, v97\n"
    "v_mfma_f32_16x16x32_bf16 v[200:203], a[40:43], v[116:119], v[200:203]\n"
    "v_cvt_pk_bf16_f32 v112, v96, v97\n"
    "v_mul_f32 v184, v27, v184\n"
    "v_mul_f32 v185, v27, v185\n"
    "v_mfma_f32_16x16x32_bf16 v[204:207], a[44:47], v[116:119], v[204:207]\n"
    "v_exp_f32 v98, v98\n"
    "v_mul_f32 v186, v27, v186\n"
    "v_mfma_f32_16x16x32_bf16 v[208:211], a[48:51], v[116:119], v[208:211]\n"
    "v_mul_f32 v187, v27, v187\n"
    "v_mul_f32 v188, v27, v188\n"
    "v_mul_f32 v189, v27, v189\n"
    "v_mfma_f32_16x16x32_bf16 v[212:215], a[52:55], v[116:119], v[212:215]\n"
    "v_exp_f32 v99, v99\n"
    "v_mul_f32 v190, v27, v190\n"
    "v_mfma_f32_16x16x32_bf16 v[216:219], a[56:59], v[116:119], v[216:219]\n"
    "v_cvt_pk_bf16_f32 v113, v98, v99\n"
    "v_mul_f32 v191, v27, v191\n"
    "v_cvt_pk_bf16_f32 v184, v184, v185\n"
    "v_mfma_f32_16x16x32_bf16 v[220:223], a[60:63], v[116:119], v[220:223]\n"
    "v_exp_f32 v100, v100\n"
    "v_cvt_pk_bf16_f32 v185, v186, v187\n"
    "v_mfma_f32_16x16x32_bf16 v[72:75], v[120:123], v[116:119], v[72:75]\n"
    "v_cvt_pk_bf16_f32 v186, v188, v189\n"
    "v_cvt_pk_bf16_f32 v187, v190, v191\n"
    "buffer_store_dwordx4 v[184:187], v2, s[24:27], s68 offen offset:3072 sc1\n"
    "v_mfma_f32_16x16x32_bf16 v[104:107], a[224:227], a[140:143], v[92:95]\n"
    "v_mfma_f32_16x16x32_bf16 v[104:107], a[228:231], a[156:159], v[104:107]\n"
    "v_exp_f32 v101, v101\n"
    "v_mfma_f32_16x16x32_bf16 v[104:107], a[232:235], a[188:191], v[104:107]\n"
    "v_cvt_pk_bf16_f32 v114, v100, v101\n"
    "v_mfma_f32_16x16x32_bf16 v[104:107], a[236:239], v[28:31], v[104:107]\n"
    "v_exp_f32 v102, v102\n"
    "v_rcp_f32 v38, v72\n"
    "v_mfma_f32_16x16x32_bf16 v[108:111], a[252:255], v[28:31], v[92:95]\n"
    "v_cmp_nlt_f32 s[80:81], v72, s73\n"
    "v_mul_f32 v192, v38, v192\n"
    "v_mul_f32 v193, v38, v193\n"
    "v_mfma_f32_16x16x32_bf16 v[108:111], a[248:251], a[188:191], v[108:111]\n"
    "v_exp_f32 v103, v103\n"
    "v_mul_f32 v194, v38, v194\n"
    "v_mfma_f32_16x16x32_bf16 v[108:111], a[244:247], a[156:159], v[108:111]\n"
    "v_cvt_pk_bf16_f32 v115, v102, v103\n"
    "v_mul_f32 v195, v38, v195\n"
    "v_mul_f32 v196, v38, v196\n"
    "v_mfma_f32_16x16x32_bf16 v[108:111], a[240:243], a[140:143], v[108:111]\n"
    "v_exp_f32 v104, v104\n"
    "v_mul_f32 v197, v38, v197\n"
    "v_mfma_f32_16x16x32_bf16 v[224:227], a[0:3], v[112:115], v[224:227]\n"
    "v_mul_f32 v198, v38, v198\n"
    "v_mul_f32 v199, v38, v199\n"
    "v_cvt_pk_bf16_f32 v192, v192, v193\n"
    "v_mfma_f32_16x16x32_bf16 v[228:231], a[4:7], v[112:115], v[228:231]\n"
    "v_exp_f32 v105, v105\n"
    "v_cvt_pk_bf16_f32 v193, v194, v195\n"
    "v_mfma_f32_16x16x32_bf16 v[232:235], a[8:11], v[112:115], v[232:235]\n"
    "v_cvt_pk_bf16_f32 v40, v104, v105\n"
    "v_cvt_pk_bf16_f32 v194, v196, v197\n"
    "v_cvt_pk_bf16_f32 v195, v198, v199\n"
    "v_mfma_f32_16x16x32_bf16 v[236:239], a[12:15], v[112:115], v[236:239]\n"
    "s_add_u32 s68, s41, s62\n"
    "buffer_store_dwordx4 v[192:195], v2, s[24:27], s68 offen offset:0 sc1\n"
    "v_exp_f32 v106, v106\n"
    "v_mfma_f32_16x16x32_bf16 v[240:243], a[16:19], v[112:115], v[240:243]\n"
    "v_mul_f32 v200, v38, v200\n"
    "v_mul_f32 v201, v38, v201\n"
    "v_mul_f32 v202, v38, v202\n"
    "v_mfma_f32_16x16x32_bf16 v[244:247], a[20:23], v[112:115], v[244:247]\n"
    "v_exp_f32 v107, v107\n"
    "v_mul_f32 v203, v38, v203\n"
    "v_mfma_f32_16x16x32_bf16 v[248:251], a[24:27], v[112:115], v[248:251]\n"
    "v_cvt_pk_bf16_f32 v41, v106, v107\n"
    "v_mul_f32 v204, v38, v204\n"
    "v_mul_f32 v205, v38, v205\n"
    "v_mfma_f32_16x16x32_bf16 v[252:255], a[28:31], v[112:115], v[252:255]\n"
    "v_exp_f32 v108, v108\n"
    "v_mul_f32 v206, v38, v206\n"
    "v_mfma_f32_16x16x32_bf16 v[76:79], v[120:123], v[112:115], v[76:79]\n"
    "v_mul_f32 v207, v38, v207\n"
    "v_cvt_pk_bf16_f32 v200, v200, v201\n"
    "v_cvt_pk_bf16_f32 v201, v202, v203\n"
    "v_exp_f32 v109, v109\n"
    "s_nop 0\n"
    "v_cvt_pk_bf16_f32 v42, v108, v109\n"
    "v_exp_f32 v110, v110\n"
    "v_exp_f32 v111, v111\n"
    "s_nop 0\n"
    "v_cvt_pk_bf16_f32 v43, v110, v111\n"
    "s_nop 1\n"
    "v_mfma_f32_16x16x32_bf16 v[224:227], a[32:35], v[40:43], v[224:227]\n"
    "v_cvt_pk_bf16_f32 v202, v204, v205\n"
    "v_cvt_pk_bf16_f32 v203, v206, v207\n"
    "v_mfma_f32_16x16x32_bf16 v[228:231], a[36:39], v[40:43], v[228:231]\n"
    "buffer_store_dwordx4 v[200:203], v2, s[24:27], s68 offen offset:1024 sc1\n"
    "v_mul_f32 v208, v38, v208\n"
    "v_mfma_f32_16x16x32_bf16 v[232:235], a[40:43], v[40:43], v[232:235]\n"
    "v_mul_f32 v209, v38, v209\n"
    "v_mul_f32 v210, v38, v210\n"
    "v_mul_f32 v211, v38, v211\n"
    "v_mfma_f32_16x16x32_bf16 v[236:239], a[44:47], v[40:43], v[236:239]\n"
    "v_mul_f32 v212, v38, v212\n"
    "v_mul_f32 v213, v38, v213\n"
    "v_mul_f32 v214, v38, v214\n"
    "v_mfma_f32_16x16x32_bf16 v[240:243], a[48:51], v[40:43], v[240:243]\n"
    "v_mul_f32 v215, v38, v215\n"
    "v_cvt_pk_bf16_f32 v208, v208, v209\n"
    "v_cvt_pk_bf16_f32 v209, v210, v211\n"
    "v_mfma_f32_16x16x32_bf16 v[244:247], a[52:55], v[40:43], v[244:247]\n"
    "v_cvt_pk_bf16_f32 v210, v212, v213\n"
    "v_cvt_pk_bf16_f32 v211, v214, v215\n"
    "v_mfma_f32_16x16x32_bf16 v[248:251], a[56:59], v[40:43], v[248:251]\n"
    "buffer_store_dwordx4 v[208:211], v2, s[24:27], s68 offen offset:2048 sc1\n"
    "v_mul_f32 v216, v38, v216\n"
    "v_mfma_f32_16x16x32_bf16 v[252:255], a[60:63], v[40:43], v[252:255]\n"
    "v_mul_f32 v217, v38, v217\n"
    "v_mul_f32 v218, v38, v218\n"
    "v_mul_f32 v219, v38, v219\n"
    "v_mfma_f32_16x16x32_bf16 v[76:79], v[120:123], v[40:43], v[76:79]\n"
    "v_mul_f32 v220, v38, v220\n"
    "v_mul_f32 v221, v38, v221\n"
    "v_mul_f32 v222, v38, v222\n"
    "v_mul_f32 v223, v38, v223\n"
    "v_cvt_pk_bf16_f32 v216, v216, v217\n"
    "v_cvt_pk_bf16_f32 v217, v218, v219\n"
    "v_cvt_pk_bf16_f32 v218, v220, v221\n"
    "v_cvt_pk_bf16_f32 v219, v222, v223\n"
    "buffer_store_dwordx4 v[216:219], v2, s[24:27], s68 offen offset:3072 sc1\n"
    "s_nop 1\n"
    "v_add_u32 v22, 0xffff8000, v8\n"
    "v_add_u32 v23, 0xffff8000, v9\n"
    "v_add_u32 v24, 0x8000, v8\n"
    "v_add_u32 v25, 0x8000, v9\n"
    "v_add_u32 v26, 0x18000, v8\n"
    "v_add_u32 v27, 0x18000, v9\n"
    "v_rcp_f32 v96, v76\n"
    "v_cmp_nlt_f32 s[82:83], v76, s73\n"
    "v_pk_mul_f32 v[224:225], v[224:225], v[96:97] op_sel_hi:[1,0]\n"
    "v_pk_mul_f32 v[226:227], v[226:227], v[96:97] op_sel_hi:[1,0]\n"
    "v_pk_mul_f32 v[228:229], v[228:229], v[96:97] op_sel_hi:[1,0]\n"
    "v_pk_mul_f32 v[230:231], v[230:231], v[96:97] op_sel_hi:[1,0]\n"
    "v_cvt_pk_bf16_f32 v224, v224, v225\n"
    "v_cvt_pk_bf16_f32 v225, v226, v227\n"
    "v_cvt_pk_bf16_f32 v226, v228, v229\n"
    "v_cvt_pk_bf16_f32 v227, v230, v231\n"
    "s_add_u32 s68, s41, s63\n"
    "buffer_store_dwordx4 v[224:227], v2, s[24:27], s68 offen offset:0 sc1\n"
    "ds_read_b64_tr_b16 a[0:1], v26 offset:0\n"
    "ds_read_b64_tr_b16 a[2:3], v26 offset:4096\n"
    "ds_read_b64_tr_b16 a[4:5], v26 offset:8192\n"
    "ds_read_b64_tr_b16 a[6:7], v26 offset:12288\n"
    "v_pk_mul_f32 v[232:233], v[232:233], v[96:97] op_sel_hi:[1,0]\n"
    "v_pk_mul_f32 v[234:235], v[234:235], v[96:97] op_sel_hi:[1,0]\n"
    "v_pk_mul_f32 v[236:237], v[236:237], v[96:97] op_sel_hi:[1,0]\n"
    "v_pk_mul_f32 v[238:239], v[238:239], v[96:97] op_sel_hi:[1,0]\n"
    "v_cvt_pk_bf16_f32 v232, v232, v233\n"
    "v_cvt_pk_bf16_f32 v233, v234, v235\n"
    "v_cvt_pk_bf16_f32 v234, v236, v237\n"
    "v_cvt_pk_bf16_f32 v235, v238, v239\n"
    "buffer_store_dwordx4 v[232:235], v2, s[24:27], s68 offen offset:1024 sc1\n"
    "ds_read_b64_tr_b16 a[8:9], v27 offset:0\n"
    "ds_read_b64_tr_b16 a[10:11], v27 offset:4096\n"
    "ds_read_b64_tr_b16 a[12:13], v27 offset:8192\n"
    "ds_read_b64_tr_b16 a[14:15], v27 offset:12288\n"
    "v_pk_mul_f32 v[240:241], v[240:241], v[96:97] op_sel_hi:[1,0]\n"
    "v_pk_mul_f32 v[242:243], v[242:243], v[96:97] op_sel_hi:[1,0]\n"
    "v_pk_mul_f32 v[244:245], v[244:245], v[96:97] op_sel_hi:[1,0]\n"
    "v_pk_mul_f32 v[246:247], v[246:247], v[96:97] op_sel_hi:[1,0]\n"
    "v_cvt_pk_bf16_f32 v240, v240, v241\n"
    "v_cvt_pk_bf16_f32 v241, v242, v243\n"
    "v_cvt_pk_bf16_f32 v242, v244, v245\n"
    "v_cvt_pk_bf16_f32 v243, v246, v247\n"
    "buffer_store_dwordx4 v[240:243], v2, s[24:27], s68 offen offset:2048 sc1\n"
    "ds_read_b64_tr_b16 a[16:17], v26 offset:512\n"
    "ds_read_b64_tr_b16 a[18:19], v26 offset:4608\n"
    "ds_read_b64_tr_b16 a[20:21], v26 offset:8704\n"
    "ds_read_b64_tr_b16 a[22:23], v26 offset:12800\n"
    "v_pk_mul_f32 v[248:249], v[248:249], v[96:97] op_sel_hi:[1,0]\n"
    "v_pk_mul_f32 v[250:251], v[250:251], v[96:97] op_sel_hi:[1,0]\n"
    "v_pk_mul_f32 v[252:253], v[252:253], v[96:97] op_sel_hi:[1,0]\n"
    "v_pk_mul_f32 v[254:255], v[254:255], v[96:97] op_sel_hi:[1,0]\n"
    "v_cvt_pk_bf16_f32 v248, v248, v249\n"
    "v_cvt_pk_bf16_f32 v249, v250, v251\n"
    "v_cvt_pk_bf16_f32 v250, v252, v253\n"
    "v_cvt_pk_bf16_f32 v251, v254, v255\n"
    "buffer_store_dwordx4 v[248:251], v2, s[24:27], s68 offen offset:3072 sc1\n"
    "s_or_b64 s[76:77], s[76:77], s[78:79]\n"
    "s_or_b64 s[80:81], s[80:81], s[82:83]\n"
    "s_or_b64 s[76:77], s[76:77], s[80:81]\n"
    "s_cselect_b32 s68, 1, 0\n"
    "v_mov_b32 v36, s68\n"
    "s_lshl_b32 s68, s64, 4\n"
    "s_add_u32 s68, s68, 0x1c000\n"
    "v_mov_b32 v37, s68\n"
    "ds_write_b32 v37, v36 offset:0\n"
    "v_and_b32 v37, 3, v1\n"
    "v_lshlrev_b32 v37, 4, v37\n"
    "v_add_u32 v37, 0x1c000, v37\n"
    "s_waitcnt lgkmcnt(9)\n"
    "v_mfma_f32_16x16x32_bf16 v[128:131], a[0:3], a[64:67], 0\n"
    "ds_read_b64_tr_b16 a[24:25], v27 offset:512\n"
    "v_mfma_f32_16x16x32_bf16 v[128:131], a[4:7], a[68:71], v[128:131]\n"
    "ds_read_b64_tr_b16 a[26:27], v27 offset:4608\n"
    "v_mfma_f32_16x16x32_bf16 v[160:163], a[4:7], a[76:79], 0\n"
    "ds_read_b64_tr_b16 a[28:29], v27 offset:8704\n"
    "v_mfma_f32_16x16x32_bf16 v[160:163], a[0:3], a[72:75], v[160:163]\n"
    "ds_read_b64_tr_b16 a[30:31], v27 offset:12800\n"
    "s_waitcnt lgkmcnt(9)\n"
    "v_mfma_f32_16x16x32_bf16 v[164:167], a[8:11], a[72:75], 0\n"
    "s_waitcnt lgkmcnt(4)\n"
    "s_barrier\n"
    "ds_read_b32 v36, v37 offset:0\n"
    "ds_read_b64_tr_b16 a[32:33], v26 offset:1024\n"
    "v_mfma_f32_16x16x32_bf16 v[164:167], a[12:15], a[76:79], v[164:167]\n"
    "ds_read_b64_tr_b16 a[34:35], v26 offset:5120\n"
    "v_mfma_f32_16x16x32_bf16 v[132:135], a[12:15], a[68:71], 0\n"
    "ds_read_b64_tr_b16 a[36:37], v26 offset:9216\n"
    "v_mfma_f32_16x16x32_bf16 v[132:135], a[8:11], a[64:67], v[132:135]\n"
    "ds_read_b64_tr_b16 a[38:39], v26 offset:13312\n"
    "s_nop 0\n"
    "v_mfma_f32_16x16x32_bf16 v[136:139], a[16:19], a[64:67], 0\n"
    "ds_read_b64_tr_b16 a[40:41], v27 offset:1024\n"
    "v_mfma_f32_16x16x32_bf16 v[136:139], a[20:23], a[68:71], v[136:139]\n"
    "ds_read_b64_tr_b16 a[42:43], v27 offset:5120\n"
    "v_mfma_f32_16x16x32_bf16 v[168:171], a[20:23], a[76:79], 0\n"
    "ds_read_b64_tr_b16 a[44:45], v27 offset:9216\n"
    "v_mfma_f32_16x16x32_bf16 v[168:171], a[16:19], a[72:75], v[168:171]\n"
    "ds_read_b64_tr_b16 a[46:47], v27 offset:13312\n"
    "s_waitcnt lgkmcnt(9)\n"
    "v_mfma_f32_16x16x32_bf16 v[172:175], a[24:27], a[72:75], 0\n"
    "ds_read_b64_tr_b16 a[48:49], v26 offset:1536\n"
    "v_mfma_f32_16x16x32_bf16 v[172:175], a[28:31], a[76:79], v[172:175]\n"
    "ds_read_b64_tr_b16 a[50:51], v26 offset:5632\n"
    "v_mfma_f32_16x16x32_bf16 v[140:143], a[28:31], a[68:71], 0\n"
    "ds_read_b64_tr_b16 a[52:53], v26 offset:9728\n"
    "v_mfma_f32_16x16x32_bf16 v[140:143], a[24:27], a[64:67], v[140:143]\n"
    "ds_read_b64_tr_b16 a[54:55], v26 offset:13824\n"
    "s_waitcnt lgkmcnt(8)\n"
    "v_mfma_f32_16x16x32_bf16 v[144:147], a[32:35], a[64:67], 0\n"
    "ds_read_b64_tr_b16 a[56:57], v27 offset:1536\n"
    "v_mfma_f32_16x16x32_bf16 v[144:147], a[36:39], a[68:71], v[144:147]\n"
    "ds_read_b64_tr_b16 a[58:59], v27 offset:5632\n"
    "v_mfma_f32_16x16x32_bf16 v[176:179], a[36:39], a[76:79], 0\n"
    "ds_read_b64_tr_b16 a[60:61], v27 offset:9728\n"
    "v_mfma_f32_16x16x32_bf16 v[176:179], a[32:35], a[72:75], v[176:179]\n"
    "ds_read_b64_tr_b16 a[62:63], v27 offset:13824\n"
    "s_waitcnt lgkmcnt(8)\n"
    "v_mfma_f32_16x16x32_bf16 v[180:183], a[40:43], a[72:75], 0\n"
    "ds_read_b64_tr_b16 a[0:1], v26 offset:16384\n"
    "v_mfma_f32_16x16x32_bf16 v[180:183], a[44:47], a[76:79], v[180:183]\n"
    "ds_read_b64_tr_b16 a[2:3], v26 offset:20480\n"
    "v_mfma_f32_16x16x32_bf16 v[148:151], a[44:47], a[68:71], 0\n"
    "ds_read_b64_tr_b16 a[4:5], v26 offset:24576\n"
    "v_mfma_f32_16x16x32_bf16 v[148:151], a[40:43], a[64:67], v[148:151]\n"
    "ds_read_b64_tr_b16 a[6:7], v26 offset:28672\n"
    "s_waitcnt lgkmcnt(8)\n"
    "v_mfma_f32_16x16x32_bf16 v[152:155], a[48:51], a[64:67], 0\n"
    "ds_read_b64_tr_b16 a[8:9], v27 offset:16384\n"
    "v_mfma_f32_16x16x32_bf16 v[152:155], a[52:55], a[68:71], v[152:155]\n"
    "ds_read_b64_tr_b16 a[10:11], v27 offset:20480\n"
    "v_mfma_f32_16x16x32_bf16 v[184:187], a[52:55], a[76:79], 0\n"
    "ds_read_b64_tr_b16 a[12:13], v27 offset:24576\n"
    "v_mfma_f32_16x16x32_bf16 v[184:187], a[48:51], a[72:75], v[184:187]\n"
    "ds_read_b64_tr_b16 a[14:15], v27 offset:28672\n"
    "s_waitcnt lgkmcnt(8)\n"
    "v_mfma_f32_16x16x32_bf16 v[188:191], a[56:59], a[72:75], 0\n"
    "ds_read_b64_tr_b16 a[16:17], v26 offset:16896\n"
    "v_mfma_f32_16x16x32_bf16 v[188:191], a[60:63], a[76:79], v[188:191]\n"
    "ds_read_b64_tr_b16 a[18:19], v26 offset:20992\n"
    "v_mfma_f32_16x16x32_bf16 v[156:159], a[60:63], a[68:71], 0\n"
    "ds_read_b64_tr_b16 a[20:21], v26 offset:25088\n"
    "v_mfma_f32_16x16x32_bf16 v[156:159], a[56:59], a[64:67], v[156:159]\n"
    "ds_read_b64_tr_b16 a[22:23], v26 offset:29184\n"
    "s_waitcnt lgkmcnt(8)\n"
    "v_mfma_f32_16x16x32_bf16 v[128:131], a[0:3], a[80:83], v[128:131]\n"
    "s_nop 0\n"
    "v_cmp_ne_u32 vcc, 0, v36\n"
    "s_cmp_lg_u32 s74, 0\n"
    "s_cbranch_scc1 L_fine%=\n"
    "s_cbranch_vccnz L_fb%=\n"
    "L_fine%=:\n"
    "ds_read_b64_tr_b16 a[24:25], v27 offset:16896\n"
    "v_mfma_f32_16x16x32_bf16 v[128:131], a[4:7], a[84:87], v[128:131]\n"
    "ds_read_b64_tr_b16 a[26:27], v27 offset:20992\n"
    "v_mfma_f32_16x16x32_bf16 v[160:163], a[4:7], a[92:95], v[160:163]\n"
    "ds_read_b64_tr_b16 a[28:29], v27 offset:25088\n"
    "v_mfma_f32_16x16x32_bf16 v[160:163], a[0:3], a[88:91], v[160:163]\n"
    "ds_read_b64_tr_b16 a[30:31], v27 offset:29184\n"
    "s_waitcnt lgkmcnt(8)\n"
    "v_mfma_f32_16x16x32_bf16 v[164:167], a[8:11], a[88:91], v[164:167]\n"
    "ds_read_b64_tr_b16 a[32:33], v26 offset:17408\n"
    "v_mfma_f32_16x16x32_bf16 v[164:167], a[12:15], a[92:95], v[164:167]\n"
    "ds_read_b64_tr_b16 a[34:35], v26 offset:21504\n"
    "v_mfma_f32_16x16x32_bf16 v[132:135], a[12:15], a[84:87], v[132:135]\n"
    "ds_read_b64_tr_b16 a[36:37], v26 offset:25600\n"
    "v_mfma_f32_16x16x32_bf16 v[132:135], a[8:11], a[80:83], v[132:135]\n"
    "ds_read_b64_tr_b16 a[38:39], v26 offset:29696\n"
    "s_waitcnt lgkmcnt(8)\n"
    "v_mfma_f32_16x16x32_bf16 v[136:139], a[16:19], a[80:83], v[136:139]\n"
    "ds_read_b64_tr_b16 a[40:41], v27 offset:17408\n"
    "v_mfma_f32_16x16x32_bf16 v[136:139], a[20:23], a[84:87], v[136:139]\n"
    "ds_read_b64_tr_b16 a[42:43], v27 offset:21504\n"
    "v_mfma_f32_16x16x32_bf16 v[168:171], a[20:23], a[92:95], v[168:171]\n"
    "ds_read_b64_tr_b16 a[44:45], v27 offset:25600\n"
    "v_mfma_f32_16x16x32_bf16 v[168:171], a[16:19], a[88:91], v[168:171]\n"
    "ds_read_b64_tr_b16 a[46:47], v27 offset:29696\n"
    "s_waitcnt lgkmcnt(8)\n"
    "v_mfma_f32_16x16x32_bf16 v[172:175], a[24:27], a[88:91], v[172:175]\n"
    "ds_read_b64_tr_b16 a[48:49], v26 offset:17920\n"
    "v_mfma_f32_16x16x32_bf16 v[172:175], a[28:31], a[92:95], v[172:175]\n"
    "ds_read_b64_tr_b16 a[50:51], v26 offset:22016\n"
    "v_mfma_f32_16x16x32_bf16 v[140:143], a[28:31], a[84:87], v[140:143]\n"
    "ds_read_b64_tr_b16 a[52:53], v26 offset:26112\n"
    "v_mfma_f32_16x16x32_bf16 v[140:143], a[24:27], a[80:83], v[140:143]\n"
    "ds_read_b64_tr_b16 a[54:55], v26 offset:30208\n"
    "s_waitcnt lgkmcnt(8)\n"
    "v_mfma_f32_16x16x32_bf16 v[144:147], a[32:35], a[80:83], v[144:147]\n"
    "ds_read_b64_tr_b16 a[56:57], v27 offset:17920\n"
    "v_mfma_f32_16x16x32_bf16 v[144:147], a[36:39], a[84:87], v[144:147]\n"
    "ds_read_b64_tr_b16 a[58:59], v27 offset:22016\n"
    "v_mfma_f32_16x16x32_bf16 v[176:179], a[36:39], a[92:95], v[176:179]\n"
    "ds_read_b64_tr_b16 a[60:61], v27 offset:26112\n"
    "v_mfma_f32_16x16x32_bf16 v[176:179], a[32:35], a[88:91], v[176:179]\n"
    "ds_read_b64_tr_b16 a[62:63], v27 offset:30208\n"
    "s_waitcnt lgkmcnt(8)\n"
    "v_mfma_f32_16x16x32_bf16 v[180:183], a[40:43], a[88:91], v[180:183]\n"
    "s_waitcnt lgkmcnt(0)\n"
    "s_barrier\n"
    "ds_read_b64_tr_b16 a[0:1], v22 offset:32768\n"
    "v_mfma_f32_16x16x32_bf16 v[180:183], a[44:47], a[92:95], v[180:183]\n"
    "ds_read_b64_tr_b16 a[2:3], v22 offset:36864\n"
    "v_mfma_f32_16x16x32_bf16 v[148:151], a[44:47], a[84:87], v[148:151]\n"
    "ds_read_b64_tr_b16 a[4:5], v22 offset:40960\n"
    "v_mfma_f32_16x16x32_bf16 v[148:151], a[40:43], a[80:83], v[148:151]\n"
    "ds_read_b64_tr_b16 a[6:7], v22 offset:45056\n"
    "s_nop 0\n"
    "v_mfma_f32_16x16x32_bf16 v[152:155], a[48:51], a[80:83], v[152:155]\n"
    "ds_read_b64_tr_b16 a[8:9], v23 offset:32768\n"
    "v_mfma_f32_16x16x32_bf16 v[152:155], a[52:55], a[84:87], v[152:155]\n"
    "ds_read_b64_tr_b16 a[10:11], v23 offset:36864\n"
    "v_mfma_f32_16x16x32_bf16 v[184:187], a[52:55], a[92:95], v[184:187]\n"
    "ds_read_b64_tr_b16 a[12:13], v23 offset:40960\n"
    "v_mfma_f32_16x16x32_bf16 v[184:187], a[48:51], a[88:91], v[184:187]\n"
    "ds_read_b64_tr_b16 a[14:15], v23 offset:45056\n"
    "s_nop 0\n"
    "v_mfma_f32_16x16x32_bf16 v[188:191], a[56:59], a[88:91], v[188:191]\n"
    "ds_read_b64_tr_b16 a[16:17], v22 offset:33280\n"
    "v_mfma_f32_16x16x32_bf16 v[188:191], a[60:63], a[92:95], v[188:191]\n"
    "ds_read_b64_tr_b16 a[18:19], v22 offset:37376\n"
    "v_mfma_f32_16x16x32_bf16 v[156:159], a[60:63], a[84:87], v[156:159]\n"
    "ds_read_b64_tr_b16 a[20:21], v22 offset:41472\n"
    "v_mfma_f32_16x16x32_bf16 v[156:159], a[56:59], a[80:83], v[156:159]\n"
    "ds_read_b64_tr_b16 a[22:23], v22 offset:45568\n"
    "s_waitcnt lgkmcnt(8)\n"
    "v_mfma_f32_16x16x32_bf16 v[128:131], a[0:3], a[96:99], v[128:131]\n"
    "ds_read_b64_tr_b16 a[24:25], v23 offset:33280\n"
    "v_mfma_f32_16x16x32_bf16 v[128:131], a[4:7], a[100:103], v[128:131]\n"
    "ds_read_b64_tr_b16 a[26:27], v23 offset:37376\n"
    "v_mfma_f32_16x16x32_bf16 v[160:163], a[4:7], a[108:111], v[160:163]\n"
    "ds_read_b64_tr_b16 a[28:29], v23 offset:41472\n"
    "v_mfma_f32_16x16x32_bf16 v[160:163], a[0:3], a[104:107], v[160:163]\n"
    "ds_read_b64_tr_b16 a[30:31], v23 offset:45568\n"
    "s_waitcnt lgkmcnt(8)\n"
    "v_mfma_f32_16x16x32_bf16 v[164:167], a[8:11], a[104:107], v[164:167]\n"
    "ds_read_b64_tr_b16 a[32:33], v22 offset:33792\n"
    "v_mov_b32 v39, s65\n"
    "v_mfma_f32_16x16x32_bf16 v[164:167], a[12:15], a[108:111], v[164:167]\n"
    "ds_read_b64_tr_b16 a[34:35], v22 offset:37888\n"
    "v_lshrrev_b32 v39, 1, v39\n"
    "v_mfma_f32_16x16x32_bf16 v[132:135], a[12:15], a[100:103], v[132:135]\n"
    "ds_read_b64_tr_b16 a[36:37], v22 offset:41984\n"
    "v_add_u32 v39, v39, v2\n"
    "v_mfma_f32_16x16x32_bf16 v[132:135], a[8:11], a[96:99], v[132:135]\n"
    "ds_read_b64_tr_b16 a[38:39], v22 offset:46080\n"
    "v_and_b32 v36, 15, v1\n"
    "s_waitcnt lgkmcnt(8)\n"
    "v_mfma_f32_16x16x32_bf16 v[136:139], a[16:19], a[96:99], v[136:139]\n"
    "ds_read_b64_tr_b16 a[40:41], v23 offset:33792\n"
    "v_lshrrev_b32 v37, 4, v1\n"
    "v_mfma_f32_16x16x32_bf16 v[136:139], a[20:23], a[100:103], v[136:139]\n"
    "ds_read_b64_tr_b16 a[42:43], v23 offset:37888\n"
    "v_lshlrev_b32 v37, 3, v37\n"
    "v_mfma_f32_16x16x32_bf16 v[168:171], a[20:23], a[108:111], v[168:171]\n"
    "ds_read_b64_tr_b16 a[44:45], v23 offset:41984\n"
    "v_lshl_add_u32 v38, v36, 10, v37\n"
    "v_mfma_f32_16x16x32_bf16 v[168:171], a[16:19], a[104:107], v[168:171]\n"
    "ds_read_b64_tr_b16 a[46:47], v23 offset:46080\n"
    "v_cmp_gt_u32 vcc, 0x800, v3\n"
    "s_waitcnt lgkmcnt(8)\n"
    "v_mfma_f32_16x16x32_bf16 v[172:175], a[24:27], a[104:107], v[172:175]\n"
    "s_waitcnt vmcnt(0)\n"
    "s_barrier\n"
    "buffer_store_dwordx2 v[32:33], v3, s[24:27], s49 offen offset:64 sc1\n"
    "ds_read_b64_tr_b16 a[48:49], v22 offset:34304\n"
    "v_lshlrev_b32 v36, 3, v3\n"
    "v_mfma_f32_16x16x32_bf16 v[172:175], a[28:31], a[108:111], v[172:175]\n"
    "ds_read_b64_tr_b16 a[50:51], v22 offset:38400\n"
    "v_mov_b32 v37, 0x7ff00000\n"
    "v_mfma_f32_16x16x32_bf16 v[140:143], a[28:31], a[100:103], v[140:143]\n"
    "ds_read_b64_tr_b16 a[52:53], v22 offset:42496\n"
    "v_cndmask_b32 v20, v37, v36, vcc\n"
    "v_mfma_f32_16x16x32_bf16 v[140:143], a[24:27], a[96:99], v[140:143]\n"
    "ds_read_b64_tr_b16 a[54:55], v22 offset:46592\n"
    "s_waitcnt lgkmcnt(8)\n"
    "v_mfma_f32_16x16x32_bf16 v[144:147], a[32:35], a[96:99], v[144:147]\n"
    "ds_read_b64_tr_b16 a[56:57], v23 offset:34304\n"
    "v_mfma_f32_16x16x32_bf16 v[144:147], a[36:39], a[100:103], v[144:147]\n"
    "s_add_u32 m0, s65, 0x1c000\n"
    "s_xor_b32 s69, s89, 0x30000\n"
    "ds_read_b64_tr_b16 a[58:59], v23 offset:38400\n"
    "v_mfma_f32_16x16x32_bf16 v[176:179], a[36:39], a[108:111], v[176:179]\n"
    "ds_read_b64_tr_b16 a[60:61], v23 offset:42496\n"
    "buffer_load_dwordx4 v16, s[32:35], s69 offen offset:0 lds\n"
    "v_mfma_f32_16x16x32_bf16 v[176:179], a[32:35], a[104:107], v[176:179]\n"
    "ds_read_b64_tr_b16 a[62:63], v23 offset:46592\n"
    "s_waitcnt lgkmcnt(8)\n"
    "v_mfma_f32_16x16x32_bf16 v[180:183], a[40:43], a[104:107], v[180:183]\n"
    "ds_read_b64_tr_b16 a[0:1], v22 offset:16384\n"
    "v_mfma_f32_16x16x32_bf16 v[180:183], a[44:47], a[108:111], v[180:183]\n"
    "ds_read_b64_tr_b16 a[2:3], v22 offset:20480\n"
    "v_mfma_f32_16x16x32_bf16 v[148:151], a[44:47], a[100:103], v[148:151]\n"
    "ds_read_b64_tr_b16 a[4:5], v22 offset:24576\n"
    "buffer_load_dwordx4 v17, s[32:35], s69 offen offset:1024 lds\n"
    "v_mfma_f32_16x16x32_bf16 v[148:151], a[40:43], a[96:99], v[148:151]\n"
    "ds_read_b64_tr_b16 a[6:7], v22 offset:28672\n"
    "s_waitcnt lgkmcnt(8)\n"
    "v_mfma_f32_16x16x32_bf16 v[152:155], a[48:51], a[96:99], v[152:155]\n"
    "ds_read_b64_tr_b16 a[8:9], v23 offset:16384\n"
    "v_mfma_f32_16x16x32_bf16 v[152:155], a[52:55], a[100:103], v[152:155]\n"
    "ds_read_b64_tr_b16 a[10:11], v23 offset:20480\n"
    "v_mfma_f32_16x16x32_bf16 v[184:187], a[52:55], a[108:111], v[184:187]\n"
    "ds_read_b64_tr_b16 a[12:13], v23 offset:24576\n"
    "buffer_load_dwordx4 v18, s[32:35], s69 offen offset:2048 lds\n"
    "v_mfma_f32_16x16x32_bf16 v[184:187], a[48:51], a[104:107], v[184:187]\n"
    "ds_read_b64_tr_b16 a[14:15], v23 offset:28672\n"
    "s_waitcnt lgkmcnt(8)\n"
    "v_mfma_f32_16x16x32_bf16 v[188:191], a[56:59], a[104:107], v[188:191]\n"
    "ds_read_b64_tr_b16 a[16:17], v22 offset:16896\n"
    "v_mfma_f32_16x16x32_bf16 v[188:191], a[60:63], a[108:111], v[188:191]\n"
    "ds_read_b64_tr_b16 a[18:19], v22 offset:20992\n"
    "v_mfma_f32_16x16x32_bf16 v[156:159], a[60:63], a[100:103], v[156:159]\n"
    "ds_read_b64_tr_b16 a[20:21], v22 offset:25088\n"
    "buffer_load_dwordx4 v19, s[32:35], s69 offen offset:3072 lds\n"
    "v_mfma_f32_16x16x32_bf16 v[156:159], a[56:59], a[96:99], v[156:159]\n"
    "ds_read_b64_tr_b16 a[22:23], v22 offset:29184\n"
    "s_waitcnt lgkmcnt(8)\n"
    "v_mfma_f32_16x16x32_bf16 v[128:131], a[0:3], a[112:115], v[128:131]\n"
    "ds_read_b64_tr_b16 a[24:25], v23 offset:16896\n"
    "v_mfma_f32_16x16x32_bf16 v[128:131], a[4:7], a[116:119], v[128:131]\n"
    "s_add_u32 m0, s65, 0x20000\n"
    "s_xor_b32 s69, s89, 0x40000\n"
    "ds_read_b64_tr_b16 a[26:27], v23 offset:20992\n"
    "v_mfma_f32_16x16x32_bf16 v[160:163], a[4:7], a[124:127], v[160:163]\n"
    "ds_read_b64_tr_b16 a[28:29], v23 offset:25088\n"
    "buffer_load_dwordx4 v16, s[32:35], s69 offen offset:0 lds\n"
    "v_mfma_f32_16x16x32_bf16 v[160:163], a[0:3], a[120:123], v[160:163]\n"
    "ds_read_b64_tr_b16 a[30:31], v23 offset:29184\n"
    "s_waitcnt lgkmcnt(8)\n"
    "v_mfma_f32_16x16x32_bf16 v[164:167], a[8:11], a[120:123], v[164:167]\n"
    "ds_read_b64_tr_b16 a[32:33], v22 offset:17408\n"
    "v_mfma_f32_16x16x32_bf16 v[164:167], a[12:15], a[124:127], v[164:167]\n"
    "ds_read_b64_tr_b16 a[34:35], v22 offset:21504\n"
    "v_mfma_f32_16x16x32_bf16 v[132:135], a[12:15], a[116:119], v[132:135]\n"
    "ds_read_b64_tr_b16 a[36:37], v22 offset:25600\n"
    "buffer_load_dwordx4 v17, s[32:35], s69 offen offset:1024 lds\n"
    "v_mfma_f32_16x16x32_bf16 v[132:135], a[8:11], a[112:115], v[132:135]\n"
    "ds_read_b64_tr_b16 a[38:39], v22 offset:29696\n"
    "s_waitcnt lgkmcnt(8)\n"
    "v_mfma_f32_16x16x32_bf16 v[136:139], a[16:19], a[112:115], v[136:139]\n"
    "ds_read_b64_tr_b16 a[40:41], v23 offset:17408\n"
    "v_mfma_f32_16x16x32_bf16 v[136:139], a[20:23], a[116:119], v[136:139]\n"
    "ds_read_b64_tr_b16 a[42:43], v23 offset:21504\n"
    "v_mfma_f32_16x16x32_bf16 v[168:171], a[20:23], a[124:127], v[168:171]\n"
    "ds_read_b64_tr_b16 a[44:45], v23 offset:25600\n"
    "buffer_load_dwordx4 v18, s[32:35], s69 offen offset:2048 lds\n"
    "v_mfma_f32_16x16x32_bf16 v[168:171], a[16:19], a[120:123], v[168:171]\n"
    "ds_read_b64_tr_b16 a[46:47], v23 offset:29696\n"
    "s_waitcnt lgkmcnt(8)\n"
    "v_mfma_f32_16x16x32_bf16 v[172:175], a[24:27], a[120:123], v[172:175]\n"
    "ds_read_b64_tr_b16 a[48:49], v22 offset:17920\n"
    "v_mfma_f32_16x16x32_bf16 v[172:175], a[28:31], a[124:127], v[172:175]\n"
    "ds_read_b64_tr_b16 a[50:51], v22 offset:22016\n"
    "v_mfma_f32_16x16x32_bf16 v[140:143], a[28:31], a[116:119], v[140:143]\n"
    "ds_read_b64_tr_b16 a[52:53], v22 offset:26112\n"
    "buffer_load_dwordx4 v19, s[32:35], s69 offen offset:3072 lds\n"
    "v_mfma_f32_16x16x32_bf16 v[140:143], a[24:27], a[112:115], v[140:143]\n"
    "ds_read_b64_tr_b16 a[54:55], v22 offset:30208\n"
    "s_waitcnt lgkmcnt(8)\n"
    "v_mfma_f32_16x16x32_bf16 v[144:147], a[32:35], a[112:115], v[144:147]\n"
    "ds_read_b64_tr_b16 a[56:57], v23 offset:17920\n"
    "v_mfma_f32_16x16x32_bf16 v[144:147], a[36:39], a[116:119], v[144:147]\n"
    "s_add_u32 m0, s65, 0x24000\n"
    "s_xor_b32 s69, s89, 0x50000\n"
    "ds_read_b64_tr_b16 a[58:59], v23 offset:22016\n"
    "v_mfma_f32_16x16x32_bf16 v[176:179], a[36:39], a[124:127], v[176:179]\n"
    "ds_read_b64_tr_b16 a[60:61], v23 offset:26112\n"
    "buffer_load_dwordx4 v16, s[32:35], s69 offen offset:0 lds\n"
    "v_mfma_f32_16x16x32_bf16 v[176:179], a[32:35], a[120:123], v[176:179]\n"
    "ds_read_b64_tr_b16 a[62:63], v23 offset:30208\n"
    "s_waitcnt lgkmcnt(8)\n"
    "v_mfma_f32_16x16x32_bf16 v[180:183], a[40:43], a[120:123], v[180:183]\n"
    "s_waitcnt lgkmcnt(0)\n"
    "s_barrier\n"
    "ds_read_b64_tr_b16 a[0:1], v22 offset:49152\n"
    "v_mfma_f32_16x16x32_bf16 v[180:183], a[44:47], a[124:127], v[180:183]\n"
    "ds_read_b64_tr_b16 a[2:3], v22 offset:53248\n"
    "v_mfma_f32_16x16x32_bf16 v[148:151], a[44:47], a[116:119], v[148:151]\n"
    "ds_read_b64_tr_b16 a[4:5], v22 offset:57344\n"
    "buffer_load_dwordx4 v17, s[32:35], s69 offen offset:1024 lds\n"
    "v_mfma_f32_16x16x32_bf16 v[148:151], a[40:43], a[112:115], v[148:151]\n"
    "ds_read_b64_tr_b16 a[6:7], v22 offset:61440\n"
    "s_nop 0\n"
    "v_mfma_f32_16x16x32_bf16 v[152:155], a[48:51], a[112:115], v[152:155]\n"
    "ds_read_b64_tr_b16 a[8:9], v23 offset:49152\n"
    "v_mfma_f32_16x16x32_bf16 v[152:155], a[52:55], a[116:119], v[152:155]\n"
    "ds_read_b64_tr_b16 a[10:11], v23 offset:53248\n"
    "v_mfma_f32_16x16x32_bf16 v[184:187], a[52:55], a[124:127], v[184:187]\n"
    "ds_read_b64_tr_b16 a[12:13], v23 offset:57344\n"
    "buffer_load_dwordx4 v18, s[32:35], s69 offen offset:2048 lds\n"
    "v_mfma_f32_16x16x32_bf16 v[184:187], a[48:51], a[120:123], v[184:187]\n"
    "ds_read_b64_tr_b16 a[14:15], v23 offset:61440\n"
    "s_nop 0\n"
    "v_mfma_f32_16x16x32_bf16 v[188:191], a[56:59], a[120:123], v[188:191]\n"
    "ds_read_b64_tr_b16 a[16:17], v22 offset:49664\n"
    "v_mfma_f32_16x16x32_bf16 v[188:191], a[60:63], a[124:127], v[188:191]\n"
    "ds_read_b64_tr_b16 a[18:19], v22 offset:53760\n"
    "v_mfma_f32_16x16x32_bf16 v[156:159], a[60:63], a[116:119], v[156:159]\n"
    "ds_read_b64_tr_b16 a[20:21], v22 offset:57856\n"
    "buffer_load_dwordx4 v19, s[32:35], s69 offen offset:3072 lds\n"
    "v_mfma_f32_16x16x32_bf16 v[156:159], a[56:59], a[112:115], v[156:159]\n"
    "ds_read_b64_tr_b16 a[22:23], v22 offset:61952\n"
    "s_waitcnt lgkmcnt(8)\n"
    "v_mfma_f32_16x16x32_bf16 v[128:131], a[0:3], a[160:163], v[128:131]\n"
    "ds_read_b64_tr_b16 a[24:25], v23 offset:49664\n"
    "v_mfma_f32_16x16x32_bf16 v[128:131], a[4:7], a[164:167], v[128:131]\n"
    "s_xor_b32 s87, s46, 0\n"
    "s_add_u32 s87, s87, s48\n"
    "ds_read_b64_tr_b16 a[26:27], v23 offset:53760\n"
    "v_mfma_f32_16x16x32_bf16 v[160:163], a[4:7], a[172:175], v[160:163]\n"
    "ds_read_b64_tr_b16 a[28:29], v23 offset:57856\n"
    "buffer_load_dwordx4 v[192:195], v2, s[24:27], s87 offen offset:0 sc1\n"
    "v_mfma_f32_16x16x32_bf16 v[160:163], a[0:3], a[168:171], v[160:163]\n"
    "ds_read_b64_tr_b16 a[30:31], v23 offset:61952\n"
    "s_waitcnt lgkmcnt(8)\n"
    "v_mfma_f32_16x16x32_bf16 v[164:167], a[8:11], a[168:171], v[164:167]\n"
    "ds_read_b64_tr_b16 a[32:33], v22 offset:50176\n"
    "v_mfma_f32_16x16x32_bf16 v[164:167], a[12:15], a[172:175], v[164:167]\n"
    "ds_read_b64_tr_b16 a[34:35], v22 offset:54272\n"
    "v_mfma_f32_16x16x32_bf16 v[132:135], a[12:15], a[164:167], v[132:135]\n"
    "ds_read_b64_tr_b16 a[36:37], v22 offset:58368\n"
    "buffer_load_dwordx4 v[196:199], v2, s[24:27], s87 offen offset:1024 sc1\n"
    "v_mfma_f32_16x16x32_bf16 v[132:135], a[8:11], a[160:163], v[132:135]\n"
    "ds_read_b64_tr_b16 a[38:39], v22 offset:62464\n"
    "s_waitcnt lgkmcnt(8)\n"
    "v_mfma_f32_16x16x32_bf16 v[136:139], a[16:19], a[160:163], v[136:139]\n"
    "ds_read_b64_tr_b16 a[40:41], v23 offset:50176\n"
    "v_mfma_f32_16x16x32_bf16 v[136:139], a[20:23], a[164:167], v[136:139]\n"
    "s_add_u32 s88, s87, 0x2c000\n"
    "ds_read_b64_tr_b16 a[42:43], v23 offset:54272\n"
    "v_mfma_f32_16x16x32_bf16 v[168:171], a[20:23], a[172:175], v[168:171]\n"
    "ds_read_b64_tr_b16 a[44:45], v23 offset:58368\n"
    "buffer_load_dwordx4 v[200:203], v2, s[24:27], s88 offen offset:0 sc1\n"
    "v_mfma_f32_16x16x32_bf16 v[168:171], a[16:19], a[168:171], v[168:171]\n"
    "ds_read_b64_tr_b16 a[46:47], v23 offset:62464\n"
    "s_waitcnt lgkmcnt(8)\n"
    "v_mfma_f32_16x16x32_bf16 v[172:175], a[24:27], a[168:171], v[172:175]\n"
    "ds_read_b64_tr_b16 a[48:49], v22 offset:50688\n"
    "v_mfma_f32_16x16x32_bf16 v[172:175], a[28:31], a[172:175], v[172:175]\n"
    "ds_read_b64_tr_b16 a[50:51], v22 offset:54784\n"
    "v_mfma_f32_16x16x32_bf16 v[140:143], a[28:31], a[164:167], v[140:143]\n"
    "ds_read_b64_tr_b16 a[52:53], v22 offset:58880\n"
    "buffer_load_dwordx4 v[204:207], v2, s[24:27], s88 offen offset:1024 sc1\n"
    "v_mfma_f32_16x16x32_bf16 v[140:143], a[24:27], a[160:163], v[140:143]\n"
    "ds_read_b64_tr_b16 a[54:55], v22 offset:62976\n"
    "s_waitcnt lgkmcnt(8)\n"
    "v_mfma_f32_16x16x32_bf16 v[144:147], a[32:35], a[160:163], v[144:147]\n"
    "ds_read_b64_tr_b16 a[56:57], v23 offset:50688\n"
    "v_mfma_f32_16x16x32_bf16 v[144:147], a[36:39], a[164:167], v[144:147]\n"
    "s_xor_b32 s87, s46, 0x800\n"
    "s_add_u32 s87, s87, s48\n"
    "ds_read_b64_tr_b16 a[58:59], v23 offset:54784\n"
    "v_mfma_f32_16x16x32_bf16 v[176:179], a[36:39], a[172:175], v[176:179]\n"
    "ds_read_b64_tr_b16 a[60:61], v23 offset:58880\n"
    "buffer_load_dwordx4 v[208:211], v2, s[24:27], s87 offen offset:0 sc1\n"
    "v_mfma_f32_16x16x32_bf16 v[176:179], a[32:35], a[168:171], v[176:179]\n"
    "ds_read_b64_tr_b16 a[62:63], v23 offset:62976\n"
    "s_waitcnt lgkmcnt(8)\n"
    "v_mfma_f32_16x16x32_bf16 v[180:183], a[40:43], a[168:171], v[180:183]\n"
    "ds_read_b64_tr_b16 a[0:1], v24 offset:0\n"
    "v_mfma_f32_16x16x32_bf16 v[180:183], a[44:47], a[172:175], v[180:183]\n"
    "ds_read_b64_tr_b16 a[2:3], v24 offset:4096\n"
    "v_mfma_f32_16x16x32_bf16 v[148:151], a[44:47], a[164:167], v[148:151]\n"
    "ds_read_b64_tr_b16 a[4:5], v24 offset:8192\n"
    "buffer_load_dwordx4 v[212:215], v2, s[24:27], s87 offen offset:1024 sc1\n"
    "v_mfma_f32_16x16x32_bf16 v[148:151], a[40:43], a[160:163], v[148:151]\n"
    "ds_read_b64_tr_b16 a[6:7], v24 offset:12288\n"
    "s_waitcnt lgkmcnt(8)\n"
    "v_mfma_f32_16x16x32_bf16 v[152:155], a[48:51], a[160:163], v[152:155]\n"
    "ds_read_b64_tr_b16 a[8:9], v25 offset:0\n"
    "v_mfma_f32_16x16x32_bf16 v[152:155], a[52:55], a[164:167], v[152:155]\n"
    "s_add_u32 s88, s87, 0x2c000\n"
    "ds_read_b64_tr_b16 a[10:11], v25 offset:4096\n"
    "v_mfma_f32_16x16x32_bf16 v[184:187], a[52:55], a[172:175], v[184:187]\n"
    "ds_read_b64_tr_b16 a[12:13], v25 offset:8192\n"
    "buffer_load_dwordx4 v[216:219], v2, s[24:27], s88 offen offset:0 sc1\n"
    "v_mfma_f32_16x16x32_bf16 v[184:187], a[48:51], a[168:171], v[184:187]\n"
    "ds_read_b64_tr_b16 a[14:15], v25 offset:12288\n"
    "s_waitcnt lgkmcnt(8)\n"
    "v_mfma_f32_16x16x32_bf16 v[188:191], a[56:59], a[168:171], v[188:191]\n"
    "ds_read_b64_tr_b16 a[16:17], v24 offset:512\n"
    "v_mfma_f32_16x16x32_bf16 v[188:191], a[60:63], a[172:175], v[188:191]\n"
    "ds_read_b64_tr_b16 a[18:19], v24 offset:4608\n"
    "v_mfma_f32_16x16x32_bf16 v[156:159], a[60:63], a[164:167], v[156:159]\n"
    "ds_read_b64_tr_b16 a[20:21], v24 offset:8704\n"
    "buffer_load_dwordx4 v[220:223], v2, s[24:27], s88 offen offset:1024 sc1\n"
    "v_mfma_f32_16x16x32_bf16 v[156:159], a[56:59], a[160:163], v[156:159]\n"
    "ds_read_b64_tr_b16 a[22:23], v24 offset:12800\n"
    "s_waitcnt lgkmcnt(8)\n"
    "v_mfma_f32_16x16x32_bf16 v[128:131], a[0:3], v[44:47], v[128:131]\n"
    "buffer_load_dwordx2 v[34:35], v21, s[24:27], s51 offen offset:64 sc1\n"
    "ds_read_b64_tr_b16 a[24:25], v25 offset:512\n"
    "v_mfma_f32_16x16x32_bf16 v[128:131], a[4:7], v[48:51], v[128:131]\n"
    "s_add_u32 m0, s65, 0x8000\n"
    "s_xor_b32 s69, s89, 0x60000\n"
    "ds_read_b64_tr_b16 a[26:27], v25 offset:4608\n"
    "v_mfma_f32_16x16x32_bf16 v[160:163], a[4:7], v[80:83], v[160:163]\n"
    "ds_read_b64_tr_b16 a[28:29], v25 offset:8704\n"
    "buffer_load_dwordx4 v16, s[32:35], s69 offen offset:0 lds\n"
    "v_mfma_f32_16x16x32_bf16 v[160:163], a[0:3], v[52:55], v[160:163]\n"
    "ds_read_b64_tr_b16 a[30:31], v25 offset:12800\n"
    "s_waitcnt lgkmcnt(8)\n"
    "v_mfma_f32_16x16x32_bf16 v[164:167], a[8:11], v[52:55], v[164:167]\n"
    "ds_read_b64_tr_b16 a[32:33], v24 offset:1024\n"
    "v_mfma_f32_16x16x32_bf16 v[164:167], a[12:15], v[80:83], v[164:167]\n"
    "ds_read_b64_tr_b16 a[34:35], v24 offset:5120\n"
    "v_mfma_f32_16x16x32_bf16 v[132:135], a[12:15], v[48:51], v[132:135]\n"
    "ds_read_b64_tr_b16 a[36:37], v24 offset:9216\n"
    "buffer_load_dwordx4 v17, s[32:35], s69 offen offset:1024 lds\n"
    "v_mfma_f32_16x16x32_bf16 v[132:135], a[8:11], v[44:47], v[132:135]\n"
    "ds_read_b64_tr_b16 a[38:39], v24 offset:13312\n"
    "s_waitcnt lgkmcnt(8)\n"
    "v_mfma_f32_16x16x32_bf16 v[136:139], a[16:19], v[44:47], v[136:139]\n"
    "ds_read_b64_tr_b16 a[40:41], v25 offset:1024\n"
    "v_mfma_f32_16x16x32_bf16 v[136:139], a[20:23], v[48:51], v[136:139]\n"
    "ds_read_b64_tr_b16 a[42:43], v25 offset:5120\n"
    "v_mfma_f32_16x16x32_bf16 v[168:171], a[20:23], v[80:83], v[168:171]\n"
    "ds_read_b64_tr_b16 a[44:45], v25 offset:9216\n"
    "buffer_load_dwordx4 v18, s[32:35], s69 offen offset:2048 lds\n"
    "v_mfma_f32_16x16x32_bf16 v[168:171], a[16:19], v[52:55], v[168:171]\n"
    "ds_read_b64_tr_b16 a[46:47], v25 offset:13312\n"
    "s_waitcnt lgkmcnt(8)\n"
    "v_mfma_f32_16x16x32_bf16 v[172:175], a[24:27], v[52:55], v[172:175]\n"
    "ds_read_b64_tr_b16 a[48:49], v24 offset:1536\n"
    "v_mfma_f32_16x16x32_bf16 v[172:175], a[28:31], v[80:83], v[172:175]\n"
    "ds_read_b64_tr_b16 a[50:51], v24 offset:5632\n"
    "v_mfma_f32_16x16x32_bf16 v[140:143], a[28:31], v[48:51], v[140:143]\n"
    "ds_read_b64_tr_b16 a[52:53], v24 offset:9728\n"
    "buffer_load_dwordx4 v19, s[32:35], s69 offen offset:3072 lds\n"
    "v_mfma_f32_16x16x32_bf16 v[140:143], a[24:27], v[44:47], v[140:143]\n"
    "ds_read_b64_tr_b16 a[54:55], v24 offset:13824\n"
    "s_waitcnt lgkmcnt(8)\n"
    "v_mfma_f32_16x16x32_bf16 v[144:147], a[32:35], v[44:47], v[144:147]\n"
    "ds_read_b64_tr_b16 a[56:57], v25 offset:1536\n"
    "v_mfma_f32_16x16x32_bf16 v[144:147], a[36:39], v[48:51], v[144:147]\n"
    "s_add_u32 m0, s65, 0x4000\n"
    "s_xor_b32 s69, s89, 0x70000\n"
    "ds_read_b64_tr_b16 a[58:59], v25 offset:5632\n"
    "v_mfma_f32_16x16x32_bf16 v[176:179], a[36:39], v[80:83], v[176:179]\n"
    "ds_read_b64_tr_b16 a[60:61], v25 offset:9728\n"
    "buffer_load_dwordx4 v16, s[32:35], s69 offen offset:0 lds\n"
    "v_mfma_f32_16x16x32_bf16 v[176:179], a[32:35], v[52:55], v[176:179]\n"
    "ds_read_b64_tr_b16 a[62:63], v25 offset:13824\n"
    "s_waitcnt lgkmcnt(8)\n"
    "v_mfma_f32_16x16x32_bf16 v[180:183], a[40:43], v[52:55], v[180:183]\n"
    "s_waitcnt lgkmcnt(0)\n"
    "s_barrier\n"
    "ds_read_b64_tr_b16 a[0:1], v22 offset:0\n"
    "v_mfma_f32_16x16x32_bf16 v[180:183], a[44:47], v[80:83], v[180:183]\n"
    "ds_read_b64_tr_b16 a[2:3], v22 offset:4096\n"
    "v_mfma_f32_16x16x32_bf16 v[148:151], a[44:47], v[48:51], v[148:151]\n"
    "ds_read_b64_tr_b16 a[4:5], v22 offset:8192\n"
    "buffer_load_dwordx4 v17, s[32:35], s69 offen offset:1024 lds\n"
    "v_mfma_f32_16x16x32_bf16 v[148:151], a[40:43], v[44:47], v[148:151]\n"
    "ds_read_b64_tr_b16 a[6:7], v22 offset:12288\n"
    "s_nop 0\n"
    "v_mfma_f32_16x16x32_bf16 v[152:155], a[48:51], v[44:47], v[152:155]\n"
    "ds_read_b64_tr_b16 a[8:9], v23 offset:0\n"
    "v_mfma_f32_16x16x32_bf16 v[152:155], a[52:55], v[48:51], v[152:155]\n"
    "ds_read_b64_tr_b16 a[10:11], v23 offset:4096\n"
    "v_mfma_f32_16x16x32_bf16 v[184:187], a[52:55], v[80:83], v[184:187]\n"
    "ds_read_b64_tr_b16 a[12:13], v23 offset:8192\n"
    "buffer_load_dwordx4 v18, s[32:35], s69 offen offset:2048 lds\n"
    "v_mfma_f32_16x16x32_bf16 v[184:187], a[48:51], v[52:55], v[184:187]\n"
    "ds_read_b64_tr_b16 a[14:15], v23 offset:12288\n"
    "s_nop 0\n"
    "v_mfma_f32_16x16x32_bf16 v[188:191], a[56:59], v[52:55], v[188:191]\n"
    "ds_read_b64_tr_b16 a[16:17], v22 offset:512\n"
    "v_mfma_f32_16x16x32_bf16 v[188:191], a[60:63], v[80:83], v[188:191]\n"
    "ds_read_b64_tr_b16 a[18:19], v22 offset:4608\n"
    "v_mfma_f32_16x16x32_bf16 v[156:159], a[60:63], v[48:51], v[156:159]\n"
    "ds_read_b64_tr_b16 a[20:21], v22 offset:8704\n"
    "buffer_load_dwordx4 v19, s[32:35], s69 offen offset:3072 lds\n"
    "v_mfma_f32_16x16x32_bf16 v[156:159], a[56:59], v[44:47], v[156:159]\n"
    "ds_read_b64_tr_b16 a[22:23], v22 offset:12800\n"
    "s_waitcnt vmcnt(8)\n"
    "v_cmp_ne_u64 vcc, s[90:91], v[34:35]\n"
    "s_and_b32 s68, vcc_lo, 0xe0e0e0e\n"
    "s_cbranch_scc0 L_po1ok%=\n"
    "s_mov_b32 s86, 0\n"
    "buffer_load_dwordx2 v[34:35], v21, s[24:27], s51 offen offset:64 sc1\n"
    "s_sleep 5\n"
    "buffer_load_dwordx2 v[10:11], v21, s[24:27], s51 offen offset:64 sc1\n"
    "s_sleep 5\n"
    "buffer_load_dwordx2 v[12:13], v21, s[24:27], s51 offen offset:64 sc1\n"
    "s_sleep 5\n"
    "L_po1%=:\n"
    "s_waitcnt vmcnt(2)\n"
    "v_cmp_ne_u64 vcc, s[90:91], v[34:35]\n"
    "s_and_b32 s68, vcc_lo, 0xe0e0e0e\n"
    "s_cbranch_scc0 L_po1ok%=\n"
    "buffer_load_dwordx2 v[34:35], v21, s[24:27], s51 offen offset:64 sc1\n"
    "s_waitcnt vmcnt(2)\n"
    "v_cmp_ne_u64 vcc, s[90:91], v[10:11]\n"
    "s_and_b32 s68, vcc_lo, 0xe0e0e0e\n"
    "s_cbranch_scc0 L_po1ok%=\n"
    "buffer_load_dwordx2 v[10:11], v21, s[24:27], s51 offen offset:64 sc1\n"
    "s_waitcnt vmcnt(2)\n"
    "v_cmp_ne_u64 vcc, s[90:91], v[12:13]\n"
    "s_and_b32 s68, vcc_lo, 0xe0e0e0e\n"
    "s_cbranch_scc0 L_po1ok%=\n"
    "buffer_load_dwordx2 v[12:13], v21, s[24:27], s51 offen offset:64 sc1\n"
    "s_add_u32 s86, s86, 1\n"
    "s_cmp_lt_u32 s86, 0x100000\n"
    "s_cbranch_scc1 L_po1%=\n"
    "L_po1ok%=:\n"
    "s_xor_b32 s87, s46, 0x1000\n"
    "s_add_u32 s87, s87, s48\n"
    "buffer_load_dwordx4 v[224:227], v2, s[24:27], s87 offen offset:0 sc1\n"
    "buffer_load_dwordx4 v[228:231], v2, s[24:27], s87 offen offset:1024 sc1\n"
    "s_add_u32 s88, s87, 0x2c000\n"
    "buffer_load_dwordx4 v[232:235], v2, s[24:27], s88 offen offset:0 sc1\n"
    "buffer_load_dwordx4 v[236:239], v2, s[24:27], s88 offen offset:1024 sc1\n"
    "s_xor_b32 s87, s46, 0x1800\n"
    "s_add_u32 s87, s87, s48\n"
    "buffer_load_dwordx4 v[240:243], v2, s[24:27], s87 offen offset:0 sc1\n"
    "buffer_load_dwordx4 v[244:247], v2, s[24:27], s87 offen offset:1024 sc1\n"
    "s_add_u32 s88, s87, 0x2c000\n"
    "buffer_load_dwordx4 v[248:251], v2, s[24:27], s88 offen offset:0 sc1\n"
    "buffer_load_dwordx4 v[252:255], v2, s[24:27], s88 offen offset:1024 sc1\n"
    "buffer_load_dwordx2 v[14:15], v21, s[24:27], s51 offen offset:64 sc1\n"
    "s_waitcnt lgkmcnt(8)\n"
    "v_mfma_f32_16x16x32_bf16 v[64:67], a[0:3], v[192:195], 0\n"
    "ds_read_b64_tr_b16 a[24:25], v23 offset:512\n"
    "v_mfma_f32_16x16x32_bf16 v[64:67], a[4:7], v[196:199], v[64:67]\n"
    "s_xor_b32 s87, s46, 0x2000\n"
    "s_add_u32 s87, s87, s48\n"
    "ds_read_b64_tr_b16 a[26:27], v23 offset:4608\n"
    "v_mfma_f32_16x16x32_bf16 v[96:99], a[4:7], v[204:207], 0\n"
    "ds_read_b64_tr_b16 a[28:29], v23 offset:8704\n"
    "buffer_load_dwordx4 a[64:67], v2, s[24:27], s87 offen offset:0 sc1\n"
    "v_mfma_f32_16x16x32_bf16 v[96:99], a[0:3], v[200:203], v[96:99]\n"
    "ds_read_b64_tr_b16 a[30:31], v23 offset:12800\n"
    "s_waitcnt lgkmcnt(8)\n"
    "v_mfma_f32_16x16x32_bf16 v[100:103], a[8:11], v[200:203], 0\n"
    "ds_read_b64_tr_b16 a[32:33], v22 offset:1024\n"
    "v_mfma_f32_16x16x32_bf16 v[100:103], a[12:15], v[204:207], v[100:103]\n"
    "ds_read_b64_tr_b16 a[34:35], v22 offset:5120\n"
    "v_mfma_f32_16x16x32_bf16 v[68:71], a[12:15], v[196:199], 0\n"
    "ds_read_b64_tr_b16 a[36:37], v22 offset:9216\n"
    "buffer_load_dwordx4 a[68:71], v2, s[24:27], s87 offen offset:1024 sc1\n"
    "v_mfma_f32_16x16x32_bf16 v[68:71], a[8:11], v[192:195], v[68:71]\n"
    "ds_read_b64_tr_b16 a[38:39], v22 offset:13312\n"
    "s_waitcnt lgkmcnt(8)\n"
    "v_mfma_f32_16x16x32_bf16 v[72:75], a[16:19], v[192:195], 0\n"
    "ds_read_b64_tr_b16 a[40:41], v23 offset:1024\n"
    "v_mfma_f32_16x16x32_bf16 v[72:75], a[20:23], v[196:199], v[72:75]\n"
    "s_add_u32 s88, s87, 0x2c000\n"
    "ds_read_b64_tr_b16 a[42:43], v23 offset:5120\n"
    "v_mfma_f32_16x16x32_bf16 v[104:107], a[20:23], v[204:207], 0\n"
    "ds_read_b64_tr_b16 a[44:45], v23 offset:9216\n"
    "buffer_load_dwordx4 a[72:75], v2, s[24:27], s88 offen offset:0 sc1\n"
    "v_mfma_f32_16x16x32_bf16 v[104:107], a[16:19], v[200:203], v[104:107]\n"
    "ds_read_b64_tr_b16 a[46:47], v23 offset:13312\n"
    "s_waitcnt lgkmcnt(8)\n"
    "v_mfma_f32_16x16x32_bf16 v[108:111], a[24:27], v[200:203], 0\n"
    "ds_read_b64_tr_b16 a[48:49], v22 offset:1536\n"
    "v_cvt_pk_bf16_f32 v136, v136, v137\n"
    "v_mfma_f32_16x16x32_bf16 v[108:111], a[28:31], v[204:207], v[108:111]\n"
    "ds_read_b64_tr_b16 a[50:51], v22 offset:5632\n"
    "v_cvt_pk_bf16_f32 v137, v138, v139\n"
    "v_mfma_f32_16x16x32_bf16 v[76:79], a[28:31], v[196:199], 0\n"
    "ds_read_b64_tr_b16 a[52:53], v22 offset:9728\n"
    "buffer_load_dwordx4 a[76:79], v2, s[24:27], s88 offen offset:1024 sc1\n"
    "v_mfma_f32_16x16x32_bf16 v[76:79], a[24:27], v[192:195], v[76:79]\n"
    "ds_read_b64_tr_b16 a[54:55], v22 offset:13824\n"
    "v_cvt_pk_bf16_f32 v138, v140, v141\n"
    "s_waitcnt lgkmcnt(8)\n"
    "v_mfma_f32_16x16x32_bf16 v[80:83], a[32:35], v[192:195], 0\n"
    "ds_read_b64_tr_b16 a[56:57], v23 offset:1536\n"
    "v_cvt_pk_bf16_f32 v139, v142, v143\n"
    "v_mfma_f32_16x16x32_bf16 v[80:83], a[36:39], v[196:199], v[80:83]\n"
    "s_xor_b32 s87, s46, 0x2800\n"
    "s_add_u32 s87, s87, s48\n"
    "ds_read_b64_tr_b16 a[58:59], v23 offset:5632\n"
    "v_cvt_pk_bf16_f32 v168, v168, v169\n"
    "v_mfma_f32_16x16x32_bf16 v[112:115], a[36:39], v[204:207], 0\n"
    "ds_read_b64_tr_b16 a[60:61], v23 offset:9728\n"
    "buffer_load_dwordx4 a[80:83], v2, s[24:27], s87 offen offset:0 sc1\n"
    "v_mfma_f32_16x16x32_bf16 v[112:115], a[32:35], v[200:203], v[112:115]\n"
    "ds_read_b64_tr_b16 a[62:63], v23 offset:13824\n"
    "v_cvt_pk_bf16_f32 v169, v170, v171\n"
    "s_waitcnt lgkmcnt(8)\n"
    "v_mfma_f32_16x16x32_bf16 v[116:119], a[40:43], v[200:203], 0\n"
    "ds_read_b64_tr_b16 a[0:1], v24 offset:16384\n"
    "v_cvt_pk_bf16_f32 v170, v172, v173\n"
    "v_mfma_f32_16x16x32_bf16 v[116:119], a[44:47], v[204:207], v[116:119]\n"
    "ds_read_b64_tr_b16 a[2:3], v24 offset:20480\n"
    "v_cvt_pk_bf16_f32 v171, v174, v175\n"
    "v_mfma_f32_16x16x32_bf16 v[84:87], a[44:47], v[196:199], 0\n"
    "ds_read_b64_tr_b16 a[4:5], v24 offset:24576\n"
    "buffer_load_dwordx4 a[84:87], v2, s[24:27], s87 offen offset:1024 sc1\n"
    "v_mfma_f32_16x16x32_bf16 v[84:87], a[40:43], v[192:195], v[84:87]\n"
    "ds_read_b64_tr_b16 a[6:7], v24 offset:28672\n"
    "v_cvt_pk_bf16_f32 v144, v144, v145\n"
    "s_waitcnt lgkmcnt(8)\n"
    "v_mfma_f32_16x16x32_bf16 v[88:91], a[48:51], v[192:195], 0\n"
    "ds_read_b64_tr_b16 a[8:9], v25 offset:16384\n"
    "v_cvt_pk_bf16_f32 v145, v146, v147\n"
    "v_mfma_f32_16x16x32_bf16 v[88:91], a[52:55], v[196:199], v[88:91]\n"
    "s_add_u32 s88, s87, 0x2c000\n"
    "ds_read_b64_tr_b16 a[10:11], v25 offset:20480\n"
    "v_cvt_pk_bf16_f32 v146, v148, v149\n"
    "v_mfma_f32_16x16x32_bf16 v[120:123], a[52:55], v[204:207], 0\n"
    "ds_read_b64_tr_b16 a[12:13], v25 offset:24576\n"
    "buffer_load_dwordx4 a[88:91], v2, s[24:27], s88 offen offset:0 sc1\n"
    "v_mfma_f32_16x16x32_bf16 v[120:123], a[48:51], v[200:203], v[120:123]\n"
    "ds_read_b64_tr_b16 a[14:15], v25 offset:28672\n"
    "v_cvt_pk_bf16_f32 v147, v150, v151\n"
    "s_waitcnt lgkmcnt(8)\n"
    "v_mfma_f32_16x16x32_bf16 v[124:127], a[56:59], v[200:203], 0\n"
    "ds_read_b64_tr_b16 a[16:17], v24 offset:16896\n"
    "v_cvt_pk_bf16_f32 v176, v176, v177\n"
    "v_mfma_f32_16x16x32_bf16 v[124:127], a[60:63], v[204:207], v[124:127]\n"
    "ds_read_b64_tr_b16 a[18:19], v24 offset:20992\n"
    "v_cvt_pk_bf16_f32 v177, v178, v179\n"
    "v_mfma_f32_16x16x32_bf16 v[92:95], a[60:63], v[196:199], 0\n"
    "ds_read_b64_tr_b16 a[20:21], v24 offset:25088\n"
    "buffer_load_dwordx4 a[92:95], v2, s[24:27], s88 offen offset:1024 sc1\n"
    "v_mfma_f32_16x16x32_bf16 v[92:95], a[56:59], v[192:195], v[92:95]\n"
    "ds_read_b64_tr_b16 a[22:23], v24 offset:29184\n"
    "v_cvt_pk_bf16_f32 v178, v180, v181\n"
    "s_waitcnt lgkmcnt(8)\n"
    "v_mfma_f32_16x16x32_bf16 v[64:67], a[0:3], v[208:211], v[64:67]\n"
    "ds_read_b64_tr_b16 a[24:25], v25 offset:16896\n"
    "v_cvt_pk_bf16_f32 v179, v182, v183\n"
    "v_mfma_f32_16x16x32_bf16 v[64:67], a[4:7], v[212:215], v[64:67]\n"
    "s_xor_b32 s87, s46, 0x3000\n"
    "s_add_u32 s87, s87, s48\n"
    "ds_read_b64_tr_b16 a[26:27], v25 offset:20992\n"
    "v_cvt_pk_bf16_f32 v152, v152, v153\n"
    "v_mfma_f32_16x16x32_bf16 v[96:99], a[4:7], v[220:223], v[96:99]\n"
    "ds_read_b64_tr_b16 a[28:29], v25 offset:25088\n"
    "buffer_load_dwordx4 a[96:99], v2, s[24:27], s87 offen offset:0 sc1\n"
    "v_mfma_f32_16x16x32_bf16 v[96:99], a[0:3], v[216:219], v[96:99]\n"
    "ds_read_b64_tr_b16 a[30:31], v25 offset:29184\n"
    "v_cvt_pk_bf16_f32 v153, v154, v155\n"
    "s_waitcnt lgkmcnt(8)\n"
    "v_mfma_f32_16x16x32_bf16 v[100:103], a[8:11], v[216:219], v[100:103]\n"
    "ds_read_b64_tr_b16 a[32:33], v24 offset:17408\n"
    "v_cvt_pk_bf16_f32 v154, v156, v157\n"
    "v_mfma_f32_16x16x32_bf16 v[100:103], a[12:15], v[220:223], v[100:103]\n"
    "ds_read_b64_tr_b16 a[34:35], v24 offset:21504\n"
    "v_cvt_pk_bf16_f32 v155, v158, v159\n"
    "v_mfma_f32_16x16x32_bf16 v[68:71], a[12:15], v[212:215], v[68:71]\n"
    "ds_read_b64_tr_b16 a[36:37], v24 offset:25600\n"
    "buffer_load_dwordx4 a[100:103], v2, s[24:27], s87 offen offset:1024 sc1\n"
    "v_mfma_f32_16x16x32_bf16 v[68:71], a[8:11], v[208:211], v[68:71]\n"
    "ds_read_b64_tr_b16 a[38:39], v24 offset:29696\n"
    "v_cvt_pk_bf16_f32 v184, v184, v185\n"
    "s_waitcnt lgkmcnt(8)\n"
    "v_mfma_f32_16x16x32_bf16 v[72:75], a[16:19], v[208:211], v[72:75]\n"
    "ds_read_b64_tr_b16 a[40:41], v25 offset:17408\n"
    "v_cvt_pk_bf16_f32 v185, v186, v187\n"
    "v_mfma_f32_16x16x32_bf16 v[72:75], a[20:23], v[212:215], v[72:75]\n"
    "s_add_u32 s88, s87, 0x2c000\n"
    "ds_read_b64_tr_b16 a[42:43], v25 offset:21504\n"
    "v_cvt_pk_bf16_f32 v186, v188, v189\n"
    "v_mfma_f32_16x16x32_bf16 v[104:107], a[20:23], v[220:223], v[104:107]\n"
    "ds_read_b64_tr_b16 a[44:45], v25 offset:25600\n"
    "buffer_load_dwordx4 a[104:107], v2, s[24:27], s88 offen offset:0 sc1\n"
    "v_mfma_f32_16x16x32_bf16 v[104:107], a[16:19], v[216:219], v[104:107]\n"
    "ds_read_b64_tr_b16 a[46:47], v25 offset:29696\n"
    "v_cvt_pk_bf16_f32 v187, v190, v191\n"
    "s_waitcnt lgkmcnt(8)\n"
    "v_mfma_f32_16x16x32_bf16 v[108:111], a[24:27], v[216:219], v[108:111]\n"
    "ds_read_b64_tr_b16 a[48:49], v24 offset:17920\n"
    "v_mfma_f32_16x16x32_bf16 v[108:111], a[28:31], v[220:223], v[108:111]\n"
    "ds_read_b64_tr_b16 a[50:51], v24 offset:22016\n"
    "v_mfma_f32_16x16x32_bf16 v[76:79], a[28:31], v[212:215], v[76:79]\n"
    "ds_read_b64_tr_b16 a[52:53], v24 offset:26112\n"
    "buffer_load_dwordx4 a[108:111], v2, s[24:27], s88 offen offset:1024 sc1\n"
    "v_mfma_f32_16x16x32_bf16 v[76:79], a[24:27], v[208:211], v[76:79]\n"
    "ds_read_b64_tr_b16 a[54:55], v24 offset:30208\n"
    "s_waitcnt lgkmcnt(8)\n"
    "v_mfma_f32_16x16x32_bf16 v[80:83], a[32:35], v[208:211], v[80:83]\n"
    "ds_read_b64_tr_b16 a[56:57], v25 offset:17920\n"
    "v_mfma_f32_16x16x32_bf16 v[80:83], a[36:39], v[212:215], v[80:83]\n"
    "s_xor_b32 s87, s46, 0x3800\n"
    "s_add_u32 s87, s87, s48\n"
    "ds_read_b64_tr_b16 a[58:59], v25 offset:22016\n"
    "v_mfma_f32_16x16x32_bf16 v[112:115], a[36:39], v[220:223], v[112:115]\n"
    "ds_read_b64_tr_b16 a[60:61], v25 offset:26112\n"
    "buffer_load_dwordx4 a[112:115], v2, s[24:27], s87 offen offset:0 sc1\n"
    "v_mfma_f32_16x16x32_bf16 v[112:115], a[32:35], v[216:219], v[112:115]\n"
    "ds_read_b64_tr_b16 a[62:63], v25 offset:30208\n"
    "s_waitcnt lgkmcnt(8)\n"
    "v_mfma_f32_16x16x32_bf16 v[116:119], a[40:43], v[216:219], v[116:119]\n"
    "s_nop 0\n"
    "s_barrier\n"
    "ds_read_b64_tr_b16 a[0:1], v24 offset:32768\n"
    "v_mfma_f32_16x16x32_bf16 v[116:119], a[44:47], v[220:223], v[116:119]\n"
    "ds_read_b64_tr_b16 a[2:3], v24 offset:36864\n"
    "v_mfma_f32_16x16x32_bf16 v[84:87], a[44:47], v[212:215], v[84:87]\n"
    "ds_read_b64_tr_b16 a[4:5], v24 offset:40960\n"
    "buffer_load_dwordx4 a[116:119], v2, s[24:27], s87 offen offset:1024 sc1\n"
    "v_mfma_f32_16x16x32_bf16 v[84:87], a[40:43], v[208:211], v[84:87]\n"
    "ds_read_b64_tr_b16 a[6:7], v24 offset:45056\n"
    "s_waitcnt lgkmcnt(8)\n"
    "v_mfma_f32_16x16x32_bf16 v[88:91], a[48:51], v[208:211], v[88:91]\n"
    "ds_read_b64_tr_b16 a[8:9], v25 offset:32768\n"
    "v_mfma_f32_16x16x32_bf16 v[88:91], a[52:55], v[212:215], v[88:91]\n"
    "s_add_u32 s88, s87, 0x2c000\n"
    "ds_read_b64_tr_b16 a[10:11], v25 offset:36864\n"
    "v_mfma_f32_16x16x32_bf16 v[120:123], a[52:55], v[220:223], v[120:123]\n"
    "ds_read_b64_tr_b16 a[12:13], v25 offset:40960\n"
    "buffer_load_dwordx4 a[120:123], v2, s[24:27], s88 offen offset:0 sc1\n"
    "v_mfma_f32_16x16x32_bf16 v[120:123], a[48:51], v[216:219], v[120:123]\n"
    "ds_read_b64_tr_b16 a[14:15], v25 offset:45056\n"
    "s_waitcnt lgkmcnt(8)\n"
    "v_mfma_f32_16x16x32_bf16 v[124:127], a[56:59], v[216:219], v[124:127]\n"
    "ds_read_b64_tr_b16 a[16:17], v24 offset:33280\n"
    "v_mfma_f32_16x16x32_bf16 v[124:127], a[60:63], v[220:223], v[124:127]\n"
    "ds_read_b64_tr_b16 a[18:19], v24 offset:37376\n"
    "v_mfma_f32_16x16x32_bf16 v[92:95], a[60:63], v[212:215], v[92:95]\n"
    "ds_read_b64_tr_b16 a[20:21], v24 offset:41472\n"
    "buffer_load_dwordx4 a[124:127], v2, s[24:27], s88 offen offset:1024 sc1\n"
    "v_mfma_f32_16x16x32_bf16 v[92:95], a[56:59], v[208:211], v[92:95]\n"
    "ds_read_b64_tr_b16 a[22:23], v24 offset:45568\n"
    "s_waitcnt vmcnt(16)\n"
    "v_cmp_ne_u64 vcc, s[90:91], v[14:15]\n"
    "s_and_b32 s68, vcc_lo, 0xf0f0f0f0\n"
    "s_cbranch_scc0 L_po2ok%=\n"
    "s_mov_b32 s86, 0\n"
    "buffer_load_dwordx2 v[14:15], v21, s[24:27], s51 offen offset:64 sc1\n"
    "s_sleep 5\n"
    "buffer_load_dwordx2 v[10:11], v21, s[24:27], s51 offen offset:64 sc1\n"
    "s_sleep 5\n"
    "buffer_load_dwordx2 v[12:13], v21, s[24:27], s51 offen offset:64 sc1\n"
    "s_sleep 5\n"
    "L_po2%=:\n"
    "s_waitcnt vmcnt(2)\n"
    "v_cmp_ne_u64 vcc, s[90:91], v[14:15]\n"
    "s_and_b32 s68, vcc_lo, 0xf0f0f0f0\n"
    "s_cbranch_scc0 L_po2ok%=\n"
    "buffer_load_dwordx2 v[14:15], v21, s[24:27], s51 offen offset:64 sc1\n"
    "s_waitcnt vmcnt(2)\n"
    "v_cmp_ne_u64 vcc, s[90:91], v[10:11]\n"
    "s_and_b32 s68, vcc_lo, 0xf0f0f0f0\n"
    "s_cbranch_scc0 L_po2ok%=\n"
    "buffer_load_dwordx2 v[10:11], v21, s[24:27], s51 offen offset:64 sc1\n"
    "s_waitcnt vmcnt(2)\n"
    "v_cmp_ne_u64 vcc, s[90:91], v[12:13]\n"
    "s_and_b32 s68, vcc_lo, 0xf0f0f0f0\n"
    "s_cbranch_scc0 L_po2ok%=\n"
    "buffer_load_dwordx2 v[12:13], v21, s[24:27], s51 offen offset:64 sc1\n"
    "s_add_u32 s86, s86, 1\n"
    "s_cmp_lt_u32 s86, 0x100000\n"
    "s_cbranch_scc1 L_po2%=\n"
    "L_po2ok%=:\n"
    "v_and_b32 v36, 3, v1\n"
    "v_lshlrev_b32 v21, 3, v36\n"
    "s_waitcnt lgkmcnt(8)\n"
    "v_mfma_f32_16x16x32_bf16 v[64:67], a[0:3], v[224:227], v[64:67]\n"
    "ds_read_b64_tr_b16 a[24:25], v25 offset:33280\n"
    "v_mfma_f32_16x16x32_bf16 v[64:67], a[4:7], v[228:231], v[64:67]\n"
    "s_xor_b32 s87, s46, 0x4000\n"
    "s_add_u32 s87, s87, s48\n"
    "ds_read_b64_tr_b16 a[26:27], v25 offset:37376\n"
    "v_mfma_f32_16x16x32_bf16 v[96:99], a[4:7], v[236:239], v[96:99]\n"
    "ds_read_b64_tr_b16 a[28:29], v25 offset:41472\n"
    "buffer_load_dwordx4 a[128:131], v2, s[24:27], s87 offen offset:0 sc1\n"
    "v_mfma_f32_16x16x32_bf16 v[96:99], a[0:3], v[232:235], v[96:99]\n"
    "ds_read_b64_tr_b16 a[30:31], v25 offset:45568\n"
    "s_waitcnt lgkmcnt(8)\n"
    "v_mfma_f32_16x16x32_bf16 v[100:103], a[8:11], v[232:235], v[100:103]\n"
    "ds_read_b64_tr_b16 a[32:33], v24 offset:33792\n"
    "v_mfma_f32_16x16x32_bf16 v[100:103], a[12:15], v[236:239], v[100:103]\n"
    "ds_read_b64_tr_b16 a[34:35], v24 offset:37888\n"
    "v_mfma_f32_16x16x32_bf16 v[68:71], a[12:15], v[228:231], v[68:71]\n"
    "ds_read_b64_tr_b16 a[36:37], v24 offset:41984\n"
    "buffer_load_dwordx4 a[132:135], v2, s[24:27], s87 offen offset:1024 sc1\n"
    "v_mfma_f32_16x16x32_bf16 v[68:71], a[8:11], v[224:227], v[68:71]\n"
    "ds_read_b64_tr_b16 a[38:39], v24 offset:46080\n"
    "s_waitcnt lgkmcnt(8)\n"
    "v_mfma_f32_16x16x32_bf16 v[72:75], a[16:19], v[224:227], v[72:75]\n"
    "s_add_u32 s96, s52, 0\n"
    "ds_read_b64_tr_b16 a[40:41], v25 offset:33792\n"
    "v_mfma_f32_16x16x32_bf16 v[72:75], a[20:23], v[228:231], v[72:75]\n"
    "ds_read_b64_tr_b16 a[42:43], v25 offset:37888\n"
    "v_mfma_f32_16x16x32_bf16 v[104:107], a[20:23], v[236:239], v[104:107]\n"
    "ds_read_b64_tr_b16 a[44:45], v25 offset:41984\n"
    "buffer_store_dwordx4 v[136:139], v39, s[24:27], s96 offen offset:0 sc1\n"
    "v_mfma_f32_16x16x32_bf16 v[104:107], a[16:19], v[232:235], v[104:107]\n"
    "ds_read_b64_tr_b16 a[46:47], v25 offset:46080\n"
    "s_waitcnt lgkmcnt(8)\n"
    "v_mfma_f32_16x16x32_bf16 v[108:111], a[24:27], v[232:235], v[108:111]\n"
    "ds_read_b64_tr_b16 a[48:49], v24 offset:34304\n"
    "v_mfma_f32_16x16x32_bf16 v[108:111], a[28:31], v[236:239], v[108:111]\n"
    "s_add_u32 s88, s87, 0x2c000\n"
    "ds_read_b64_tr_b16 a[50:51], v24 offset:38400\n"
    "v_mfma_f32_16x16x32_bf16 v[76:79], a[28:31], v[228:231], v[76:79]\n"
    "ds_read_b64_tr_b16 a[52:53], v24 offset:42496\n"
    "buffer_load_dwordx4 a[136:139], v2, s[24:27], s88 offen offset:0 sc1\n"
    "v_mfma_f32_16x16x32_bf16 v[76:79], a[24:27], v[224:227], v[76:79]\n"
    "ds_read_b64_tr_b16 a[54:55], v24 offset:46592\n"
    "s_waitcnt lgkmcnt(8)\n"
    "v_mfma_f32_16x16x32_bf16 v[80:83], a[32:35], v[224:227], v[80:83]\n"
    "ds_read_b64_tr_b16 a[56:57], v25 offset:34304\n"
    "v_mfma_f32_16x16x32_bf16 v[80:83], a[36:39], v[228:231], v[80:83]\n"
    "ds_read_b64_tr_b16 a[58:59], v25 offset:38400\n"
    "v_mfma_f32_16x16x32_bf16 v[112:115], a[36:39], v[236:239], v[112:115]\n"
    "ds_read_b64_tr_b16 a[60:61], v25 offset:42496\n"
    "buffer_load_dwordx4 a[140:143], v2, s[24:27], s88 offen offset:1024 sc1\n"
    "v_mfma_f32_16x16x32_bf16 v[112:115], a[32:35], v[232:235], v[112:115]\n"
    "ds_read_b64_tr_b16 a[62:63], v25 offset:46592\n"
    "s_waitcnt lgkmcnt(8)\n"
    "v_mfma_f32_16x16x32_bf16 v[116:119], a[40:43], v[232:235], v[116:119]\n"
    "ds_read_b64_tr_b16 a[0:1], v24 offset:49152\n"
    "v_mfma_f32_16x16x32_bf16 v[116:119], a[44:47], v[236:239], v[116:119]\n"
    "ds_read_b64_tr_b16 a[2:3], v24 offset:53248\n"
    "v_mfma_f32_16x16x32_bf16 v[84:87], a[44:47], v[228:231], v[84:87]\n"
    "ds_read_b64_tr_b16 a[4:5], v24 offset:57344\n"
    "buffer_store_dwordx4 v[168:171], v39, s[24:27], s96 offen offset:1024 sc1\n"
    "v_mfma_f32_16x16x32_bf16 v[84:87], a[40:43], v[224:227], v[84:87]\n"
    "s_add_u32 s96, s53, 0\n"
    "ds_read_b64_tr_b16 a[6:7], v24 offset:61440\n"
    "s_waitcnt lgkmcnt(8)\n"
    "v_mfma_f32_16x16x32_bf16 v[88:91], a[48:51], v[224:227], v[88:91]\n"
    "ds_read_b64_tr_b16 a[8:9], v25 offset:49152\n"
    "v_mfma_f32_16x16x32_bf16 v[88:91], a[52:55], v[228:231], v[88:91]\n"
    "s_xor_b32 s87, s46, 0x4800\n"
    "s_add_u32 s87, s87, s48\n"
    "ds_read_b64_tr_b16 a[10:11], v25 offset:53248\n"
    "v_mfma_f32_16x16x32_bf16 v[120:123], a[52:55], v[236:239], v[120:123]\n"
    "ds_read_b64_tr_b16 a[12:13], v25 offset:57344\n"
    "buffer_load_dwordx4 a[144:147], v2, s[24:27], s87 offen offset:0 sc1\n"
    "v_mfma_f32_16x16x32_bf16 v[120:123], a[48:51], v[232:235], v[120:123]\n"
    "ds_read_b64_tr_b16 a[14:15], v25 offset:61440\n"
    "s_waitcnt lgkmcnt(8)\n"
    "v_mfma_f32_16x16x32_bf16 v[124:127], a[56:59], v[232:235], v[124:127]\n"
    "ds_read_b64_tr_b16 a[16:17], v24 offset:49664\n"
    "v_mfma_f32_16x16x32_bf16 v[124:127], a[60:63], v[236:239], v[124:127]\n"
    "ds_read_b64_tr_b16 a[18:19], v24 offset:53760\n"
    "v_mfma_f32_16x16x32_bf16 v[92:95], a[60:63], v[228:231], v[92:95]\n"
    "ds_read_b64_tr_b16 a[20:21], v24 offset:57856\n"
    "buffer_load_dwordx4 a[148:151], v2, s[24:27], s87 offen offset:1024 sc1\n"
    "v_mfma_f32_16x16x32_bf16 v[92:95], a[56:59], v[224:227], v[92:95]\n"
    "ds_read_b64_tr_b16 a[22:23], v24 offset:61952\n"
    "s_waitcnt lgkmcnt(8)\n"
    "v_mfma_f32_16x16x32_bf16 v[64:67], a[0:3], v[240:243], v[64:67]\n"
    "ds_read_b64_tr_b16 a[24:25], v25 offset:49664\n"
    "v_mfma_f32_16x16x32_bf16 v[64:67], a[4:7], v[244:247], v[64:67]\n"
    "ds_read_b64_tr_b16 a[26:27], v25 offset:53760\n"
    "v_mfma_f32_16x16x32_bf16 v[96:99], a[4:7], v[252:255], v[96:99]\n"
    "ds_read_b64_tr_b16 a[28:29], v25 offset:57856\n"
    "buffer_store_dwordx4 v[144:147], v39, s[24:27], s96 offen offset:0 sc1\n"
    "v_mfma_f32_16x16x32_bf16 v[96:99], a[0:3], v[248:251], v[96:99]\n"
    "ds_read_b64_tr_b16 a[30:31], v25 offset:61952\n"
    "s_waitcnt lgkmcnt(8)\n"
    "v_mfma_f32_16x16x32_bf16 v[100:103], a[8:11], v[248:251], v[100:103]\n"
    "ds_read_b64_tr_b16 a[32:33], v24 offset:50176\n"
    "v_mfma_f32_16x16x32_bf16 v[100:103], a[12:15], v[252:255], v[100:103]\n"
    "s_add_u32 s88, s87, 0x2c000\n"
    "ds_read_b64_tr_b16 a[34:35], v24 offset:54272\n"
    "v_mfma_f32_16x16x32_bf16 v[68:71], a[12:15], v[244:247], v[68:71]\n"
    "ds_read_b64_tr_b16 a[36:37], v24 offset:58368\n"
    "buffer_load_dwordx4 a[152:155], v2, s[24:27], s88 offen offset:0 sc1\n"
    "v_mfma_f32_16x16x32_bf16 v[68:71], a[8:11], v[240:243], v[68:71]\n"
    "ds_read_b64_tr_b16 a[38:39], v24 offset:62464\n"
    "s_waitcnt lgkmcnt(8)\n"
    "v_mfma_f32_16x16x32_bf16 v[72:75], a[16:19], v[240:243], v[72:75]\n"
    "ds_read_b64_tr_b16 a[40:41], v25 offset:50176\n"
    "v_mfma_f32_16x16x32_bf16 v[72:75], a[20:23], v[244:247], v[72:75]\n"
    "ds_read_b64_tr_b16 a[42:43], v25 offset:54272\n"
    "v_mfma_f32_16x16x32_bf16 v[104:107], a[20:23], v[252:255], v[104:107]\n"
    "ds_read_b64_tr_b16 a[44:45], v25 offset:58368\n"
    "buffer_load_dwordx4 a[156:159], v2, s[24:27], s88 offen offset:1024 sc1\n"
    "v_mfma_f32_16x16x32_bf16 v[104:107], a[16:19], v[248:251], v[104:107]\n"
    "ds_read_b64_tr_b16 a[46:47], v25 offset:62464\n"
    "s_waitcnt lgkmcnt(8)\n"
    "v_mfma_f32_16x16x32_bf16 v[108:111], a[24:27], v[248:251], v[108:111]\n"
    "ds_read_b64_tr_b16 a[48:49], v24 offset:50688\n"
    "v_mfma_f32_16x16x32_bf16 v[108:111], a[28:31], v[252:255], v[108:111]\n"
    "ds_read_b64_tr_b16 a[50:51], v24 offset:54784\n"
    "v_mfma_f32_16x16x32_bf16 v[76:79], a[28:31], v[244:247], v[76:79]\n"
    "ds_read_b64_tr_b16 a[52:53], v24 offset:58880\n"
    "buffer_store_dwordx4 v[176:179], v39, s[24:27], s96 offen offset:1024 sc1\n"
    "v_mfma_f32_16x16x32_bf16 v[76:79], a[24:27], v[240:243], v[76:79]\n"
    "s_add_u32 s96, s54, 0\n"
    "ds_read_b64_tr_b16 a[54:55], v24 offset:62976\n"
    "s_waitcnt lgkmcnt(8)\n"
    "v_mfma_f32_16x16x32_bf16 v[80:83], a[32:35], v[240:243], v[80:83]\n"
    "ds_read_b64_tr_b16 a[56:57], v25 offset:50688\n"
    "v_mfma_f32_16x16x32_bf16 v[80:83], a[36:39], v[244:247], v[80:83]\n"
    "s_xor_b32 s87, s46, 0x5000\n"
    "s_add_u32 s87, s87, s48\n"
    "ds_read_b64_tr_b16 a[58:59], v25 offset:54784\n"
    "v_mfma_f32_16x16x32_bf16 v[112:115], a[36:39], v[252:255], v[112:115]\n"
    "ds_read_b64_tr_b16 a[60:61], v25 offset:58880\n"
    "buffer_load_dwordx4 a[160:163], v2, s[24:27], s87 offen offset:0 sc1\n"
    "v_mfma_f32_16x16x32_bf16 v[112:115], a[32:35], v[248:251], v[112:115]\n"
    "ds_read_b64_tr_b16 a[62:63], v25 offset:62976\n"
    "s_waitcnt lgkmcnt(8)\n"
    "v_mfma_f32_16x16x32_bf16 v[116:119], a[40:43], v[248:251], v[116:119]\n"
    "s_nop 0\n"
    "s_barrier\n"
    "ds_read_b64_tr_b16 a[0:1], v26 offset:0\n"
    "v_mfma_f32_16x16x32_bf16 v[116:119], a[44:47], v[252:255], v[116:119]\n"
    "ds_read_b64_tr_b16 a[2:3], v26 offset:4096\n"
    "v_mfma_f32_16x16x32_bf16 v[84:87], a[44:47], v[244:247], v[84:87]\n"
    "ds_read_b64_tr_b16 a[4:5], v26 offset:8192\n"
    "buffer_load_dwordx4 a[164:167], v2, s[24:27], s87 offen offset:1024 sc1\n"
    "v_mfma_f32_16x16x32_bf16 v[84:87], a[40:43], v[240:243], v[84:87]\n"
    "ds_read_b64_tr_b16 a[6:7], v26 offset:12288\n"
    "s_waitcnt lgkmcnt(8)\n"
    "v_mfma_f32_16x16x32_bf16 v[88:91], a[48:51], v[240:243], v[88:91]\n"
    "ds_read_b64_tr_b16 a[8:9], v27 offset:0\n"
    "v_mfma_f32_16x16x32_bf16 v[88:91], a[52:55], v[244:247], v[88:91]\n"
    "ds_read_b64_tr_b16 a[10:11], v27 offset:4096\n"
    "v_mfma_f32_16x16x32_bf16 v[120:123], a[52:55], v[252:255], v[120:123]\n"
    "ds_read_b64_tr_b16 a[12:13], v27 offset:8192\n"
    "buffer_store_dwordx4 v[152:155], v39, s[24:27], s96 offen offset:0 sc1\n"
    "v_mfma_f32_16x16x32_bf16 v[120:123], a[48:51], v[248:251], v[120:123]\n"
    "ds_read_b64_tr_b16 a[14:15], v27 offset:12288\n"
    "s_waitcnt lgkmcnt(8)\n"
    "v_mfma_f32_16x16x32_bf16 v[124:127], a[56:59], v[248:251], v[124:127]\n"
    "ds_read_b64_tr_b16 a[16:17], v26 offset:512\n"
    "v_mfma_f32_16x16x32_bf16 v[124:127], a[60:63], v[252:255], v[124:127]\n"
    "s_add_u32 s88, s87, 0x2c000\n"
    "ds_read_b64_tr_b16 a[18:19], v26 offset:4608\n"
    "v_mfma_f32_16x16x32_bf16 v[92:95], a[60:63], v[244:247], v[92:95]\n"
    "ds_read_b64_tr_b16 a[20:21], v26 offset:8704\n"
    "buffer_load_dwordx4 a[168:171], v2, s[24:27], s88 offen offset:0 sc1\n"
    "v_mfma_f32_16x16x32_bf16 v[92:95], a[56:59], v[240:243], v[92:95]\n"
    "ds_read_b64_tr_b16 a[22:23], v26 offset:12800\n"
    "s_waitcnt vmcnt(28) lgkmcnt(8)\n"
    "v_mfma_f32_16x16x32_bf16 v[64:67], a[0:3], a[64:67], v[64:67]\n"
    "ds_read_b64_tr_b16 a[24:25], v27 offset:512\n"
    "v_mfma_f32_16x16x32_bf16 v[64:67], a[4:7], a[68:71], v[64:67]\n"
    "ds_read_b64_tr_b16 a[26:27], v27 offset:4608\n"
    "v_mfma_f32_16x16x32_bf16 v[96:99], a[4:7], a[76:79], v[96:99]\n"
    "ds_read_b64_tr_b16 a[28:29], v27 offset:8704\n"
    "buffer_load_dwordx4 a[172:175], v2, s[24:27], s88 offen offset:1024 sc1\n"
    "v_mfma_f32_16x16x32_bf16 v[96:99], a[0:3], a[72:75], v[96:99]\n"
    "ds_read_b64_tr_b16 a[30:31], v27 offset:12800\n"
    "s_waitcnt lgkmcnt(8)\n"
    "v_mfma_f32_16x16x32_bf16 v[100:103], a[8:11], a[72:75], v[100:103]\n"
    "ds_read_b64_tr_b16 a[32:33], v26 offset:1024\n"
    "v_mfma_f32_16x16x32_bf16 v[100:103], a[12:15], a[76:79], v[100:103]\n"
    "ds_read_b64_tr_b16 a[34:35], v26 offset:5120\n"
    "v_mfma_f32_16x16x32_bf16 v[68:71], a[12:15], a[68:71], v[68:71]\n"
    "ds_read_b64_tr_b16 a[36:37], v26 offset:9216\n"
    "buffer_store_dwordx4 v[184:187], v39, s[24:27], s96 offen offset:1024 sc1\n"
    "v_mfma_f32_16x16x32_bf16 v[68:71], a[8:11], a[64:67], v[68:71]\n"
    "ds_read_b64_tr_b16 a[38:39], v26 offset:13312\n"
    "s_waitcnt lgkmcnt(8)\n"
    "v_mfma_f32_16x16x32_bf16 v[72:75], a[16:19], a[64:67], v[72:75]\n"
    "ds_read_b64_tr_b16 a[40:41], v27 offset:1024\n"
    "v_mfma_f32_16x16x32_bf16 v[72:75], a[20:23], a[68:71], v[72:75]\n"
    "s_add_u32 m0, s65, 0xc000\n"
    "s_xor_b32 s69, s89, 0x80000\n"
    "ds_read_b64_tr_b16 a[42:43], v27 offset:5120\n"
    "v_mfma_f32_16x16x32_bf16 v[104:107], a[20:23], a[76:79], v[104:107]\n"
    "ds_read_b64_tr_b16 a[44:45], v27 offset:9216\n"
    "buffer_load_dwordx4 v16, s[32:35], s69 offen offset:0 lds\n"
    "v_mfma_f32_16x16x32_bf16 v[104:107], a[16:19], a[72:75], v[104:107]\n"
    "ds_read_b64_tr_b16 a[46:47], v27 offset:13312\n"
    "s_waitcnt lgkmcnt(8)\n"
    "v_mfma_f32_16x16x32_bf16 v[108:111], a[24:27], a[72:75], v[108:111]\n"
    "ds_read_b64_tr_b16 a[48:49], v26 offset:1536\n"
    "v_mfma_f32_16x16x32_bf16 v[108:111], a[28:31], a[76:79], v[108:111]\n"
    "ds_read_b64_tr_b16 a[50:51], v26 offset:5632\n"
    "v_mfma_f32_16x16x32_bf16 v[76:79], a[28:31], a[68:71], v[76:79]\n"
    "ds_read_b64_tr_b16 a[52:53], v26 offset:9728\n"
    "buffer_load_dwordx4 v17, s[32:35], s69 offen offset:1024 lds\n"
    "v_mfma_f32_16x16x32_bf16 v[76:79], a[24:27], a[64:67], v[76:79]\n"
    "ds_read_b64_tr_b16 a[54:55], v26 offset:13824\n"
    "s_waitcnt lgkmcnt(8)\n"
    "v_mfma_f32_16x16x32_bf16 v[80:83], a[32:35], a[64:67], v[80:83]\n"
    "ds_read_b64_tr_b16 a[56:57], v27 offset:1536\n"
    "v_mfma_f32_16x16x32_bf16 v[80:83], a[36:39], a[68:71], v[80:83]\n"
    "ds_read_b64_tr_b16 a[58:59], v27 offset:5632\n"
    "v_mfma_f32_16x16x32_bf16 v[112:115], a[36:39], a[76:79], v[112:115]\n"
    "ds_read_b64_tr_b16 a[60:61], v27 offset:9728\n"
    "buffer_load_dwordx4 v18, s[32:35], s69 offen offset:2048 lds\n"
    "v_mfma_f32_16x16x32_bf16 v[112:115], a[32:35], a[72:75], v[112:115]\n"
    "ds_read_b64_tr_b16 a[62:63], v27 offset:13824\n"
    "s_waitcnt lgkmcnt(8)\n"
    "v_mfma_f32_16x16x32_bf16 v[116:119], a[40:43], a[72:75], v[116:119]\n"
    "ds_read_b64_tr_b16 a[0:1], v26 offset:16384\n"
    "v_mfma_f32_16x16x32_bf16 v[116:119], a[44:47], a[76:79], v[116:119]\n"
    "ds_read_b64_tr_b16 a[2:3], v26 offset:20480\n"
    "v_mfma_f32_16x16x32_bf16 v[84:87], a[44:47], a[68:71], v[84:87]\n"
    "ds_read_b64_tr_b16 a[4:5], v26 offset:24576\n"
    "buffer_load_dwordx4 v19, s[32:35], s69 offen offset:3072 lds\n"
    "v_mfma_f32_16x16x32_bf16 v[84:87], a[40:43], a[64:67], v[84:87]\n"
    "ds_read_b64_tr_b16 a[6:7], v26 offset:28672\n"
    "s_waitcnt lgkmcnt(8)\n"
    "v_mfma_f32_16x16x32_bf16 v[88:91], a[48:51], a[64:67], v[88:91]\n"
    "ds_read_b64_tr_b16 a[8:9], v27 offset:16384\n"
    "v_mfma_f32_16x16x32_bf16 v[88:91], a[52:55], a[68:71], v[88:91]\n"
    "s_add_u32 m0, s65, 0x10000\n"
    "s_xor_b32 s69, s89, 0x90000\n"
    "ds_read_b64_tr_b16 a[10:11], v27 offset:20480\n"
    "v_mfma_f32_16x16x32_bf16 v[120:123], a[52:55], a[76:79], v[120:123]\n"
    "ds_read_b64_tr_b16 a[12:13], v27 offset:24576\n"
    "buffer_load_dwordx4 v16, s[32:35], s69 offen offset:0 lds\n"
    "v_mfma_f32_16x16x32_bf16 v[120:123], a[48:51], a[72:75], v[120:123]\n"
    "ds_read_b64_tr_b16 a[14:15], v27 offset:28672\n"
    "s_waitcnt lgkmcnt(8)\n"
    "v_mfma_f32_16x16x32_bf16 v[124:127], a[56:59], a[72:75], v[124:127]\n"
    "ds_read_b64_tr_b16 a[16:17], v26 offset:16896\n"
    "v_mfma_f32_16x16x32_bf16 v[124:127], a[60:63], a[76:79], v[124:127]\n"
    "ds_read_b64_tr_b16 a[18:19], v26 offset:20992\n"
    "v_mfma_f32_16x16x32_bf16 v[92:95], a[60:63], a[68:71], v[92:95]\n"
    "ds_read_b64_tr_b16 a[20:21], v26 offset:25088\n"
    "buffer_load_dwordx4 v17, s[32:35], s69 offen offset:1024 lds\n"
    "v_mfma_f32_16x16x32_bf16 v[92:95], a[56:59], a[64:67], v[92:95]\n"
    "ds_read_b64_tr_b16 a[22:23], v26 offset:29184\n"
    "s_waitcnt vmcnt(32) lgkmcnt(8)\n"
    "v_mfma_f32_16x16x32_bf16 v[64:67], a[0:3], a[80:83], v[64:67]\n"
    "ds_read_b64_tr_b16 a[24:25], v27 offset:16896\n"
    "v_mfma_f32_16x16x32_bf16 v[64:67], a[4:7], a[84:87], v[64:67]\n"
    "ds_read_b64_tr_b16 a[26:27], v27 offset:20992\n"
    "v_mfma_f32_16x16x32_bf16 v[96:99], a[4:7], a[92:95], v[96:99]\n"
    "ds_read_b64_tr_b16 a[28:29], v27 offset:25088\n"
    "buffer_load_dwordx4 v18, s[32:35], s69 offen offset:2048 lds\n"
    "v_mfma_f32_16x16x32_bf16 v[96:99], a[0:3], a[88:91], v[96:99]\n"
    "ds_read_b64_tr_b16 a[30:31], v27 offset:29184\n"
    "s_waitcnt lgkmcnt(8)\n"
    "v_mfma_f32_16x16x32_bf16 v[100:103], a[8:11], a[88:91], v[100:103]\n"
    "ds_read_b64_tr_b16 a[32:33], v26 offset:17408\n"
    "v_mfma_f32_16x16x32_bf16 v[100:103], a[12:15], a[92:95], v[100:103]\n"
    "ds_read_b64_tr_b16 a[34:35], v26 offset:21504\n"
    "v_mfma_f32_16x16x32_bf16 v[68:71], a[12:15], a[84:87], v[68:71]\n"
    "ds_read_b64_tr_b16 a[36:37], v26 offset:25600\n"
    "buffer_load_dwordx4 v19, s[32:35], s69 offen offset:3072 lds\n"
    "v_mfma_f32_16x16x32_bf16 v[68:71], a[8:11], a[80:83], v[68:71]\n"
    "ds_read_b64_tr_b16 a[38:39], v26 offset:29696\n"
    "s_waitcnt lgkmcnt(8)\n"
    "v_mfma_f32_16x16x32_bf16 v[72:75], a[16:19], a[80:83], v[72:75]\n"
    "ds_read_b64_tr_b16 a[40:41], v27 offset:17408\n"
    "v_mfma_f32_16x16x32_bf16 v[72:75], a[20:23], a[84:87], v[72:75]\n"
    "s_xor_b32 s87, s46, 0x5800\n"
    "s_add_u32 s87, s87, s48\n"
    "ds_read_b64_tr_b16 a[42:43], v27 offset:21504\n"
    "v_mfma_f32_16x16x32_bf16 v[104:107], a[20:23], a[92:95], v[104:107]\n"
    "ds_read_b64_tr_b16 a[44:45], v27 offset:25600\n"
    "buffer_load_dwordx4 a[176:179], v2, s[24:27], s87 offen offset:0 sc1\n"
    "v_mfma_f32_16x16x32_bf16 v[104:107], a[16:19], a[88:91], v[104:107]\n"
    "ds_read_b64_tr_b16 a[46:47], v27 offset:29696\n"
    "s_waitcnt lgkmcnt(8)\n"
    "v_mfma_f32_16x16x32_bf16 v[108:111], a[24:27], a[88:91], v[108:111]\n"
    "ds_read_b64_tr_b16 a[48:49], v26 offset:17920\n"
    "v_mfma_f32_16x16x32_bf16 v[108:111], a[28:31], a[92:95], v[108:111]\n"
    "ds_read_b64_tr_b16 a[50:51], v26 offset:22016\n"
    "v_mfma_f32_16x16x32_bf16 v[76:79], a[28:31], a[84:87], v[76:79]\n"
    "ds_read_b64_tr_b16 a[52:53], v26 offset:26112\n"
    "buffer_load_dwordx4 a[180:183], v2, s[24:27], s87 offen offset:1024 sc1\n"
    "v_mfma_f32_16x16x32_bf16 v[76:79], a[24:27], a[80:83], v[76:79]\n"
    "ds_read_b64_tr_b16 a[54:55], v26 offset:30208\n"
    "s_waitcnt lgkmcnt(8)\n"
    "v_mfma_f32_16x16x32_bf16 v[80:83], a[32:35], a[80:83], v[80:83]\n"
    "ds_read_b64_tr_b16 a[56:57], v27 offset:17920\n"
    "v_mfma_f32_16x16x32_bf16 v[80:83], a[36:39], a[84:87], v[80:83]\n"
    "s_add_u32 s88, s87, 0x2c000\n"
    "ds_read_b64_tr_b16 a[58:59], v27 offset:22016\n"
    "v_mfma_f32_16x16x32_bf16 v[112:115], a[36:39], a[92:95], v[112:115]\n"
    "ds_read_b64_tr_b16 a[60:61], v27 offset:26112\n"
    "buffer_load_dwordx4 a[184:187], v2, s[24:27], s88 offen offset:0 sc1\n"
    "v_mfma_f32_16x16x32_bf16 v[112:115], a[32:35], a[88:91], v[112:115]\n"
    "ds_read_b64_tr_b16 a[62:63], v27 offset:30208\n"
    "s_waitcnt lgkmcnt(8)\n"
    "v_mfma_f32_16x16x32_bf16 v[116:119], a[40:43], a[88:91], v[116:119]\n"
    "s_waitcnt vmcnt(11)\n"
    "s_barrier\n"
    "buffer_store_dwordx2 v[32:33], v20, s[24:27], s50 offen offset:128 sc1\n"
    "ds_read_b64_tr_b16 a[0:1], v22 offset:33280\n"
    "v_mfma_f32_16x16x32_bf16 v[116:119], a[44:47], a[92:95], v[116:119]\n"
    "ds_read_b64_tr_b16 a[2:3], v22 offset:37376\n"
    "v_mfma_f32_16x16x32_bf16 v[84:87], a[44:47], a[84:87], v[84:87]\n"
    "ds_read_b64_tr_b16 a[4:5], v22 offset:41472\n"
    "buffer_load_dwordx4 a[188:191], v2, s[24:27], s88 offen offset:1024 sc1\n"
    "v_mfma_f32_16x16x32_bf16 v[84:87], a[40:43], a[80:83], v[84:87]\n"
    "ds_read_b64_tr_b16 a[6:7], v22 offset:45568\n"
    "s_waitcnt lgkmcnt(8)\n"
    "v_mfma_f32_16x16x32_bf16 v[88:91], a[48:51], a[80:83], v[88:91]\n"
    "ds_read_b64_tr_b16 a[8:9], v23 offset:33280\n"
    "v_mfma_f32_16x16x32_bf16 v[88:91], a[52:55], a[84:87], v[88:91]\n"
    "s_xor_b32 s87, s46, 0x6000\n"
    "s_add_u32 s87, s87, s48\n"
    "ds_read_b64_tr_b16 a[10:11], v23 offset:37376\n"
    "v_mfma_f32_16x16x32_bf16 v[120:123], a[52:55], a[92:95], v[120:123]\n"
    "ds_read_b64_tr_b16 a[12:13], v23 offset:41472\n"
    "buffer_load_dwordx4 a[192:195], v2, s[24:27], s87 offen offset:0 sc1\n"
    "v_mfma_f32_16x16x32_bf16 v[120:123], a[48:51], a[88:91], v[120:123]\n"
    "ds_read_b64_tr_b16 a[14:15], v23 offset:45568\n"
    "s_waitcnt lgkmcnt(8)\n"
    "v_mfma_f32_16x16x32_bf16 v[124:127], a[56:59], a[88:91], v[124:127]\n"
    "ds_read_b64_tr_b16 a[16:17], v22 offset:33792\n"
    "v_mfma_f32_16x16x32_bf16 v[124:127], a[60:63], a[92:95], v[124:127]\n"
    "ds_read_b64_tr_b16 a[18:19], v22 offset:37888\n"
    "v_mfma_f32_16x16x32_bf16 v[92:95], a[60:63], a[84:87], v[92:95]\n"
    "ds_read_b64_tr_b16 a[20:21], v22 offset:41984\n"
    "buffer_load_dwordx4 a[196:199], v2, s[24:27], s87 offen offset:1024 sc1\n"
    "v_mfma_f32_16x16x32_bf16 v[92:95], a[56:59], a[80:83], v[92:95]\n"
    "ds_read_b64_tr_b16 a[22:23], v22 offset:46080\n"
    "s_waitcnt lgkmcnt(8)\n"
    "v_mfma_f32_16x16x32_bf16 v[72:75], a[0:3], a[96:99], v[72:75]\n"
    "ds_read_b64_tr_b16 a[24:25], v23 offset:33792\n"
    "v_mfma_f32_16x16x32_bf16 v[72:75], a[4:7], a[100:103], v[72:75]\n"
    "s_add_u32 s88, s87, 0x2c000\n"
    "ds_read_b64_tr_b16 a[26:27], v23 offset:37888\n"
    "v_mfma_f32_16x16x32_bf16 v[104:107], a[4:7], a[108:111], v[104:107]\n"
    "ds_read_b64_tr_b16 a[28:29], v23 offset:41984\n"
    "buffer_load_dwordx4 a[200:203], v2, s[24:27], s88 offen offset:0 sc1\n"
    "v_mfma_f32_16x16x32_bf16 v[104:107], a[0:3], a[104:107], v[104:107]\n"
    "ds_read_b64_tr_b16 a[30:31], v23 offset:46080\n"
    "s_waitcnt lgkmcnt(8)\n"
    "v_mfma_f32_16x16x32_bf16 v[108:111], a[8:11], a[104:107], v[108:111]\n"
    "ds_read_b64_tr_b16 a[32:33], v22 offset:34304\n"
    "v_mfma_f32_16x16x32_bf16 v[108:111], a[12:15], a[108:111], v[108:111]\n"
    "ds_read_b64_tr_b16 a[34:35], v22 offset:38400\n"
    "v_mfma_f32_16x16x32_bf16 v[76:79], a[12:15], a[100:103], v[76:79]\n"
    "ds_read_b64_tr_b16 a[36:37], v22 offset:42496\n"
    "buffer_load_dwordx4 a[204:207], v2, s[24:27], s88 offen offset:1024 sc1\n"
    "v_mfma_f32_16x16x32_bf16 v[76:79], a[8:11], a[96:99], v[76:79]\n"
    "ds_read_b64_tr_b16 a[38:39], v22 offset:46592\n"
    "s_waitcnt lgkmcnt(8)\n"
    "v_mfma_f32_16x16x32_bf16 v[80:83], a[16:19], a[96:99], v[80:83]\n"
    "ds_read_b64_tr_b16 a[40:41], v23 offset:34304\n"
    "v_mfma_f32_16x16x32_bf16 v[80:83], a[20:23], a[100:103], v[80:83]\n"
    "s_add_u32 m0, s65, 0\n"
    "s_xor_b32 s69, s89, 0xa0000\n"
    "ds_read_b64_tr_b16 a[42:43], v23 offset:38400\n"
    "v_mfma_f32_16x16x32_bf16 v[112:115], a[20:23], a[108:111], v[112:115]\n"
    "ds_read_b64_tr_b16 a[44:45], v23 offset:42496\n"
    "buffer_load_dwordx4 v16, s[32:35], s69 offen offset:0 lds\n"
    "v_mfma_f32_16x16x32_bf16 v[112:115], a[16:19], a[104:107], v[112:115]\n"
    "ds_read_b64_tr_b16 a[46:47], v23 offset:46592\n"
    "s_waitcnt lgkmcnt(8)\n"
    "v_mfma_f32_16x16x32_bf16 v[116:119], a[24:27], a[104:107], v[116:119]\n"
    "ds_read_b64_tr_b16 a[48:49], v22 offset:16896\n"
    "v_mfma_f32_16x16x32_bf16 v[116:119], a[28:31], a[108:111], v[116:119]\n"
    "ds_read_b64_tr_b16 a[50:51], v22 offset:20992\n"
    "v_mfma_f32_16x16x32_bf16 v[84:87], a[28:31], a[100:103], v[84:87]\n"
    "ds_read_b64_tr_b16 a[52:53], v22 offset:25088\n"
    "buffer_load_dwordx4 v17, s[32:35], s69 offen offset:1024 lds\n"
    "v_mfma_f32_16x16x32_bf16 v[84:87], a[24:27], a[96:99], v[84:87]\n"
    "ds_read_b64_tr_b16 a[54:55], v22 offset:29184\n"
    "s_waitcnt lgkmcnt(8)\n"
    "v_mfma_f32_16x16x32_bf16 v[88:91], a[32:35], a[96:99], v[88:91]\n"
    "ds_read_b64_tr_b16 a[56:57], v23 offset:16896\n"
    "v_mfma_f32_16x16x32_bf16 v[88:91], a[36:39], a[100:103], v[88:91]\n"
    "ds_read_b64_tr_b16 a[58:59], v23 offset:20992\n"
    "v_mfma_f32_16x16x32_bf16 v[120:123], a[36:39], a[108:111], v[120:123]\n"
    "ds_read_b64_tr_b16 a[60:61], v23 offset:25088\n"
    "buffer_load_dwordx4 v18, s[32:35], s69 offen offset:2048 lds\n"
    "v_mfma_f32_16x16x32_bf16 v[120:123], a[32:35], a[104:107], v[120:123]\n"
    "ds_read_b64_tr_b16 a[62:63], v23 offset:29184\n"
    "s_waitcnt lgkmcnt(8)\n"
    "v_mfma_f32_16x16x32_bf16 v[124:127], a[40:43], a[104:107], v[124:127]\n"
    "ds_read_b64_tr_b16 a[0:1], v22 offset:17408\n"
    "v_mfma_f32_16x16x32_bf16 v[124:127], a[44:47], a[108:111], v[124:127]\n"
    "ds_read_b64_tr_b16 a[2:3], v22 offset:21504\n"
    "v_mfma_f32_16x16x32_bf16 v[92:95], a[44:47], a[100:103], v[92:95]\n"
    "ds_read_b64_tr_b16 a[4:5], v22 offset:25600\n"
    "buffer_load_dwordx4 v19, s[32:35], s69 offen offset:3072 lds\n"
    "v_mfma_f32_16x16x32_bf16 v[92:95], a[40:43], a[96:99], v[92:95]\n"
    "ds_read_b64_tr_b16 a[6:7], v22 offset:29696\n"
    "s_waitcnt lgkmcnt(8)\n"
    "v_mfma_f32_16x16x32_bf16 v[72:75], a[48:51], a[112:115], v[72:75]\n"
    "ds_read_b64_tr_b16 a[8:9], v23 offset:17408\n"
    "v_mfma_f32_16x16x32_bf16 v[72:75], a[52:55], a[116:119], v[72:75]\n"
    "s_add_u32 m0, s65, 0x14000\n"
    "s_xor_b32 s69, s89, 0xb0000\n"
    "ds_read_b64_tr_b16 a[10:11], v23 offset:21504\n"
    "v_mfma_f32_16x16x32_bf16 v[104:107], a[52:55], a[124:127], v[104:107]\n"
    "ds_read_b64_tr_b16 a[12:13], v23 offset:25600\n"
    "buffer_load_dwordx4 v16, s[32:35], s69 offen offset:0 lds\n"
    "v_mfma_f32_16x16x32_bf16 v[104:107], a[48:51], a[120:123], v[104:107]\n"
    "ds_read_b64_tr_b16 a[14:15], v23 offset:29696\n"
    "s_waitcnt lgkmcnt(8)\n"
    "v_mfma_f32_16x16x32_bf16 v[108:111], a[56:59], a[120:123], v[108:111]\n"
    "ds_read_b64_tr_b16 a[16:17], v22 offset:17920\n"
    "v_mfma_f32_16x16x32_bf16 v[108:111], a[60:63], a[124:127], v[108:111]\n"
    "ds_read_b64_tr_b16 a[18:19], v22 offset:22016\n"
    "v_mfma_f32_16x16x32_bf16 v[76:79], a[60:63], a[116:119], v[76:79]\n"
    "ds_read_b64_tr_b16 a[20:21], v22 offset:26112\n"
    "buffer_load_dwordx4 v17, s[32:35], s69 offen offset:1024 lds\n"
    "v_mfma_f32_16x16x32_bf16 v[76:79], a[56:59], a[112:115], v[76:79]\n"
    "ds_read_b64_tr_b16 a[22:23], v22 offset:30208\n"
    "s_waitcnt lgkmcnt(8)\n"
    "v_mfma_f32_16x16x32_bf16 v[80:83], a[0:3], a[112:115], v[80:83]\n"
    "ds_read_b64_tr_b16 a[24:25], v23 offset:17920\n"
    "v_mfma_f32_16x16x32_bf16 v[80:83], a[4:7], a[116:119], v[80:83]\n"
    "ds_read_b64_tr_b16 a[26:27], v23 offset:22016\n"
    "v_mfma_f32_16x16x32_bf16 v[112:115], a[4:7], a[124:127], v[112:115]\n"
    "ds_read_b64_tr_b16 a[28:29], v23 offset:26112\n"
    "buffer_load_dwordx4 v18, s[32:35], s69 offen offset:2048 lds\n"
    "v_mfma_f32_16x16x32_bf16 v[112:115], a[0:3], a[120:123], v[112:115]\n"
    "ds_read_b64_tr_b16 a[30:31], v23 offset:30208\n"
    "s_waitcnt lgkmcnt(8)\n"
    "v_mfma_f32_16x16x32_bf16 v[116:119], a[8:11], a[120:123], v[116:119]\n"
    "s_waitcnt vmcnt(16)\n"
    "s_barrier\n"
    "ds_read_b64_tr_b16 a[32:33], v22 offset:49664\n"
    "v_mfma_f32_16x16x32_bf16 v[116:119], a[12:15], a[124:127], v[116:119]\n"
    "ds_read_b64_tr_b16 a[34:35], v22 offset:53760\n"
    "v_mfma_f32_16x16x32_bf16 v[84:87], a[12:15], a[116:119], v[84:87]\n"
    "ds_read_b64_tr_b16 a[36:37], v22 offset:57856\n"
    "buffer_load_dwordx4 v19, s[32:35], s69 offen offset:3072 lds\n"
    "v_mfma_f32_16x16x32_bf16 v[84:87], a[8:11], a[112:115], v[84:87]\n"
    "ds_read_b64_tr_b16 a[38:39], v22 offset:61952\n"
    "s_waitcnt lgkmcnt(8)\n"
    "v_mfma_f32_16x16x32_bf16 v[88:91], a[16:19], a[112:115], v[88:91]\n"
    "ds_read_b64_tr_b16 a[40:41], v23 offset:49664\n"
    "v_mfma_f32_16x16x32_bf16 v[88:91], a[20:23], a[116:119], v[88:91]\n"
    "s_xor_b32 s87, s46, 0x6800\n"
    "s_add_u32 s87, s87, s48\n"
    "ds_read_b64_tr_b16 a[42:43], v23 offset:53760\n"
    "v_mfma_f32_16x16x32_bf16 v[120:123], a[20:23], a[124:127], v[120:123]\n"
    "ds_read_b64_tr_b16 a[44:45], v23 offset:57856\n"
    "buffer_load_dwordx4 a[208:211], v2, s[24:27], s87 offen offset:0 sc1\n"
    "v_mfma_f32_16x16x32_bf16 v[120:123], a[16:19], a[120:123], v[120:123]\n"
    "ds_read_b64_tr_b16 a[46:47], v23 offset:61952\n"
    "s_waitcnt lgkmcnt(8)\n"
    "v_mfma_f32_16x16x32_bf16 v[124:127], a[24:27], a[120:123], v[124:127]\n"
    "ds_read_b64_tr_b16 a[48:49], v22 offset:50176\n"
    "v_mfma_f32_16x16x32_bf16 v[124:127], a[28:31], a[124:127], v[124:127]\n"
    "ds_read_b64_tr_b16 a[50:51], v22 offset:54272\n"
    "v_mfma_f32_16x16x32_bf16 v[92:95], a[28:31], a[116:119], v[92:95]\n"
    "ds_read_b64_tr_b16 a[52:53], v22 offset:58368\n"
    "buffer_load_dwordx4 a[212:215], v2, s[24:27], s87 offen offset:1024 sc1\n"
    "v_mfma_f32_16x16x32_bf16 v[92:95], a[24:27], a[112:115], v[92:95]\n"
    "ds_read_b64_tr_b16 a[54:55], v22 offset:62464\n"
    "s_waitcnt lgkmcnt(8)\n"
    "v_mfma_f32_16x16x32_bf16 v[72:75], a[32:35], a[128:131], v[72:75]\n"
    "ds_read_b64_tr_b16 a[56:57], v23 offset:50176\n"
    "v_mfma_f32_16x16x32_bf16 v[72:75], a[36:39], a[132:135], v[72:75]\n"
    "s_add_u32 s88, s87, 0x2c000\n"
    "ds_read_b64_tr_b16 a[58:59], v23 offset:54272\n"
    "v_mfma_f32_16x16x32_bf16 v[104:107], a[36:39], a[140:143], v[104:107]\n"
    "ds_read_b64_tr_b16 a[60:61], v23 offset:58368\n"
    "buffer_load_dwordx4 a[216:219], v2, s[24:27], s88 offen offset:0 sc1\n"
    "v_mfma_f32_16x16x32_bf16 v[104:107], a[32:35], a[136:139], v[104:107]\n"
    "ds_read_b64_tr_b16 a[62:63], v23 offset:62464\n"
    "s_waitcnt lgkmcnt(8)\n"
    "v_mfma_f32_16x16x32_bf16 v[108:111], a[40:43], a[136:139], v[108:111]\n"
    "ds_read_b64_tr_b16 a[0:1], v22 offset:50688\n"
    "v_mfma_f32_16x16x32_bf16 v[108:111], a[44:47], a[140:143], v[108:111]\n"
    "ds_read_b64_tr_b16 a[2:3], v22 offset:54784\n"
    "v_mfma_f32_16x16x32_bf16 v[76:79], a[44:47], a[132:135], v[76:79]\n"
    "ds_read_b64_tr_b16 a[4:5], v22 offset:58880\n"
    "buffer_load_dwordx4 a[220:223], v2, s[24:27], s88 offen offset:1024 sc1\n"
    "v_mfma_f32_16x16x32_bf16 v[76:79], a[40:43], a[128:131], v[76:79]\n"
    "ds_read_b64_tr_b16 a[6:7], v22 offset:62976\n"
    "s_waitcnt lgkmcnt(8)\n"
    "v_mfma_f32_16x16x32_bf16 v[80:83], a[48:51], a[128:131], v[80:83]\n"
    "ds_read_b64_tr_b16 a[8:9], v23 offset:50688\n"
    "v_mfma_f32_16x16x32_bf16 v[80:83], a[52:55], a[132:135], v[80:83]\n"
    "s_xor_b32 s87, s46, 0x7000\n"
    "s_add_u32 s87, s87, s48\n"
    "ds_read_b64_tr_b16 a[10:11], v23 offset:54784\n"
    "v_mfma_f32_16x16x32_bf16 v[112:115], a[52:55], a[140:143], v[112:115]\n"
    "ds_read_b64_tr_b16 a[12:13], v23 offset:58880\n"
    "buffer_load_dwordx4 a[224:227], v2, s[24:27], s87 offen offset:0 sc1\n"
    "v_mfma_f32_16x16x32_bf16 v[112:115], a[48:51], a[136:139], v[112:115]\n"
    "ds_read_b64_tr_b16 a[14:15], v23 offset:62976\n"
    "s_waitcnt lgkmcnt(8)\n"
    "v_mfma_f32_16x16x32_bf16 v[116:119], a[56:59], a[136:139], v[116:119]\n"
    "ds_read_b64_tr_b16 a[16:17], v24 offset:512\n"
    "v_mfma_f32_16x16x32_bf16 v[116:119], a[60:63], a[140:143], v[116:119]\n"
    "ds_read_b64_tr_b16 a[18:19], v24 offset:4608\n"
    "v_mfma_f32_16x16x32_bf16 v[84:87], a[60:63], a[132:135], v[84:87]\n"
    "ds_read_b64_tr_b16 a[20:21], v24 offset:8704\n"
    "buffer_load_dwordx4 a[228:231], v2, s[24:27], s87 offen offset:1024 sc1\n"
    "v_mfma_f32_16x16x32_bf16 v[84:87], a[56:59], a[128:131], v[84:87]\n"
    "ds_read_b64_tr_b16 a[22:23], v24 offset:12800\n"
    "s_waitcnt lgkmcnt(8)\n"
    "v_mfma_f32_16x16x32_bf16 v[88:91], a[0:3], a[128:131], v[88:91]\n"
    "ds_read_b64_tr_b16 a[24:25], v25 offset:512\n"
    "v_mfma_f32_16x16x32_bf16 v[88:91], a[4:7], a[132:135], v[88:91]\n"
    "s_add_u32 s88, s87, 0x2c000\n"
    "ds_read_b64_tr_b16 a[26:27], v25 offset:4608\n"
    "v_mfma_f32_16x16x32_bf16 v[120:123], a[4:7], a[140:143], v[120:123]\n"
    "ds_read_b64_tr_b16 a[28:29], v25 offset:8704\n"
    "buffer_load_dwordx4 a[232:235], v2, s[24:27], s88 offen offset:0 sc1\n"
    "v_mfma_f32_16x16x32_bf16 v[120:123], a[0:3], a[136:139], v[120:123]\n"
    "ds_read_b64_tr_b16 a[30:31], v25 offset:12800\n"
    "s_waitcnt lgkmcnt(8)\n"
    "v_mfma_f32_16x16x32_bf16 v[124:127], a[8:11], a[136:139], v[124:127]\n"
    "ds_read_b64_tr_b16 a[32:33], v24 offset:1024\n"
    "v_mfma_f32_16x16x32_bf16 v[124:127], a[12:15], a[140:143], v[124:127]\n"
    "ds_read_b64_tr_b16 a[34:35], v24 offset:5120\n"
    "v_mfma_f32_16x16x32_bf16 v[92:95], a[12:15], a[132:135], v[92:95]\n"
    "ds_read_b64_tr_b16 a[36:37], v24 offset:9216\n"
    "buffer_load_dwordx4 a[236:239], v2, s[24:27], s88 offen offset:1024 sc1\n"
    "v_mfma_f32_16x16x32_bf16 v[92:95], a[8:11], a[128:131], v[92:95]\n"
    "ds_read_b64_tr_b16 a[38:39], v24 offset:13312\n"
    "s_waitcnt lgkmcnt(8)\n"
    "v_mfma_f32_16x16x32_bf16 v[72:75], a[16:19], a[144:147], v[72:75]\n"
    "ds_read_b64_tr_b16 a[40:41], v25 offset:1024\n"
    "v_mfma_f32_16x16x32_bf16 v[72:75], a[20:23], a[148:151], v[72:75]\n"
    "s_add_u32 m0, s65, 0x18000\n"
    "s_xor_b32 s69, s89, 0xc0000\n"
    "ds_read_b64_tr_b16 a[42:43], v25 offset:5120\n"
    "v_mfma_f32_16x16x32_bf16 v[104:107], a[20:23], a[156:159], v[104:107]\n"
    "ds_read_b64_tr_b16 a[44:45], v25 offset:9216\n"
    "buffer_load_dwordx4 v16, s[32:35], s69 offen offset:0 lds\n"
    "v_mfma_f32_16x16x32_bf16 v[104:107], a[16:19], a[152:155], v[104:107]\n"
    "ds_read_b64_tr_b16 a[46:47], v25 offset:13312\n"
    "s_waitcnt lgkmcnt(8)\n"
    "v_mfma_f32_16x16x32_bf16 v[108:111], a[24:27], a[152:155], v[108:111]\n"
    "ds_read_b64_tr_b16 a[48:49], v24 offset:1536\n"
    "v_mfma_f32_16x16x32_bf16 v[108:111], a[28:31], a[156:159], v[108:111]\n"
    "ds_read_b64_tr_b16 a[50:51], v24 offset:5632\n"
    "v_mfma_f32_16x16x32_bf16 v[76:79], a[28:31], a[148:151], v[76:79]\n"
    "ds_read_b64_tr_b16 a[52:53], v24 offset:9728\n"
    "buffer_load_dwordx4 v17, s[32:35], s69 offen offset:1024 lds\n"
    "v_mfma_f32_16x16x32_bf16 v[76:79], a[24:27], a[144:147], v[76:79]\n"
    "ds_read_b64_tr_b16 a[54:55], v24 offset:13824\n"
    "s_waitcnt lgkmcnt(8)\n"
    "v_mfma_f32_16x16x32_bf16 v[80:83], a[32:35], a[144:147], v[80:83]\n"
    "ds_read_b64_tr_b16 a[56:57], v25 offset:1536\n"
    "v_mfma_f32_16x16x32_bf16 v[80:83], a[36:39], a[148:151], v[80:83]\n"
    "ds_read_b64_tr_b16 a[58:59], v25 offset:5632\n"
    "v_mfma_f32_16x16x32_bf16 v[112:115], a[36:39], a[156:159], v[112:115]\n"
    "ds_read_b64_tr_b16 a[60:61], v25 offset:9728\n"
    "buffer_load_dwordx4 v18, s[32:35], s69 offen offset:2048 lds\n"
    "v_mfma_f32_16x16x32_bf16 v[112:115], a[32:35], a[152:155], v[112:115]\n"
    "ds_read_b64_tr_b16 a[62:63], v25 offset:13824\n"
    "s_waitcnt lgkmcnt(8)\n"
    "v_mfma_f32_16x16x32_bf16 v[116:119], a[40:43], a[152:155], v[116:119]\n"
    "s_waitcnt vmcnt(11)\n"
    "s_barrier\n"
    "ds_read_b64_tr_b16 a[0:1], v22 offset:512\n"
    "v_mfma_f32_16x16x32_bf16 v[116:119], a[44:47], a[156:159], v[116:119]\n"
    "ds_read_b64_tr_b16 a[2:3], v22 offset:4608\n"
    "v_mfma_f32_16x16x32_bf16 v[84:87], a[44:47], a[148:151], v[84:87]\n"
    "ds_read_b64_tr_b16 a[4:5], v22 offset:8704\n"
    "buffer_load_dwordx4 v19, s[32:35], s69 offen offset:3072 lds\n"
    "v_mfma_f32_16x16x32_bf16 v[84:87], a[40:43], a[144:147], v[84:87]\n"
    "ds_read_b64_tr_b16 a[6:7], v22 offset:12800\n"
    "s_waitcnt lgkmcnt(8)\n"
    "v_mfma_f32_16x16x32_bf16 v[88:91], a[48:51], a[144:147], v[88:91]\n"
    "ds_read_b64_tr_b16 a[8:9], v23 offset:512\n"
    "v_mfma_f32_16x16x32_bf16 v[88:91], a[52:55], a[148:151], v[88:91]\n"
    "s_add_u32 m0, s65, 0x1c000\n"
    "s_xor_b32 s69, s89, 0xd0000\n"
    "ds_read_b64_tr_b16 a[10:11], v23 offset:4608\n"
    "v_mfma_f32_16x16x32_bf16 v[120:123], a[52:55], a[156:159], v[120:123]\n"
    "ds_read_b64_tr_b16 a[12:13], v23 offset:8704\n"
    "buffer_load_dwordx4 v16, s[32:35], s69 offen offset:0 lds\n"
    "v_mfma_f32_16x16x32_bf16 v[120:123], a[48:51], a[152:155], v[120:123]\n"
    "ds_read_b64_tr_b16 a[14:15], v23 offset:12800\n"
    "s_waitcnt lgkmcnt(8)\n"
    "v_mfma_f32_16x16x32_bf16 v[124:127], a[56:59], a[152:155], v[124:127]\n"
    "ds_read_b64_tr_b16 a[16:17], v22 offset:1024\n"
    "v_mfma_f32_16x16x32_bf16 v[124:127], a[60:63], a[156:159], v[124:127]\n"
    "ds_read_b64_tr_b16 a[18:19], v22 offset:5120\n"
    "v_mfma_f32_16x16x32_bf16 v[92:95], a[60:63], a[148:151], v[92:95]\n"
    "ds_read_b64_tr_b16 a[20:21], v22 offset:9216\n"
    "buffer_load_dwordx4 v17, s[32:35], s69 offen offset:1024 lds\n"
    "v_mfma_f32_16x16x32_bf16 v[92:95], a[56:59], a[144:147], v[92:95]\n"
    "ds_read_b64_tr_b16 a[22:23], v22 offset:13312\n"
    "s_waitcnt lgkmcnt(8)\n"
    "v_mfma_f32_16x16x32_bf16 v[72:75], a[0:3], a[160:163], v[72:75]\n"
    "ds_read_b64_tr_b16 a[24:25], v23 offset:1024\n"
    "v_mfma_f32_16x16x32_bf16 v[72:75], a[4:7], a[164:167], v[72:75]\n"
    "ds_read_b64_tr_b16 a[26:27], v23 offset:5120\n"
    "v_mfma_f32_16x16x32_bf16 v[104:107], a[4:7], a[172:175], v[104:107]\n"
    "ds_read_b64_tr_b16 a[28:29], v23 offset:9216\n"
    "buffer_load_dwordx4 v18, s[32:35], s69 offen offset:2048 lds\n"
    "v_mfma_f32_16x16x32_bf16 v[104:107], a[0:3], a[168:171], v[104:107]\n"
    "ds_read_b64_tr_b16 a[30:31], v23 offset:13312\n"
    "s_waitcnt lgkmcnt(8)\n"
    "v_mfma_f32_16x16x32_bf16 v[108:111], a[8:11], a[168:171], v[108:111]\n"
    "ds_read_b64_tr_b16 a[32:33], v22 offset:1536\n"
    "v_mfma_f32_16x16x32_bf16 v[108:111], a[12:15], a[172:175], v[108:111]\n"
    "ds_read_b64_tr_b16 a[34:35], v22 offset:5632\n"
    "v_mfma_f32_16x16x32_bf16 v[76:79], a[12:15], a[164:167], v[76:79]\n"
    "ds_read_b64_tr_b16 a[36:37], v22 offset:9728\n"
    "buffer_load_dwordx4 v19, s[32:35], s69 offen offset:3072 lds\n"
    "v_mfma_f32_16x16x32_bf16 v[76:79], a[8:11], a[160:163], v[76:79]\n"
    "ds_read_b64_tr_b16 a[38:39], v22 offset:13824\n"
    "s_waitcnt lgkmcnt(8)\n"
    "v_mfma_f32_16x16x32_bf16 v[80:83], a[16:19], a[160:163], v[80:83]\n"
    "ds_read_b64_tr_b16 a[40:41], v23 offset:1536\n"
    "v_mfma_f32_16x16x32_bf16 v[80:83], a[20:23], a[164:167], v[80:83]\n"
    "s_xor_b32 s87, s46, 0x7800\n"
    "s_add_u32 s87, s87, s48\n"
    "ds_read_b64_tr_b16 a[42:43], v23 offset:5632\n"
    "v_mfma_f32_16x16x32_bf16 v[112:115], a[20:23], a[172:175], v[112:115]\n"
    "ds_read_b64_tr_b16 a[44:45], v23 offset:9728\n"
    "buffer_load_dwordx4 a[240:243], v2, s[24:27], s87 offen offset:0 sc1\n"
    "v_mfma_f32_16x16x32_bf16 v[112:115], a[16:19], a[168:171], v[112:115]\n"
    "ds_read_b64_tr_b16 a[46:47], v23 offset:13824\n"
    "s_waitcnt lgkmcnt(8)\n"
    "v_mfma_f32_16x16x32_bf16 v[116:119], a[24:27], a[168:171], v[116:119]\n"
    "ds_read_b64_tr_b16 a[48:49], v24 offset:16896\n"
    "v_mfma_f32_16x16x32_bf16 v[116:119], a[28:31], a[172:175], v[116:119]\n"
    "ds_read_b64_tr_b16 a[50:51], v24 offset:20992\n"
    "v_mfma_f32_16x16x32_bf16 v[84:87], a[28:31], a[164:167], v[84:87]\n"
    "ds_read_b64_tr_b16 a[52:53], v24 offset:25088\n"
    "buffer_load_dwordx4 a[244:247], v2, s[24:27], s87 offen offset:1024 sc1\n"
    "v_mfma_f32_16x16x32_bf16 v[84:87], a[24:27], a[160:163], v[84:87]\n"
    "ds_read_b64_tr_b16 a[54:55], v24 offset:29184\n"
    "s_waitcnt lgkmcnt(8)\n"
    "v_mfma_f32_16x16x32_bf16 v[88:91], a[32:35], a[160:163], v[88:91]\n"
    "ds_read_b64_tr_b16 a[56:57], v25 offset:16896\n"
    "v_mfma_f32_16x16x32_bf16 v[88:91], a[36:39], a[164:167], v[88:91]\n"
    "s_add_u32 s88, s87, 0x2c000\n"
    "ds_read_b64_tr_b16 a[58:59], v25 offset:20992\n"
    "v_mfma_f32_16x16x32_bf16 v[120:123], a[36:39], a[172:175], v[120:123]\n"
    "ds_read_b64_tr_b16 a[60:61], v25 offset:25088\n"
    "buffer_load_dwordx4 a[248:251], v2, s[24:27], s88 offen offset:0 sc1\n"
    "v_mfma_f32_16x16x32_bf16 v[120:123], a[32:35], a[168:171], v[120:123]\n"
    "ds_read_b64_tr_b16 a[62:63], v25 offset:29184\n"
    "s_waitcnt lgkmcnt(8)\n"
    "v_mfma_f32_16x16x32_bf16 v[124:127], a[40:43], a[168:171], v[124:127]\n"
    "ds_read_b64_tr_b16 a[0:1], v24 offset:17408\n"
    "v_mfma_f32_16x16x32_bf16 v[124:127], a[44:47], a[172:175], v[124:127]\n"
    "ds_read_b64_tr_b16 a[2:3], v24 offset:21504\n"
    "v_mfma_f32_16x16x32_bf16 v[92:95], a[44:47], a[164:167], v[92:95]\n"
    "ds_read_b64_tr_b16 a[4:5], v24 offset:25600\n"
    "buffer_load_dwordx4 a[252:255], v2, s[24:27], s88 offen offset:1024 sc1\n"
    "v_mfma_f32_16x16x32_bf16 v[92:95], a[40:43], a[160:163], v[92:95]\n"
    "ds_read_b64_tr_b16 a[6:7], v24 offset:29696\n"
    "s_waitcnt lgkmcnt(8)\n"
    "v_mfma_f32_16x16x32_bf16 v[72:75], a[48:51], a[176:179], v[72:75]\n"
    "ds_read_b64_tr_b16 a[8:9], v25 offset:17408\n"
    "v_mfma_f32_16x16x32_bf16 v[72:75], a[52:55], a[180:183], v[72:75]\n"
    "s_add_u32 m0, s65, 0x20000\n"
    "s_xor_b32 s69, s89, 0xe0000\n"
    "ds_read_b64_tr_b16 a[10:11], v25 offset:21504\n"
    "v_mfma_f32_16x16x32_bf16 v[104:107], a[52:55], a[188:191], v[104:107]\n"
    "ds_read_b64_tr_b16 a[12:13], v25 offset:25600\n"
    "buffer_load_dwordx4 v16, s[32:35], s69 offen offset:0 lds\n"
    "v_mfma_f32_16x16x32_bf16 v[104:107], a[48:51], a[184:187], v[104:107]\n"
    "ds_read_b64_tr_b16 a[14:15], v25 offset:29696\n"
    "s_waitcnt lgkmcnt(8)\n"
    "v_mfma_f32_16x16x32_bf16 v[108:111], a[56:59], a[184:187], v[108:111]\n"
    "ds_read_b64_tr_b16 a[16:17], v24 offset:17920\n"
    "v_mfma_f32_16x16x32_bf16 v[108:111], a[60:63], a[188:191], v[108:111]\n"
    "ds_read_b64_tr_b16 a[18:19], v24 offset:22016\n"
    "v_mfma_f32_16x16x32_bf16 v[76:79], a[60:63], a[180:183], v[76:79]\n"
    "ds_read_b64_tr_b16 a[20:21], v24 offset:26112\n"
    "buffer_load_dwordx4 v17, s[32:35], s69 offen offset:1024 lds\n"
    "v_mfma_f32_16x16x32_bf16 v[76:79], a[56:59], a[176:179], v[76:79]\n"
    "ds_read_b64_tr_b16 a[22:23], v24 offset:30208\n"
    "s_waitcnt lgkmcnt(8)\n"
    "v_mfma_f32_16x16x32_bf16 v[80:83], a[0:3], a[176:179], v[80:83]\n"
    "buffer_load_dwordx2 v[4:5], v21, s[24:27], s51 offen offset:128 sc1\n"
    "ds_read_b64_tr_b16 a[24:25], v25 offset:17920\n"
    "v_mfma_f32_16x16x32_bf16 v[80:83], a[4:7], a[180:183], v[80:83]\n"
    "ds_read_b64_tr_b16 a[26:27], v25 offset:22016\n"
    "v_mfma_f32_16x16x32_bf16 v[112:115], a[4:7], a[188:191], v[112:115]\n"
    "ds_read_b64_tr_b16 a[28:29], v25 offset:26112\n"
    "buffer_load_dwordx4 v18, s[32:35], s69 offen offset:2048 lds\n"
    "v_mfma_f32_16x16x32_bf16 v[112:115], a[0:3], a[184:187], v[112:115]\n"
    "ds_read_b64_tr_b16 a[30:31], v25 offset:30208\n"
    "s_waitcnt lgkmcnt(8)\n"
    "v_mfma_f32_16x16x32_bf16 v[116:119], a[8:11], a[184:187], v[116:119]\n"
    "s_waitcnt vmcnt(8)\n"
    "s_barrier\n"
    "ds_read_b64_tr_b16 a[32:33], v24 offset:33280\n"
    "v_mfma_f32_16x16x32_bf16 v[116:119], a[12:15], a[188:191], v[116:119]\n"
    "ds_read_b64_tr_b16 a[34:35], v24 offset:37376\n"
    "v_mfma_f32_16x16x32_bf16 v[84:87], a[12:15], a[180:183], v[84:87]\n"
    "ds_read_b64_tr_b16 a[36:37], v24 offset:41472\n"
    "buffer_load_dwordx4 v19, s[32:35], s69 offen offset:3072 lds\n"
    "v_mfma_f32_16x16x32_bf16 v[84:87], a[8:11], a[176:179], v[84:87]\n"
    "ds_read_b64_tr_b16 a[38:39], v24 offset:45568\n"
    "s_waitcnt lgkmcnt(8)\n"
    "v_mfma_f32_16x16x32_bf16 v[88:91], a[16:19], a[176:179], v[88:91]\n"
    "ds_read_b64_tr_b16 a[40:41], v25 offset:33280\n"
    "v_mfma_f32_16x16x32_bf16 v[88:91], a[20:23], a[180:183], v[88:91]\n"
    "s_add_u32 m0, s65, 0x24000\n"
    "s_xor_b32 s69, s89, 0xf0000\n"
    "ds_read_b64_tr_b16 a[42:43], v25 offset:37376\n"
    "v_mfma_f32_16x16x32_bf16 v[120:123], a[20:23], a[188:191], v[120:123]\n"
    "ds_read_b64_tr_b16 a[44:45], v25 offset:41472\n"
    "buffer_load_dwordx4 v16, s[32:35], s69 offen offset:0 lds\n"
    "v_mfma_f32_16x16x32_bf16 v[120:123], a[16:19], a[184:187], v[120:123]\n"
    "ds_read_b64_tr_b16 a[46:47], v25 offset:45568\n"
    "s_waitcnt lgkmcnt(8)\n"
    "v_mfma_f32_16x16x32_bf16 v[124:127], a[24:27], a[184:187], v[124:127]\n"
    "ds_read_b64_tr_b16 a[48:49], v24 offset:33792\n"
    "v_mfma_f32_16x16x32_bf16 v[124:127], a[28:31], a[188:191], v[124:127]\n"
    "ds_read_b64_tr_b16 a[50:51], v24 offset:37888\n"
    "v_mfma_f32_16x16x32_bf16 v[92:95], a[28:31], a[180:183], v[92:95]\n"
    "ds_read_b64_tr_b16 a[52:53], v24 offset:41984\n"
    "buffer_load_dwordx4 v17, s[32:35], s69 offen offset:1024 lds\n"
    "v_mfma_f32_16x16x32_bf16 v[92:95], a[24:27], a[176:179], v[92:95]\n"
    "ds_read_b64_tr_b16 a[54:55], v24 offset:46080\n"
    "s_waitcnt lgkmcnt(8)\n"
    "v_mfma_f32_16x16x32_bf16 v[72:75], a[32:35], a[192:195], v[72:75]\n"
    "ds_read_b64_tr_b16 a[56:57], v25 offset:33792\n"
    "v_mfma_f32_16x16x32_bf16 v[72:75], a[36:39], a[196:199], v[72:75]\n"
    "ds_read_b64_tr_b16 a[58:59], v25 offset:37888\n"
    "v_mfma_f32_16x16x32_bf16 v[104:107], a[36:39], a[204:207], v[104:107]\n"
    "ds_read_b64_tr_b16 a[60:61], v25 offset:41984\n"
    "buffer_load_dwordx4 v18, s[32:35], s69 offen offset:2048 lds\n"
    "v_mfma_f32_16x16x32_bf16 v[104:107], a[32:35], a[200:203], v[104:107]\n"
    "ds_read_b64_tr_b16 a[62:63], v25 offset:46080\n"
    "s_waitcnt lgkmcnt(8)\n"
    "v_mfma_f32_16x16x32_bf16 v[108:111], a[40:43], a[200:203], v[108:111]\n"
    "ds_read_b64_tr_b16 a[0:1], v24 offset:34304\n"
    "v_mfma_f32_16x16x32_bf16 v[108:111], a[44:47], a[204:207], v[108:111]\n"
    "ds_read_b64_tr_b16 a[2:3], v24 offset:38400\n"
    "v_mfma_f32_16x16x32_bf16 v[76:79], a[44:47], a[196:199], v[76:79]\n"
    "ds_read_b64_tr_b16 a[4:5], v24 offset:42496\n"
    "buffer_load_dwordx4 v19, s[32:35], s69 offen offset:3072 lds\n"
    "v_mfma_f32_16x16x32_bf16 v[76:79], a[40:43], a[192:195], v[76:79]\n"
    "ds_read_b64_tr_b16 a[6:7], v24 offset:46592\n"
    "s_waitcnt lgkmcnt(8)\n"
    "v_mfma_f32_16x16x32_bf16 v[80:83], a[48:51], a[192:195], v[80:83]\n"
    "ds_read_b64_tr_b16 a[8:9], v25 offset:34304\n"
    "v_mfma_f32_16x16x32_bf16 v[80:83], a[52:55], a[196:199], v[80:83]\n"
    "s_add_u32 s98, s56, 0x4000\n"
    "ds_read_b64_tr_b16 a[10:11], v25 offset:38400\n"
    "v_mfma_f32_16x16x32_bf16 v[112:115], a[52:55], a[204:207], v[112:115]\n"
    "ds_read_b64_tr_b16 a[12:13], v25 offset:42496\n"
    "buffer_load_dwordx2 v[56:57], v38, s[4:7], s56 offen offset:0\n"
    "v_mfma_f32_16x16x32_bf16 v[112:115], a[48:51], a[200:203], v[112:115]\n"
    "ds_read_b64_tr_b16 a[14:15], v25 offset:46592\n"
    "s_waitcnt lgkmcnt(8)\n"
    "v_mfma_f32_16x16x32_bf16 v[116:119], a[56:59], a[200:203], v[116:119]\n"
    "ds_read_b64_tr_b16 a[16:17], v24 offset:49664\n"
    "v_mfma_f32_16x16x32_bf16 v[116:119], a[60:63], a[204:207], v[116:119]\n"
    "ds_read_b64_tr_b16 a[18:19], v24 offset:53760\n"
    "v_mfma_f32_16x16x32_bf16 v[84:87], a[60:63], a[196:199], v[84:87]\n"
    "ds_read_b64_tr_b16 a[20:21], v24 offset:57856\n"
    "buffer_load_dwordx2 v[58:59], v38, s[4:7], s56 offen offset:32\n"
    "v_mfma_f32_16x16x32_bf16 v[84:87], a[56:59], a[192:195], v[84:87]\n"
    "ds_read_b64_tr_b16 a[22:23], v24 offset:61952\n"
    "s_waitcnt lgkmcnt(8)\n"
    "v_mfma_f32_16x16x32_bf16 v[88:91], a[0:3], a[192:195], v[88:91]\n"
    "ds_read_b64_tr_b16 a[24:25], v25 offset:49664\n"
    "v_mfma_f32_16x16x32_bf16 v[88:91], a[4:7], a[196:199], v[88:91]\n"
    "ds_read_b64_tr_b16 a[26:27], v25 offset:53760\n"
    "v_mfma_f32_16x16x32_bf16 v[120:123], a[4:7], a[204:207], v[120:123]\n"
    "ds_read_b64_tr_b16 a[28:29], v25 offset:57856\n"
    "buffer_load_dwordx2 v[60:61], v38, s[4:7], s98 offen offset:0\n"
    "v_mfma_f32_16x16x32_bf16 v[120:123], a[0:3], a[200:203], v[120:123]\n"
    "ds_read_b64_tr_b16 a[30:31], v25 offset:61952\n"
    "s_waitcnt lgkmcnt(8)\n"
    "v_mfma_f32_16x16x32_bf16 v[124:127], a[8:11], a[200:203], v[124:127]\n"
    "ds_read_b64_tr_b16 a[32:33], v24 offset:50176\n"
    "v_mfma_f32_16x16x32_bf16 v[124:127], a[12:15], a[204:207], v[124:127]\n"
    "ds_read_b64_tr_b16 a[34:35], v24 offset:54272\n"
    "v_mfma_f32_16x16x32_bf16 v[92:95], a[12:15], a[196:199], v[92:95]\n"
    "ds_read_b64_tr_b16 a[36:37], v24 offset:58368\n"
    "buffer_load_dwordx2 v[62:63], v38, s[4:7], s98 offen offset:32\n"
    "v_mfma_f32_16x16x32_bf16 v[92:95], a[8:11], a[192:195], v[92:95]\n"
    "ds_read_b64_tr_b16 a[38:39], v24 offset:62464\n"
    "s_waitcnt lgkmcnt(8)\n"
    "v_mfma_f32_16x16x32_bf16 v[72:75], a[16:19], a[208:211], v[72:75]\n"
    "ds_read_b64_tr_b16 a[40:41], v25 offset:50176\n"
    "v_mfma_f32_16x16x32_bf16 v[72:75], a[20:23], a[212:215], v[72:75]\n"
    "ds_read_b64_tr_b16 a[42:43], v25 offset:54272\n"
    "v_mfma_f32_16x16x32_bf16 v[104:107], a[20:23], a[220:223], v[104:107]\n"
    "ds_read_b64_tr_b16 a[44:45], v25 offset:58368\n"
    "v_mfma_f32_16x16x32_bf16 v[104:107], a[16:19], a[216:219], v[104:107]\n"
    "ds_read_b64_tr_b16 a[46:47], v25 offset:62464\n"
    "s_waitcnt lgkmcnt(8)\n"
    "v_mfma_f32_16x16x32_bf16 v[108:111], a[24:27], a[216:219], v[108:111]\n"
    "ds_read_b64_tr_b16 a[48:49], v24 offset:50688\n"
    "v_mfma_f32_16x16x32_bf16 v[108:111], a[28:31], a[220:223], v[108:111]\n"
    "ds_read_b64_tr_b16 a[50:51], v24 offset:54784\n"
    "v_mfma_f32_16x16x32_bf16 v[76:79], a[28:31], a[212:215], v[76:79]\n"
    "ds_read_b64_tr_b16 a[52:53], v24 offset:58880\n"
    "v_mfma_f32_16x16x32_bf16 v[76:79], a[24:27], a[208:211], v[76:79]\n"
    "ds_read_b64_tr_b16 a[54:55], v24 offset:62976\n"
    "s_waitcnt vmcnt(10)\n"
    "v_cmp_ne_u64 vcc, s[90:91], v[4:5]\n"
    "s_and_b32 s68, vcc_lo, 0xffffffff\n"
    "s_cbranch_scc0 L_pgok%=\n"
    "s_mov_b32 s86, 0\n"
    "buffer_load_dwordx2 v[4:5], v21, s[24:27], s51 offen offset:128 sc1\n"
    "s_sleep 5\n"
    "buffer_load_dwordx2 v[10:11], v21, s[24:27], s51 offen offset:128 sc1\n"
    "s_sleep 5\n"
    "buffer_load_dwordx2 v[12:13], v21, s[24:27], s51 offen offset:128 sc1\n"
    "s_sleep 5\n"
    "L_pg%=:\n"
    "s_waitcnt vmcnt(2)\n"
    "v_cmp_ne_u64 vcc, s[90:91], v[4:5]\n"
    "s_and_b32 s68, vcc_lo, 0xffffffff\n"
    "s_cbranch_scc0 L_pgok%=\n"
    "buffer_load_dwordx2 v[4:5], v21, s[24:27], s51 offen offset:128 sc1\n"
    "s_waitcnt vmcnt(2)\n"
    "v_cmp_ne_u64 vcc, s[90:91], v[10:11]\n"
    "s_and_b32 s68, vcc_lo, 0xffffffff\n"
    "s_cbranch_scc0 L_pgok%=\n"
    "buffer_load_dwordx2 v[10:11], v21, s[24:27], s51 offen offset:128 sc1\n"
    "s_waitcnt vmcnt(2)\n"
    "v_cmp_ne_u64 vcc, s[90:91], v[12:13]\n"
    "s_and_b32 s68, vcc_lo, 0xffffffff\n"
    "s_cbranch_scc0 L_pgok%=\n"
    "buffer_load_dwordx2 v[12:13], v21, s[24:27], s51 offen offset:128 sc1\n"
    "s_add_u32 s86, s86, 1\n"
    "s_cmp_lt_u32 s86, 0x100000\n"
    "s_cbranch_scc1 L_pg%=\n"
    "L_pgok%=:\n"
    "s_waitcnt lgkmcnt(8)\n"
    "v_mfma_f32_16x16x32_bf16 v[80:83], a[32:35], a[208:211], v[80:83]\n"
    "ds_read_b64_tr_b16 a[56:57], v25 offset:50688\n"
    "v_mfma_f32_16x16x32_bf16 v[80:83], a[36:39], a[212:215], v[80:83]\n"
    "s_add_u32 s68, s55, 0\n"
    "ds_read_b64_tr_b16 a[58:59], v25 offset:54784\n"
    "v_mfma_f32_16x16x32_bf16 v[112:115], a[36:39], a[220:223], v[112:115]\n"
    "ds_read_b64_tr_b16 a[60:61], v25 offset:58880\n"
    "buffer_load_dwordx4 v[140:143], v39, s[24:27], s68 offen offset:0 sc1\n"
    "v_mfma_f32_16x16x32_bf16 v[112:115], a[32:35], a[216:219], v[112:115]\n"
    "ds_read_b64_tr_b16 a[62:63], v25 offset:62976\n"
    "s_waitcnt lgkmcnt(8)\n"
    "v_mfma_f32_16x16x32_bf16 v[116:119], a[40:43], a[216:219], v[116:119]\n"
    "s_waitcnt vmcnt(5)\n"
    "s_barrier\n"
    "ds_read_b64_tr_b16 a[0:1], v26 offset:512\n"
    "v_mfma_f32_16x16x32_bf16 v[116:119], a[44:47], a[220:223], v[116:119]\n"
    "ds_read_b64_tr_b16 a[2:3], v26 offset:4608\n"
    "v_mfma_f32_16x16x32_bf16 v[84:87], a[44:47], a[212:215], v[84:87]\n"
    "ds_read_b64_tr_b16 a[4:5], v26 offset:8704\n"
    "buffer_load_dwordx4 v[172:175], v39, s[24:27], s68 offen offset:1024 sc1\n"
    "v_mfma_f32_16x16x32_bf16 v[84:87], a[40:43], a[208:211], v[84:87]\n"
    "ds_read_b64_tr_b16 a[6:7], v26 offset:12800\n"
    "s_waitcnt lgkmcnt(8)\n"
    "v_mfma_f32_16x16x32_bf16 v[88:91], a[48:51], a[208:211], v[88:91]\n"
    "ds_read_b64_tr_b16 a[8:9], v27 offset:512\n"
    "v_mfma_f32_16x16x32_bf16 v[88:91], a[52:55], a[212:215], v[88:91]\n"
    "s_add_u32 s68, s55, 0x2000\n"
    "ds_read_b64_tr_b16 a[10:11], v27 offset:4608\n"
    "v_mfma_f32_16x16x32_bf16 v[120:123], a[52:55], a[220:223], v[120:123]\n"
    "ds_read_b64_tr_b16 a[12:13], v27 offset:8704\n"
    "buffer_load_dwordx4 v[148:151], v39, s[24:27], s68 offen offset:0 sc1\n"
    "v_mfma_f32_16x16x32_bf16 v[120:123], a[48:51], a[216:219], v[120:123]\n"
    "ds_read_b64_tr_b16 a[14:15], v27 offset:12800\n"
    "s_waitcnt lgkmcnt(8)\n"
    "v_mfma_f32_16x16x32_bf16 v[124:127], a[56:59], a[216:219], v[124:127]\n"
    "ds_read_b64_tr_b16 a[16:17], v26 offset:1024\n"
    "v_mfma_f32_16x16x32_bf16 v[124:127], a[60:63], a[220:223], v[124:127]\n"
    "ds_read_b64_tr_b16 a[18:19], v26 offset:5120\n"
    "v_mfma_f32_16x16x32_bf16 v[92:95], a[60:63], a[212:215], v[92:95]\n"
    "ds_read_b64_tr_b16 a[20:21], v26 offset:9216\n"
    "buffer_load_dwordx4 v[180:183], v39, s[24:27], s68 offen offset:1024 sc1\n"
    "v_mfma_f32_16x16x32_bf16 v[92:95], a[56:59], a[208:211], v[92:95]\n"
    "ds_read_b64_tr_b16 a[22:23], v26 offset:13312\n"
    "s_waitcnt lgkmcnt(8)\n"
    "v_mfma_f32_16x16x32_bf16 v[72:75], a[0:3], a[224:227], v[72:75]\n"
    "ds_read_b64_tr_b16 a[24:25], v27 offset:1024\n"
    "v_mfma_f32_16x16x32_bf16 v[72:75], a[4:7], a[228:231], v[72:75]\n"
    "s_add_u32 s68, s55, 0x4000\n"
    "ds_read_b64_tr_b16 a[26:27], v27 offset:5120\n"
    "v_mfma_f32_16x16x32_bf16 v[104:107], a[4:7], a[236:239], v[104:107]\n"
    "ds_read_b64_tr_b16 a[28:29], v27 offset:9216\n"
    "buffer_load_dwordx4 v[156:159], v39, s[24:27], s68 offen offset:0 sc1\n"
    "v_mfma_f32_16x16x32_bf16 v[104:107], a[0:3], a[232:235], v[104:107]\n"
    "ds_read_b64_tr_b16 a[30:31], v27 offset:13312\n"
    "s_waitcnt lgkmcnt(8)\n"
    "v_mfma_f32_16x16x32_bf16 v[108:111], a[8:11], a[232:235], v[108:111]\n"
    "ds_read_b64_tr_b16 a[32:33], v26 offset:1536\n"
    "v_mfma_f32_16x16x32_bf16 v[108:111], a[12:15], a[236:239], v[108:111]\n"
    "ds_read_b64_tr_b16 a[34:35], v26 offset:5632\n"
    "v_mfma_f32_16x16x32_bf16 v[76:79], a[12:15], a[228:231], v[76:79]\n"
    "ds_read_b64_tr_b16 a[36:37], v26 offset:9728\n"
    "buffer_load_dwordx4 v[188:191], v39, s[24:27], s68 offen offset:1024 sc1\n"
    "v_mfma_f32_16x16x32_bf16 v[76:79], a[8:11], a[224:227], v[76:79]\n"
    "ds_read_b64_tr_b16 a[38:39], v26 offset:13824\n"
    "s_waitcnt lgkmcnt(8)\n"
    "v_mfma_f32_16x16x32_bf16 v[80:83], a[16:19], a[224:227], v[80:83]\n"
    "ds_read_b64_tr_b16 a[40:41], v27 offset:1536\n"
    "v_mfma_f32_16x16x32_bf16 v[80:83], a[20:23], a[228:231], v[80:83]\n"
    "ds_read_b64_tr_b16 a[42:43], v27 offset:5632\n"
    "v_mfma_f32_16x16x32_bf16 v[112:115], a[20:23], a[236:239], v[112:115]\n"
    "ds_read_b64_tr_b16 a[44:45], v27 offset:9728\n"
    "v_mfma_f32_16x16x32_bf16 v[112:115], a[16:19], a[232:235], v[112:115]\n"
    "ds_read_b64_tr_b16 a[46:47], v27 offset:13824\n"
    "s_waitcnt lgkmcnt(8)\n"
    "v_mfma_f32_16x16x32_bf16 v[116:119], a[24:27], a[232:235], v[116:119]\n"
    "ds_read_b64_tr_b16 a[48:49], v26 offset:16896\n"
    "v_mfma_f32_16x16x32_bf16 v[116:119], a[28:31], a[236:239], v[116:119]\n"
    "ds_read_b64_tr_b16 a[50:51], v26 offset:20992\n"
    "v_mfma_f32_16x16x32_bf16 v[84:87], a[28:31], a[228:231], v[84:87]\n"
    "ds_read_b64_tr_b16 a[52:53], v26 offset:25088\n"
    "v_mfma_f32_16x16x32_bf16 v[84:87], a[24:27], a[224:227], v[84:87]\n"
    "ds_read_b64_tr_b16 a[54:55], v26 offset:29184\n"
    "s_waitcnt lgkmcnt(8)\n"
    "v_mfma_f32_16x16x32_bf16 v[88:91], a[32:35], a[224:227], v[88:91]\n"
    "ds_read_b64_tr_b16 a[56:57], v27 offset:16896\n"
    "v_mfma_f32_16x16x32_bf16 v[88:91], a[36:39], a[228:231], v[88:91]\n"
    "ds_read_b64_tr_b16 a[58:59], v27 offset:20992\n"
    "v_mfma_f32_16x16x32_bf16 v[120:123], a[36:39], a[236:239], v[120:123]\n"
    "ds_read_b64_tr_b16 a[60:61], v27 offset:25088\n"
    "v_mfma_f32_16x16x32_bf16 v[120:123], a[32:35], a[232:235], v[120:123]\n"
    "ds_read_b64_tr_b16 a[62:63], v27 offset:29184\n"
    "s_waitcnt lgkmcnt(8)\n"
    "v_mfma_f32_16x16x32_bf16 v[124:127], a[40:43], a[232:235], v[124:127]\n"
    "ds_read_b64_tr_b16 a[0:1], v26 offset:17408\n"
    "v_mfma_f32_16x16x32_bf16 v[124:127], a[44:47], a[236:239], v[124:127]\n"
    "ds_read_b64_tr_b16 a[2:3], v26 offset:21504\n"
    "v_mfma_f32_16x16x32_bf16 v[92:95], a[44:47], a[228:231], v[92:95]\n"
    "ds_read_b64_tr_b16 a[4:5], v26 offset:25600\n"
    "v_mfma_f32_16x16x32_bf16 v[92:95], a[40:43], a[224:227], v[92:95]\n"
    "ds_read_b64_tr_b16 a[6:7], v26 offset:29696\n"
    "s_waitcnt lgkmcnt(8)\n"
    "v_mfma_f32_16x16x32_bf16 v[72:75], a[48:51], a[240:243], v[72:75]\n"
    "ds_read_b64_tr_b16 a[8:9], v27 offset:17408\n"
    "v_mfma_f32_16x16x32_bf16 v[72:75], a[52:55], a[244:247], v[72:75]\n"
    "ds_read_b64_tr_b16 a[10:11], v27 offset:21504\n"
    "v_mfma_f32_16x16x32_bf16 v[104:107], a[52:55], a[252:255], v[104:107]\n"
    "ds_read_b64_tr_b16 a[12:13], v27 offset:25600\n"
    "v_mfma_f32_16x16x32_bf16 v[104:107], a[48:51], a[248:251], v[104:107]\n"
    "ds_read_b64_tr_b16 a[14:15], v27 offset:29696\n"
    "s_waitcnt lgkmcnt(8)\n"
    "v_mfma_f32_16x16x32_bf16 v[108:111], a[56:59], a[248:251], v[108:111]\n"
    "ds_read_b64_tr_b16 a[16:17], v26 offset:17920\n"
    "v_mfma_f32_16x16x32_bf16 v[108:111], a[60:63], a[252:255], v[108:111]\n"
    "ds_read_b64_tr_b16 a[18:19], v26 offset:22016\n"
    "v_mfma_f32_16x16x32_bf16 v[76:79], a[60:63], a[244:247], v[76:79]\n"
    "ds_read_b64_tr_b16 a[20:21], v26 offset:26112\n"
    "v_mfma_f32_16x16x32_bf16 v[76:79], a[56:59], a[240:243], v[76:79]\n"
    "ds_read_b64_tr_b16 a[22:23], v26 offset:30208\n"
    "s_waitcnt lgkmcnt(8)\n"
    "v_mfma_f32_16x16x32_bf16 v[80:83], a[0:3], a[240:243], v[80:83]\n"
    "ds_read_b64_tr_b16 a[24:25], v27 offset:17920\n"
    "v_mfma_f32_16x16x32_bf16 v[80:83], a[4:7], a[244:247], v[80:83]\n"
    "ds_read_b64_tr_b16 a[26:27], v27 offset:22016\n"
    "v_mfma_f32_16x16x32_bf16 v[112:115], a[4:7], a[252:255], v[112:115]\n"
    "ds_read_b64_tr_b16 a[28:29], v27 offset:26112\n"
    "v_mfma_f32_16x16x32_bf16 v[112:115], a[0:3], a[248:251], v[112:115]\n"
    "ds_read_b64_tr_b16 a[30:31], v27 offset:30208\n"
    "s_waitcnt lgkmcnt(8)\n"
    "v_mfma_f32_16x16x32_bf16 v[116:119], a[8:11], a[248:251], v[116:119]\n"
    "ds_read_b64_tr_b16 a[32:33], v22 offset:32768\n"
    "v_cvt_pk_bf16_f32 v72, v72, v73\n"
    "v_cvt_pk_bf16_f32 v73, v74, v75\n"
    "v_mfma_f32_16x16x32_bf16 v[116:119], a[12:15], a[252:255], v[116:119]\n"
    "ds_read_b64_tr_b16 a[34:35], v22 offset:36864\n"
    "v_cvt_pk_bf16_f32 v74, v76, v77\n"
    "v_cvt_pk_bf16_f32 v75, v78, v79\n"
    "v_mfma_f32_16x16x32_bf16 v[84:87], a[12:15], a[244:247], v[84:87]\n"
    "s_add_u32 s96, s52, 0x6000\n"
    "ds_read_b64_tr_b16 a[36:37], v22 offset:40960\n"
    "buffer_store_dwordx4 v[72:75], v39, s[24:27], s96 offen offset:0 sc1\n"
    "v_mfma_f32_16x16x32_bf16 v[84:87], a[8:11], a[240:243], v[84:87]\n"
    "ds_read_b64_tr_b16 a[38:39], v22 offset:45056\n"
    "v_cvt_pk_bf16_f32 v104, v104, v105\n"
    "v_cvt_pk_bf16_f32 v105, v106, v107\n"
    "s_waitcnt lgkmcnt(8)\n"
    "v_mfma_f32_16x16x32_bf16 v[88:91], a[16:19], a[240:243], v[88:91]\n"
    "ds_read_b64_tr_b16 a[40:41], v22 offset:16384\n"
    "v_cvt_pk_bf16_f32 v106, v108, v109\n"
    "v_cvt_pk_bf16_f32 v107, v110, v111\n"
    "v_mfma_f32_16x16x32_bf16 v[88:91], a[20:23], a[244:247], v[88:91]\n"
    "ds_read_b64_tr_b16 a[42:43], v22 offset:20480\n"
    "buffer_store_dwordx4 v[104:107], v39, s[24:27], s96 offen offset:1024 sc1\n"
    "v_mfma_f32_16x16x32_bf16 v[120:123], a[20:23], a[252:255], v[120:123]\n"
    "ds_read_b64_tr_b16 a[44:45], v22 offset:24576\n"
    "v_mfma_f32_16x16x32_bf16 v[120:123], a[16:19], a[248:251], v[120:123]\n"
    "ds_read_b64_tr_b16 a[46:47], v22 offset:28672\n"
    "s_waitcnt lgkmcnt(8)\n"
    "v_mfma_f32_16x16x32_bf16 v[124:127], a[24:27], a[248:251], v[124:127]\n"
    "ds_read_b64_tr_b16 a[48:49], v22 offset:49152\n"
    "v_cvt_pk_bf16_f32 v80, v80, v81\n"
    "v_cvt_pk_bf16_f32 v81, v82, v83\n"
    "v_mfma_f32_16x16x32_bf16 v[124:127], a[28:31], a[252:255], v[124:127]\n"
    "ds_read_b64_tr_b16 a[50:51], v22 offset:53248\n"
    "v_cvt_pk_bf16_f32 v82, v84, v85\n"
    "v_cvt_pk_bf16_f32 v83, v86, v87\n"
    "v_mfma_f32_16x16x32_bf16 v[92:95], a[28:31], a[244:247], v[92:95]\n"
    "s_add_u32 s96, s53, 0x6000\n"
    "ds_read_b64_tr_b16 a[52:53], v22 offset:57344\n"
    "buffer_store_dwordx4 v[80:83], v39, s[24:27], s96 offen offset:0 sc1\n"
    "v_mfma_f32_16x16x32_bf16 v[92:95], a[24:27], a[240:243], v[92:95]\n"
    "ds_read_b64_tr_b16 a[54:55], v22 offset:61440\n"
    "v_cvt_pk_bf16_f32 v112, v112, v113\n"
    "v_cvt_pk_bf16_f32 v113, v114, v115\n"
    "s_waitcnt lgkmcnt(8)\n"
    "v_mfma_f32_16x16x32_bf16 v[64:67], a[32:35], a[96:99], v[64:67]\n"
    "ds_read_b64_tr_b16 a[56:57], v24 offset:0\n"
    "v_cvt_pk_bf16_f32 v114, v116, v117\n"
    "v_cvt_pk_bf16_f32 v115, v118, v119\n"
    "v_mfma_f32_16x16x32_bf16 v[64:67], a[36:39], a[100:103], v[64:67]\n"
    "ds_read_b64_tr_b16 a[58:59], v24 offset:4096\n"
    "buffer_store_dwordx4 v[112:115], v39, s[24:27], s96 offen offset:1024 sc1\n"
    "v_mfma_f32_16x16x32_bf16 v[96:99], a[36:39], a[108:111], v[96:99]\n"
    "ds_read_b64_tr_b16 a[60:61], v24 offset:8192\n"
    "v_mfma_f32_16x16x32_bf16 v[96:99], a[32:35], a[104:107], v[96:99]\n"
    "ds_read_b64_tr_b16 a[62:63], v24 offset:12288\n"
    "s_waitcnt lgkmcnt(8)\n"
    "v_mfma_f32_16x16x32_bf16 v[96:99], a[40:43], a[120:123], v[96:99]\n"
    "ds_read_b64_tr_b16 a[0:1], v22 offset:0\n"
    "v_cvt_pk_bf16_f32 v88, v88, v89\n"
    "v_cvt_pk_bf16_f32 v89, v90, v91\n"
    "v_mfma_f32_16x16x32_bf16 v[96:99], a[44:47], a[124:127], v[96:99]\n"
    "ds_read_b64_tr_b16 a[2:3], v22 offset:4096\n"
    "v_cvt_pk_bf16_f32 v90, v92, v93\n"
    "v_cvt_pk_bf16_f32 v91, v94, v95\n"
    "v_mfma_f32_16x16x32_bf16 v[64:67], a[44:47], a[116:119], v[64:67]\n"
    "s_add_u32 s96, s54, 0x6000\n"
    "ds_read_b64_tr_b16 a[4:5], v22 offset:8192\n"
    "buffer_store_dwordx4 v[88:91], v39, s[24:27], s96 offen offset:0 sc1\n"
    "v_mfma_f32_16x16x32_bf16 v[64:67], a[40:43], a[112:115], v[64:67]\n"
    "ds_read_b64_tr_b16 a[6:7], v22 offset:12288\n"
    "v_cvt_pk_bf16_f32 v120, v120, v121\n"
    "v_cvt_pk_bf16_f32 v121, v122, v123\n"
    "s_waitcnt lgkmcnt(8)\n"
    "v_mfma_f32_16x16x32_bf16 v[64:67], a[48:51], a[128:131], v[64:67]\n"
    "ds_read_b64_tr_b16 a[8:9], v24 offset:16384\n"
    "v_cvt_pk_bf16_f32 v122, v124, v125\n"
    "v_cvt_pk_bf16_f32 v123, v126, v127\n"
    "v_mfma_f32_16x16x32_bf16 v[64:67], a[52:55], a[132:135], v[64:67]\n"
    "ds_read_b64_tr_b16 a[10:11], v24 offset:20480\n"
    "buffer_store_dwordx4 v[120:123], v39, s[24:27], s96 offen offset:1024 sc1\n"
    "v_mfma_f32_16x16x32_bf16 v[96:99], a[52:55], a[140:143], v[96:99]\n"
    "ds_read_b64_tr_b16 a[12:13], v24 offset:24576\n"
    "v_mfma_f32_16x16x32_bf16 v[96:99], a[48:51], a[136:139], v[96:99]\n"
    "ds_read_b64_tr_b16 a[14:15], v24 offset:28672\n"
    "s_waitcnt lgkmcnt(8)\n"
    "v_mfma_f32_16x16x32_bf16 v[96:99], a[56:59], a[152:155], v[96:99]\n"
    "ds_read_b64_tr_b16 a[16:17], v24 offset:32768\n"
    "v_mfma_f32_16x16x32_bf16 v[96:99], a[60:63], a[156:159], v[96:99]\n"
    "ds_read_b64_tr_b16 a[18:19], v24 offset:36864\n"
    "v_mfma_f32_16x16x32_bf16 v[64:67], a[60:63], a[148:151], v[64:67]\n"
    "ds_read_b64_tr_b16 a[20:21], v24 offset:40960\n"
    "v_mfma_f32_16x16x32_bf16 v[64:67], a[56:59], a[144:147], v[64:67]\n"
    "ds_read_b64_tr_b16 a[22:23], v24 offset:45056\n"
    "s_waitcnt lgkmcnt(8)\n"
    "v_mfma_f32_16x16x32_bf16 v[64:67], a[0:3], a[160:163], v[64:67]\n"
    "ds_read_b64_tr_b16 a[24:25], v24 offset:49152\n"
    "v_mfma_f32_16x16x32_bf16 v[64:67], a[4:7], a[164:167], v[64:67]\n"
    "ds_read_b64_tr_b16 a[26:27], v24 offset:53248\n"
    "v_mfma_f32_16x16x32_bf16 v[96:99], a[4:7], a[172:175], v[96:99]\n"
    "ds_read_b64_tr_b16 a[28:29], v24 offset:57344\n"
    "v_mfma_f32_16x16x32_bf16 v[96:99], a[0:3], a[168:171], v[96:99]\n"
    "ds_read_b64_tr_b16 a[30:31], v24 offset:61440\n"
    "s_waitcnt lgkmcnt(8)\n"
    "v_mfma_f32_16x16x32_bf16 v[96:99], a[8:11], a[184:187], v[96:99]\n"
    "ds_read_b64_tr_b16 a[32:33], v26 offset:0\n"
    "v_mfma_f32_16x16x32_bf16 v[96:99], a[12:15], a[188:191], v[96:99]\n"
    "ds_read_b64_tr_b16 a[34:35], v26 offset:4096\n"
    "v_mfma_f32_16x16x32_bf16 v[64:67], a[12:15], a[180:183], v[64:67]\n"
    "ds_read_b64_tr_b16 a[36:37], v26 offset:8192\n"
    "v_mfma_f32_16x16x32_bf16 v[64:67], a[8:11], a[176:179], v[64:67]\n"
    "ds_read_b64_tr_b16 a[38:39], v26 offset:12288\n"
    "s_waitcnt lgkmcnt(8)\n"
    "v_mfma_f32_16x16x32_bf16 v[64:67], a[16:19], a[192:195], v[64:67]\n"
    "ds_read_b64_tr_b16 a[40:41], v26 offset:16384\n"
    "v_mfma_f32_16x16x32_bf16 v[64:67], a[20:23], a[196:199], v[64:67]\n"
    "ds_read_b64_tr_b16 a[42:43], v26 offset:20480\n"
    "v_mfma_f32_16x16x32_bf16 v[96:99], a[20:23], a[204:207], v[96:99]\n"
    "ds_read_b64_tr_b16 a[44:45], v26 offset:24576\n"
    "v_mfma_f32_16x16x32_bf16 v[96:99], a[16:19], a[200:203], v[96:99]\n"
    "ds_read_b64_tr_b16 a[46:47], v26 offset:28672\n"
    "s_waitcnt lgkmcnt(8)\n"
    "v_mfma_f32_16x16x32_bf16 v[96:99], a[24:27], a[216:219], v[96:99]\n"
    "ds_read_b64_tr_b16 a[48:49], v23 offset:32768\n"
    "v_mfma_f32_16x16x32_bf16 v[96:99], a[28:31], a[220:223], v[96:99]\n"
    "ds_read_b64_tr_b16 a[50:51], v23 offset:36864\n"
    "v_mfma_f32_16x16x32_bf16 v[64:67], a[28:31], a[212:215], v[64:67]\n"
    "ds_read_b64_tr_b16 a[52:53], v23 offset:40960\n"
    "v_mfma_f32_16x16x32_bf16 v[64:67], a[24:27], a[208:211], v[64:67]\n"
    "ds_read_b64_tr_b16 a[54:55], v23 offset:45056\n"
    "s_waitcnt lgkmcnt(8)\n"
    "v_mfma_f32_16x16x32_bf16 v[64:67], a[32:35], a[224:227], v[64:67]\n"
    "ds_read_b64_tr_b16 a[56:57], v23 offset:16384\n"
    "v_mfma_f32_16x16x32_bf16 v[64:67], a[36:39], a[228:231], v[64:67]\n"
    "ds_read_b64_tr_b16 a[58:59], v23 offset:20480\n"
    "v_mfma_f32_16x16x32_bf16 v[96:99], a[36:39], a[236:239], v[96:99]\n"
    "ds_read_b64_tr_b16 a[60:61], v23 offset:24576\n"
    "v_mfma_f32_16x16x32_bf16 v[96:99], a[32:35], a[232:235], v[96:99]\n"
    "ds_read_b64_tr_b16 a[62:63], v23 offset:28672\n"
    "s_waitcnt lgkmcnt(8)\n"
    "v_mfma_f32_16x16x32_bf16 v[96:99], a[40:43], a[248:251], v[96:99]\n"
    "ds_read_b64_tr_b16 a[0:1], v23 offset:49152\n"
    "v_mfma_f32_16x16x32_bf16 v[96:99], a[44:47], a[252:255], v[96:99]\n"
    "ds_read_b64_tr_b16 a[2:3], v23 offset:53248\n"
    "v_mfma_f32_16x16x32_bf16 v[64:67], a[44:47], a[244:247], v[64:67]\n"
    "ds_read_b64_tr_b16 a[4:5], v23 offset:57344\n"
    "v_mfma_f32_16x16x32_bf16 v[64:67], a[40:43], a[240:243], v[64:67]\n"
    "ds_read_b64_tr_b16 a[6:7], v23 offset:61440\n"
    "s_waitcnt lgkmcnt(8)\n"
    "v_mfma_f32_16x16x32_bf16 v[68:71], a[48:51], a[96:99], v[68:71]\n"
    "ds_read_b64_tr_b16 a[8:9], v25 offset:0\n"
    "v_mfma_f32_16x16x32_bf16 v[68:71], a[52:55], a[100:103], v[68:71]\n"
    "ds_read_b64_tr_b16 a[10:11], v25 offset:4096\n"
    "v_mfma_f32_16x16x32_bf16 v[100:103], a[52:55], a[108:111], v[100:103]\n"
    "ds_read_b64_tr_b16 a[12:13], v25 offset:8192\n"
    "v_mfma_f32_16x16x32_bf16 v[100:103], a[48:51], a[104:107], v[100:103]\n"
    "ds_read_b64_tr_b16 a[14:15], v25 offset:12288\n"
    "s_waitcnt lgkmcnt(8)\n"
    "v_mfma_f32_16x16x32_bf16 v[100:103], a[56:59], a[120:123], v[100:103]\n"
    "ds_read_b64_tr_b16 a[16:17], v23 offset:0\n"
    "v_mfma_f32_16x16x32_bf16 v[100:103], a[60:63], a[124:127], v[100:103]\n"
    "ds_read_b64_tr_b16 a[18:19], v23 offset:4096\n"
    "v_mfma_f32_16x16x32_bf16 v[68:71], a[60:63], a[116:119], v[68:71]\n"
    "ds_read_b64_tr_b16 a[20:21], v23 offset:8192\n"
    "v_mfma_f32_16x16x32_bf16 v[68:71], a[56:59], a[112:115], v[68:71]\n"
    "ds_read_b64_tr_b16 a[22:23], v23 offset:12288\n"
    "s_waitcnt lgkmcnt(8)\n"
    "v_mfma_f32_16x16x32_bf16 v[68:71], a[0:3], a[128:131], v[68:71]\n"
    "ds_read_b64_tr_b16 a[24:25], v25 offset:16384\n"
    "v_mfma_f32_16x16x32_bf16 v[68:71], a[4:7], a[132:135], v[68:71]\n"
    "ds_read_b64_tr_b16 a[26:27], v25 offset:20480\n"
    "v_mfma_f32_16x16x32_bf16 v[100:103], a[4:7], a[140:143], v[100:103]\n"
    "ds_read_b64_tr_b16 a[28:29], v25 offset:24576\n"
    "v_mfma_f32_16x16x32_bf16 v[100:103], a[0:3], a[136:139], v[100:103]\n"
    "ds_read_b64_tr_b16 a[30:31], v25 offset:28672\n"
    "s_waitcnt lgkmcnt(8)\n"
    "v_mfma_f32_16x16x32_bf16 v[100:103], a[8:11], a[152:155], v[100:103]\n"
    "ds_read_b64_tr_b16 a[32:33], v25 offset:32768\n"
    "v_mfma_f32_16x16x32_bf16 v[100:103], a[12:15], a[156:159], v[100:103]\n"
    "ds_read_b64_tr_b16 a[34:35], v25 offset:36864\n"
    "v_mfma_f32_16x16x32_bf16 v[68:71], a[12:15], a[148:151], v[68:71]\n"
    "ds_read_b64_tr_b16 a[36:37], v25 offset:40960\n"
    "v_mfma_f32_16x16x32_bf16 v[68:71], a[8:11], a[144:147], v[68:71]\n"
    "ds_read_b64_tr_b16 a[38:39], v25 offset:45056\n"
    "s_waitcnt lgkmcnt(8)\n"
    "v_mfma_f32_16x16x32_bf16 v[68:71], a[16:19], a[160:163], v[68:71]\n"
    "ds_read_b64_tr_b16 a[40:41], v25 offset:49152\n"
    "v_mfma_f32_16x16x32_bf16 v[68:71], a[20:23], a[164:167], v[68:71]\n"
    "ds_read_b64_tr_b16 a[42:43], v25 offset:53248\n"
    "v_mfma_f32_16x16x32_bf16 v[100:103], a[20:23], a[172:175], v[100:103]\n"
    "ds_read_b64_tr_b16 a[44:45], v25 offset:57344\n"
    "v_mfma_f32_16x16x32_bf16 v[100:103], a[16:19], a[168:171], v[100:103]\n"
    "ds_read_b64_tr_b16 a[46:47], v25 offset:61440\n"
    "s_waitcnt lgkmcnt(8)\n"
    "v_mfma_f32_16x16x32_bf16 v[100:103], a[24:27], a[184:187], v[100:103]\n"
    "ds_read_b64_tr_b16 a[48:49], v27 offset:0\n"
    "v_mfma_f32_16x16x32_bf16 v[100:103], a[28:31], a[188:191], v[100:103]\n"
    "ds_read_b64_tr_b16 a[50:51], v27 offset:4096\n"
    "v_mfma_f32_16x16x32_bf16 v[68:71], a[28:31], a[180:183], v[68:71]\n"
    "ds_read_b64_tr_b16 a[52:53], v27 offset:8192\n"
    "v_mfma_f32_16x16x32_bf16 v[68:71], a[24:27], a[176:179], v[68:71]\n"
    "ds_read_b64_tr_b16 a[54:55], v27 offset:12288\n"
    "s_waitcnt lgkmcnt(8)\n"
    "v_mfma_f32_16x16x32_bf16 v[68:71], a[32:35], a[192:195], v[68:71]\n"
    "ds_read_b64_tr_b16 a[56:57], v27 offset:16384\n"
    "v_mfma_f32_16x16x32_bf16 v[68:71], a[36:39], a[196:199], v[68:71]\n"
    "ds_read_b64_tr_b16 a[58:59], v27 offset:20480\n"
    "v_mfma_f32_16x16x32_bf16 v[100:103], a[36:39], a[204:207], v[100:103]\n"
    "ds_read_b64_tr_b16 a[60:61], v27 offset:24576\n"
    "v_mfma_f32_16x16x32_bf16 v[100:103], a[32:35], a[200:203], v[100:103]\n"
    "ds_read_b64_tr_b16 a[62:63], v27 offset:28672\n"
    "s_waitcnt lgkmcnt(8)\n"
    "v_mfma_f32_16x16x32_bf16 v[100:103], a[40:43], a[216:219], v[100:103]\n"
    "s_add_u32 s68, s55, 0x6000\n"
    "v_mfma_f32_16x16x32_bf16 v[100:103], a[44:47], a[220:223], v[100:103]\n"
    "v_mfma_f32_16x16x32_bf16 v[68:71], a[44:47], a[212:215], v[68:71]\n"
    "buffer_load_dwordx4 v[76:79], v39, s[24:27], s68 offen offset:0 sc1\n"
    "v_mfma_f32_16x16x32_bf16 v[68:71], a[40:43], a[208:211], v[68:71]\n"
    "s_waitcnt lgkmcnt(4)\n"
    "v_mfma_f32_16x16x32_bf16 v[68:71], a[48:51], a[224:227], v[68:71]\n"
    "v_mfma_f32_16x16x32_bf16 v[68:71], a[52:55], a[228:231], v[68:71]\n"
    "v_mfma_f32_16x16x32_bf16 v[100:103], a[52:55], a[236:239], v[100:103]\n"
    "buffer_load_dwordx4 v[108:111], v39, s[24:27], s68 offen offset:1024 sc1\n"
    "v_mfma_f32_16x16x32_bf16 v[100:103], a[48:51], a[232:235], v[100:103]\n"
    "s_add_u32 s68, s55, 0x8000\n"
    "s_waitcnt lgkmcnt(0)\n"
    "v_mfma_f32_16x16x32_bf16 v[100:103], a[56:59], a[248:251], v[100:103]\n"
    "v_mfma_f32_16x16x32_bf16 v[100:103], a[60:63], a[252:255], v[100:103]\n"
    "v_mfma_f32_16x16x32_bf16 v[68:71], a[60:63], a[244:247], v[68:71]\n"
    "buffer_load_dwordx4 v[84:87], v39, s[24:27], s68 offen offset:0 sc1\n"
    "v_mfma_f32_16x16x32_bf16 v[68:71], a[56:59], a[240:243], v[68:71]\n"
    "buffer_load_dwordx4 v[116:119], v39, s[24:27], s68 offen offset:1024 sc1\n"
    "s_add_u32 s68, s55, 0xa000\n"
    "buffer_load_dwordx4 v[92:95], v39, s[24:27], s68 offen offset:0 sc1\n"
    "buffer_load_dwordx4 v[124:127], v39, s[24:27], s68 offen offset:1024 sc1\n"
    "s_waitcnt vmcnt(12)\n"
    "v_dot2_f32_bf16 v128, v140, s70, v128\n"
    "v_dot2_f32_bf16 v129, v140, s71, v129\n"
    "v_dot2_f32_bf16 v130, v141, s70, v130\n"
    "v_dot2_f32_bf16 v131, v141, s71, v131\n"
    "v_dot2_f32_bf16 v132, v142, s70, v132\n"
    "v_dot2_f32_bf16 v133, v142, s71, v133\n"
    "v_dot2_f32_bf16 v134, v143, s70, v134\n"
    "v_dot2_f32_bf16 v135, v143, s71, v135\n"
    "v_dot2_f32_bf16 v160, v172, s70, v160\n"
    "v_dot2_f32_bf16 v161, v172, s71, v161\n"
    "v_dot2_f32_bf16 v162, v173, s70, v162\n"
    "v_dot2_f32_bf16 v163, v173, s71, v163\n"
    "v_dot2_f32_bf16 v164, v174, s70, v164\n"
    "v_dot2_f32_bf16 v165, v174, s71, v165\n"
    "v_dot2_f32_bf16 v166, v175, s70, v166\n"
    "v_dot2_f32_bf16 v167, v175, s71, v167\n"
    "v_dot2_f32_bf16 v128, v148, s70, v128\n"
    "v_dot2_f32_bf16 v129, v148, s71, v129\n"
    "v_dot2_f32_bf16 v130, v149, s70, v130\n"
    "v_dot2_f32_bf16 v131, v149, s71, v131\n"
    "v_dot2_f32_bf16 v132, v150, s70, v132\n"
    "v_dot2_f32_bf16 v133, v150, s71, v133\n"
    "v_dot2_f32_bf16 v134, v151, s70, v134\n"
    "v_dot2_f32_bf16 v135, v151, s71, v135\n"
    "v_dot2_f32_bf16 v160, v180, s70, v160\n"
    "v_dot2_f32_bf16 v161, v180, s71, v161\n"
    "v_dot2_f32_bf16 v162, v181, s70, v162\n"
    "v_dot2_f32_bf16 v163, v181, s71, v163\n"
    "v_dot2_f32_bf16 v164, v182, s70, v164\n"
    "v_dot2_f32_bf16 v165, v182, s71, v165\n"
    "v_dot2_f32_bf16 v166, v183, s70, v166\n"
    "v_dot2_f32_bf16 v167, v183, s71, v167\n"
    "v_dot2_f32_bf16 v128, v156, s70, v128\n"
    "v_dot2_f32_bf16 v129, v156, s71, v129\n"
    "v_dot2_f32_bf16 v130, v157, s70, v130\n"
    "v_dot2_f32_bf16 v131, v157, s71, v131\n"
    "v_dot2_f32_bf16 v132, v158, s70, v132\n"
    "v_dot2_f32_bf16 v133, v158, s71, v133\n"
    "v_dot2_f32_bf16 v134, v159, s70, v134\n"
    "v_dot2_f32_bf16 v135, v159, s71, v135\n"
    "v_dot2_f32_bf16 v160, v188, s70, v160\n"
    "v_dot2_f32_bf16 v161, v188, s71, v161\n"
    "v_dot2_f32_bf16 v162, v189, s70, v162\n"
    "v_dot2_f32_bf16 v163, v189, s71, v163\n"
    "v_dot2_f32_bf16 v164, v190, s70, v164\n"
    "v_dot2_f32_bf16 v165, v190, s71, v165\n"
    "v_dot2_f32_bf16 v166, v191, s70, v166\n"
    "v_dot2_f32_bf16 v167, v191, s71, v167\n"
    "v_mul_f32 v192, 0xbfb8aa3b, v128\n"
    "v_mul_f32 v193, 0xbfb8aa3b, v129\n"
    "v_mul_f32 v194, 0xbfb8aa3b, v130\n"
    "v_mul_f32 v195, 0xbfb8aa3b, v131\n"
    "v_mul_f32 v196, 0xbfb8aa3b, v132\n"
    "v_mul_f32 v197, 0xbfb8aa3b, v133\n"
    "v_mul_f32 v198, 0xbfb8aa3b, v134\n"
    "v_mul_f32 v199, 0xbfb8aa3b, v135\n"
    "v_mul_f32 v200, 0xbfb8aa3b, v160\n"
    "v_mul_f32 v201, 0xbfb8aa3b, v161\n"
    "v_mul_f32 v202, 0xbfb8aa3b, v162\n"
    "v_mul_f32 v203, 0xbfb8aa3b, v163\n"
    "v_mul_f32 v204, 0xbfb8aa3b, v164\n"
    "v_mul_f32 v205, 0xbfb8aa3b, v165\n"
    "v_mul_f32 v206, 0xbfb8aa3b, v166\n"
    "v_mul_f32 v207, 0xbfb8aa3b, v167\n"
    "v_exp_f32 v192, v192\n"
    "v_exp_f32 v193, v193\n"
    "v_exp_f32 v194, v194\n"
    "v_exp_f32 v195, v195\n"
    "v_exp_f32 v196, v196\n"
    "v_exp_f32 v197, v197\n"
    "v_exp_f32 v198, v198\n"
    "v_exp_f32 v199, v199\n"
    "v_exp_f32 v200, v200\n"
    "v_exp_f32 v201, v201\n"
    "v_exp_f32 v202, v202\n"
    "v_exp_f32 v203, v203\n"
    "v_exp_f32 v204, v204\n"
    "v_exp_f32 v205, v205\n"
    "v_exp_f32 v206, v206\n"
    "v_exp_f32 v207, v207\n"
    "v_add_f32 v192, 1.0, v192\n"
    "v_add_f32 v193, 1.0, v193\n"
    "v_add_f32 v194, 1.0, v194\n"
    "v_add_f32 v195, 1.0, v195\n"
    "v_add_f32 v196, 1.0, v196\n"
    "v_add_f32 v197, 1.0, v197\n"
    "v_add_f32 v198, 1.0, v198\n"
    "v_add_f32 v199, 1.0, v199\n"
    "v_add_f32 v200, 1.0, v200\n"
    "v_add_f32 v201, 1.0, v201\n"
    "v_add_f32 v202, 1.0, v202\n"
    "v_add_f32 v203, 1.0, v203\n"
    "v_add_f32 v204, 1.0, v204\n"
    "v_add_f32 v205, 1.0, v205\n"
    "v_add_f32 v206, 1.0, v206\n"
    "v_add_f32 v207, 1.0, v207\n"
    "v_rcp_f32 v192, v192\n"
    "v_rcp_f32 v193, v193\n"
    "v_rcp_f32 v194, v194\n"
    "v_rcp_f32 v195, v195\n"
    "v_rcp_f32 v196, v196\n"
    "v_rcp_f32 v197, v197\n"
    "v_rcp_f32 v198, v198\n"
    "v_rcp_f32 v199, v199\n"
    "v_rcp_f32 v200, v200\n"
    "v_rcp_f32 v201, v201\n"
    "v_rcp_f32 v202, v202\n"
    "v_rcp_f32 v203, v203\n"
    "v_rcp_f32 v204, v204\n"
    "v_rcp_f32 v205, v205\n"
    "v_rcp_f32 v206, v206\n"
    "v_rcp_f32 v207, v207\n"
    "v_lshlrev_b32 v208, 16, v56\n"
    "v_and_b32 v209, 0xffff0000, v56\n"
    "v_lshlrev_b32 v210, 16, v57\n"
    "v_and_b32 v211, 0xffff0000, v57\n"
    "v_lshlrev_b32 v212, 16, v58\n"
    "v_and_b32 v213, 0xffff0000, v58\n"
    "v_lshlrev_b32 v214, 16, v59\n"
    "v_and_b32 v215, 0xffff0000, v59\n"
    "v_lshlrev_b32 v216, 16, v60\n"
    "v_and_b32 v217, 0xffff0000, v60\n"
    "v_lshlrev_b32 v218, 16, v61\n"
    "v_and_b32 v219, 0xffff0000, v61\n"
    "v_lshlrev_b32 v220, 16, v62\n"
    "v_and_b32 v221, 0xffff0000, v62\n"
    "v_lshlrev_b32 v222, 16, v63\n"
    "v_and_b32 v223, 0xffff0000, v63\n"
    "v_mul_f32 v192, v128, v192\n"
    "v_mul_f32 v193, v129, v193\n"
    "v_mul_f32 v194, v130, v194\n"
    "v_mul_f32 v195, v131, v195\n"
    "v_mul_f32 v196, v132, v196\n"
    "v_mul_f32 v197, v133, v197\n"
    "v_mul_f32 v198, v134, v198\n"
    "v_mul_f32 v199, v135, v199\n"
    "v_mul_f32 v200, v160, v200\n"
    "v_mul_f32 v201, v161, v201\n"
    "v_mul_f32 v202, v162, v202\n"
    "v_mul_f32 v203, v163, v203\n"
    "v_mul_f32 v204, v164, v204\n"
    "v_mul_f32 v205, v165, v205\n"
    "v_mul_f32 v206, v166, v206\n"
    "v_mul_f32 v207, v167, v207\n"
    "s_mov_b32 s86, 0\n"
    "L_xs%=:\n"
    "s_waitcnt vmcnt(0)\n"
    "v_max3_u32 v40, v76, v77, v78\n"
    "v_max3_u32 v41, v79, v108, v109\n"
    "v_max3_u32 v40, v40, v110, v111\n"
    "v_max3_u32 v41, v41, v84, v85\n"
    "v_max3_u32 v40, v40, v86, v87\n"
    "v_max3_u32 v41, v41, v116, v117\n"
    "v_max3_u32 v40, v40, v118, v119\n"
    "v_max3_u32 v41, v41, v92, v93\n"
    "v_max3_u32 v40, v40, v94, v95\n"
    "v_max3_u32 v41, v41, v124, v125\n"
    "v_max3_u32 v40, v40, v126, v127\n"
    "v_max_u32 v40, v40, v41\n"
    "v_cmp_eq_u32 vcc, -1, v40\n"
    "s_cbranch_vccz L_xsok%=\n"
    "s_add_u32 s68, s55, 0x6000\n"
    "buffer_load_dwordx4 v[76:79], v39, s[24:27], s68 offen offset:0 sc1\n"
    "buffer_load_dwordx4 v[108:111], v39, s[24:27], s68 offen offset:1024 sc1\n"
    "s_add_u32 s68, s55, 0x8000\n"
    "buffer_load_dwordx4 v[84:87], v39, s[24:27], s68 offen offset:0 sc1\n"
    "buffer_load_dwordx4 v[116:119], v39, s[24:27], s68 offen offset:1024 sc1\n"
    "s_add_u32 s68, s55, 0xa000\n"
    "buffer_load_dwordx4 v[92:95], v39, s[24:27], s68 offen offset:0 sc1\n"
    "buffer_load_dwordx4 v[124:127], v39, s[24:27], s68 offen offset:1024 sc1\n"
    "s_add_u32 s86, s86, 1\n"
    "s_cmp_lt_u32 s86, 0x80000\n"
    "s_cbranch_scc1 L_xs%=\n"
    "s_waitcnt vmcnt(0)\n"
    "L_xsok%=:\n"
    "v_dot2_f32_bf16 v64, v76, s70, v64\n"
    "v_dot2_f32_bf16 v65, v76, s71, v65\n"
    "v_dot2_f32_bf16 v66, v77, s70, v66\n"
    "v_dot2_f32_bf16 v67, v77, s71, v67\n"
    "v_dot2_f32_bf16 v68, v78, s70, v68\n"
    "v_dot2_f32_bf16 v69, v78, s71, v69\n"
    "v_dot2_f32_bf16 v70, v79, s70, v70\n"
    "v_dot2_f32_bf16 v71, v79, s71, v71\n"
    "v_dot2_f32_bf16 v96, v108, s70, v96\n"
    "v_dot2_f32_bf16 v97, v108, s71, v97\n"
    "v_dot2_f32_bf16 v98, v109, s70, v98\n"
    "v_dot2_f32_bf16 v99, v109, s71, v99\n"
    "v_dot2_f32_bf16 v100, v110, s70, v100\n"
    "v_dot2_f32_bf16 v101, v110, s71, v101\n"
    "v_dot2_f32_bf16 v102, v111, s70, v102\n"
    "v_dot2_f32_bf16 v103, v111, s71, v103\n"
    "v_dot2_f32_bf16 v64, v84, s70, v64\n"
    "v_dot2_f32_bf16 v65, v84, s71, v65\n"
    "v_dot2_f32_bf16 v66, v85, s70, v66\n"
    "v_dot2_f32_bf16 v67, v85, s71, v67\n"
    "v_dot2_f32_bf16 v68, v86, s70, v68\n"
    "v_dot2_f32_bf16 v69, v86, s71, v69\n"
    "v_dot2_f32_bf16 v70, v87, s70, v70\n"
    "v_dot2_f32_bf16 v71, v87, s71, v71\n"
    "v_dot2_f32_bf16 v96, v116, s70, v96\n"
    "v_dot2_f32_bf16 v97, v116, s71, v97\n"
    "v_dot2_f32_bf16 v98, v117, s70, v98\n"
    "v_dot2_f32_bf16 v99, v117, s71, v99\n"
    "v_dot2_f32_bf16 v100, v118, s70, v100\n"
    "v_dot2_f32_bf16 v101, v118, s71, v101\n"
    "v_dot2_f32_bf16 v102, v119, s70, v102\n"
    "v_dot2_f32_bf16 v103, v119, s71, v103\n"
    "v_dot2_f32_bf16 v64, v92, s70, v64\n"
    "v_dot2_f32_bf16 v65, v92, s71, v65\n"
    "v_dot2_f32_bf16 v66, v93, s70, v66\n"
    "v_dot2_f32_bf16 v67, v93, s71, v67\n"
    "v_dot2_f32_bf16 v68, v94, s70, v68\n"
    "v_dot2_f32_bf16 v69, v94, s71, v69\n"
    "v_dot2_f32_bf16 v70, v95, s70, v70\n"
    "v_dot2_f32_bf16 v71, v95, s71, v71\n"
    "v_dot2_f32_bf16 v96, v124, s70, v96\n"
    "v_dot2_f32_bf16 v97, v124, s71, v97\n"
    "v_dot2_f32_bf16 v98, v125, s70, v98\n"
    "v_dot2_f32_bf16 v99, v125, s71, v99\n"
    "v_dot2_f32_bf16 v100, v126, s70, v100\n"
    "v_dot2_f32_bf16 v101, v126, s71, v101\n"
    "v_dot2_f32_bf16 v102, v127, s70, v102\n"
    "v_dot2_f32_bf16 v103, v127, s71, v103\n"
    "v_fmac_f32 v208, v64, v192\n"
    "v_fmac_f32 v209, v65, v193\n"
    "v_fmac_f32 v210, v66, v194\n"
    "v_fmac_f32 v211, v67, v195\n"
    "v_fmac_f32 v212, v68, v196\n"
    "v_fmac_f32 v213, v69, v197\n"
    "v_fmac_f32 v214, v70, v198\n"
    "v_fmac_f32 v215, v71, v199\n"
    "v_fmac_f32 v216, v96, v200\n"
    "v_fmac_f32 v217, v97, v201\n"
    "v_fmac_f32 v218, v98, v202\n"
    "v_fmac_f32 v219, v99, v203\n"
    "v_fmac_f32 v220, v100, v204\n"
    "v_fmac_f32 v221, v101, v205\n"
    "v_fmac_f32 v222, v102, v206\n"
    "v_fmac_f32 v223, v103, v207\n"
    "v_cvt_pk_bf16_f32 v56, v208, v209\n"
    "v_cvt_pk_bf16_f32 v57, v210, v211\n"
    "buffer_store_dwordx2 v[56:57], v38, s[36:39], s56 offen offset:0\n"
    "v_cvt_pk_bf16_f32 v58, v212, v213\n"
    "v_cvt_pk_bf16_f32 v59, v214, v215\n"
    "buffer_store_dwordx2 v[58:59], v38, s[36:39], s56 offen offset:32\n"
    "v_cvt_pk_bf16_f32 v60, v216, v217\n"
    "v_cvt_pk_bf16_f32 v61, v218, v219\n"
    "buffer_store_dwordx2 v[60:61], v38, s[36:39], s98 offen offset:0\n"
    "v_cvt_pk_bf16_f32 v62, v220, v221\n"
    "v_cvt_pk_bf16_f32 v63, v222, v223\n"
    "buffer_store_dwordx2 v[62:63], v38, s[36:39], s98 offen offset:32\n"
    "s_branch L_end%=\n"
    "L_fb%=:\n"
    "s_mov_b32 s74, 1\n"
    "s_waitcnt vmcnt(0) lgkmcnt(0)\n"
    "s_barrier\n"
    "s_nop 7\n"
    "s_nop 7\n"
    "s_add_u32 m0, s65, 0\n"
    "s_mov_b32 s69, 0\n"
    "buffer_load_dwordx4 v10, s[8:11], s69 offen offset:0 lds\n"
    "buffer_load_dwordx4 v11, s[8:11], s69 offen offset:1024 lds\n"
    "buffer_load_dwordx4 v12, s[8:11], s69 offen offset:2048 lds\n"
    "buffer_load_dwordx4 v13, s[8:11], s69 offen offset:3072 lds\n"
    "s_add_u32 m0, s65, 0x8000\n"
    "s_mov_b32 s69, 0\n"
    "buffer_load_dwordx4 v14, s[12:15], s69 offen offset:0 lds\n"
    "buffer_load_dwordx4 v15, s[12:15], s69 offen offset:1024 lds\n"
    "buffer_load_dwordx4 v14, s[12:15], s69 offen offset:2048 lds\n"
    "buffer_load_dwordx4 v15, s[12:15], s69 offen offset:3072 lds\n"
    "s_add_u32 m0, s65, 0x4000\n"
    "s_mov_b32 s69, 0x4000\n"
    "buffer_load_dwordx4 v10, s[8:11], s69 offen offset:0 lds\n"
    "buffer_load_dwordx4 v11, s[8:11], s69 offen offset:1024 lds\n"
    "buffer_load_dwordx4 v12, s[8:11], s69 offen offset:2048 lds\n"
    "buffer_load_dwordx4 v13, s[8:11], s69 offen offset:3072 lds\n"
    "s_add_u32 m0, s65, 0xc000\n"
    "s_mov_b32 s69, 0x4000\n"
    "buffer_load_dwordx4 v14, s[12:15], s69 offen offset:0 lds\n"
    "buffer_load_dwordx4 v15, s[12:15], s69 offen offset:1024 lds\n"
    "buffer_load_dwordx4 v14, s[12:15], s69 offen offset:2048 lds\n"
    "buffer_load_dwordx4 v15, s[12:15], s69 offen offset:3072 lds\n"
    "v_mov_b32 v104, 0xff7fffff\n"
    "v_mov_b32 v105, 0xff7fffff\n"
    "v_mov_b32 v106, 0xff7fffff\n"
    "v_mov_b32 v107, 0xff7fffff\n"
    "v_and_b32 v101, 15, v1\n"
    "v_lshrrev_b32 v102, 4, v1\n"
    "v_lshlrev_b32 v102, 4, v102\n"
    "v_lshl_add_u32 v100, v101, 8, v102\n"
    "s_mov_b32 s86, 0\n"
    "L_mx%=:\n"
    "buffer_load_dwordx4 a[192:195], v100, s[8:11], s86 offen offset:0\n"
    "buffer_load_dwordx4 a[196:199], v100, s[8:11], s86 offen offset:64\n"
    "buffer_load_dwordx4 a[200:203], v100, s[8:11], s86 offen offset:128\n"
    "buffer_load_dwordx4 a[204:207], v100, s[8:11], s86 offen offset:192\n"
    "s_waitcnt vmcnt(0)\n"
    "v_mfma_f32_16x16x32_bf16 v[80:83], a[192:195], a[128:131], 0\n"
    "v_mfma_f32_16x16x32_bf16 v[80:83], a[196:199], a[144:147], v[80:83]\n"
    "v_mfma_f32_16x16x32_bf16 v[80:83], a[200:203], a[176:179], v[80:83]\n"
    "v_mfma_f32_16x16x32_bf16 v[80:83], a[204:207], v[56:59], v[80:83]\n"
    "v_mfma_f32_16x16x32_bf16 v[84:87], a[192:195], a[132:135], 0\n"
    "v_mfma_f32_16x16x32_bf16 v[84:87], a[196:199], a[148:151], v[84:87]\n"
    "v_mfma_f32_16x16x32_bf16 v[84:87], a[200:203], a[180:183], v[84:87]\n"
    "v_mfma_f32_16x16x32_bf16 v[84:87], a[204:207], v[60:63], v[84:87]\n"
    "v_mfma_f32_16x16x32_bf16 v[88:91], a[192:195], a[136:139], 0\n"
    "v_mfma_f32_16x16x32_bf16 v[88:91], a[196:199], a[152:155], v[88:91]\n"
    "v_mfma_f32_16x16x32_bf16 v[88:91], a[200:203], a[184:187], v[88:91]\n"
    "v_mfma_f32_16x16x32_bf16 v[88:91], a[204:207], v[124:127], v[88:91]\n"
    "v_mfma_f32_16x16x32_bf16 v[92:95], a[192:195], a[140:143], 0\n"
    "v_mfma_f32_16x16x32_bf16 v[92:95], a[196:199], a[156:159], v[92:95]\n"
    "v_mfma_f32_16x16x32_bf16 v[92:95], a[200:203], a[188:191], v[92:95]\n"
    "v_mfma_f32_16x16x32_bf16 v[92:95], a[204:207], v[28:31], v[92:95]\n"
    "s_nop 7\n"
    "v_max3_f32 v104, v104, v80, v81\n"
    "v_max3_f32 v104, v104, v82, v83\n"
    "v_max3_f32 v105, v105, v84, v85\n"
    "v_max3_f32 v105, v105, v86, v87\n"
    "v_max3_f32 v106, v106, v88, v89\n"
    "v_max3_f32 v106, v106, v90, v91\n"
    "v_max3_f32 v107, v107, v92, v93\n"
    "v_max3_f32 v107, v107, v94, v95\n"
    "s_add_u32 s86, s86, 0x1000\n"
    "s_cmp_lt_u32 s86, 0x80000\n"
    "s_cbranch_scc1 L_mx%=\n"
    "s_nop 1\n"
    "v_mov_b32 v103, v104\n"
    "s_nop 1\n"
    "v_permlane16_swap_b32 v104, v103\n"
    "v_max_f32 v104, v104, v103\n"
    "v_mov_b32 v103, v104\n"
    "s_nop 1\n"
    "v_permlane32_swap_b32 v104, v103\n"
    "v_max_f32 v104, v104, v103\n"
    "v_sub_f32 v80, 0, v104\n"
    "v_sub_f32 v81, 0, v104\n"
    "v_sub_f32 v82, 0, v104\n"
    "v_sub_f32 v83, 0, v104\n"
    "v_mov_b32 v103, v105\n"
    "s_nop 1\n"
    "v_permlane16_swap_b32 v105, v103\n"
    "v_max_f32 v105, v105, v103\n"
    "v_mov_b32 v103, v105\n"
    "s_nop 1\n"
    "v_permlane32_swap_b32 v105, v103\n"
    "v_max_f32 v105, v105, v103\n"
    "v_sub_f32 v84, 0, v105\n"
    "v_sub_f32 v85, 0, v105\n"
    "v_sub_f32 v86, 0, v105\n"
    "v_sub_f32 v87, 0, v105\n"
    "v_mov_b32 v103, v106\n"
    "s_nop 1\n"
    "v_permlane16_swap_b32 v106, v103\n"
    "v_max_f32 v106, v106, v103\n"
    "v_mov_b32 v103, v106\n"
    "s_nop 1\n"
    "v_permlane32_swap_b32 v106, v103\n"
    "v_max_f32 v106, v106, v103\n"
    "v_sub_f32 v88, 0, v106\n"
    "v_sub_f32 v89, 0, v106\n"
    "v_sub_f32 v90, 0, v106\n"
    "v_sub_f32 v91, 0, v106\n"
    "v_mov_b32 v103, v107\n"
    "s_nop 1\n"
    "v_permlane16_swap_b32 v107, v103\n"
    "v_max_f32 v107, v107, v103\n"
    "v_mov_b32 v103, v107\n"
    "s_nop 1\n"
    "v_permlane32_swap_b32 v107, v103\n"
    "v_max_f32 v107, v107, v103\n"
    "v_sub_f32 v92, 0, v107\n"
    "v_sub_f32 v93, 0, v107\n"
    "v_sub_f32 v94, 0, v107\n"
    "v_sub_f32 v95, 0, v107\n"
    "s_barrier\n"
    "s_add_u32 m0, s65, 0x10000\n"
    "s_mov_b32 s69, 0x8000\n"
    "buffer_load_dwordx4 v10, s[8:11], s69 offen offset:0 lds\n"
    "buffer_load_dwordx4 v11, s[8:11], s69 offen offset:1024 lds\n"
    "buffer_load_dwordx4 v12, s[8:11], s69 offen offset:2048 lds\n"
    "buffer_load_dwordx4 v13, s[8:11], s69 offen offset:3072 lds\n"
    "ds_read_b128 a[192:195], v4 offset:0\n"
    "ds_read_b128 a[196:199], v5 offset:0\n"
    "ds_read_b128 a[200:203], v6 offset:0\n"
    "ds_read_b128 a[204:207], v7 offset:0\n"
    "ds_read_b128 a[208:211], v4 offset:4096\n"
    "ds_read_b128 a[212:215], v5 offset:4096\n"
    "ds_read_b128 a[216:219], v6 offset:4096\n"
    "ds_read_b128 a[220:223], v7 offset:4096\n"
    "v_and_b32 v96, 3, v1\n"
    "v_cmp_eq_u32 vcc, 0, v96\n"
    "v_mov_b32 v97, 0x3f803f80\n"
    "s_add_u32 s66, s65, 0x10000\n"
    "s_mov_b32 s67, 0x8000\n"
    "s_mov_b32 s99, 0x7060302\n"
    "v_cndmask_b32 v120, 0, v97, vcc\n"
    "v_cndmask_b32 v121, 0, v97, vcc\n"
    "v_cndmask_b32 v122, 0, v97, vcc\n"
    "v_cndmask_b32 v123, 0, v97, vcc\n"
    "s_branch L_go%=\n"
    "L_end%=:\n"
    : [tid] "+v"(tid), "+{s[0:1]}"(ka), "+{s2}"(bid), "+{s3}"(lb)
    :
    : "v1", "v2", "v3", "v4", "v5", "v6", "v7", "v8", "v9", "v10", "v11", "v12", "v13", "v14", "v15", "v16", "v17", "v18", "v19", "v20", "v21", "v22", "v23", "v24",
      "v25", "v26", "v27", "v28", "v29", "v30", "v31", "v32", "v33", "v34", "v35", "v36", "v37", "v38", "v39", "v40", "v41", "v42", "v43", "v44", "v45", "v46",
      "v47", "v48", "v49", "v50", "v51", "v52", "v53", "v54", "v55", "v56", "v57", "v58", "v59", "v60", "v61", "v62", "v63", "v64", "v65", "v66", "v67", "v68",
      "v69", "v70", "v71", "v72", "v73", "v74", "v75", "v76", "v77", "v78", "v79", "v80", "v81", "v82", "v83", "v84", "v85", "v86", "v87", "v88", "v89", "v90",
      "v91", "v92", "v93", "v94", "v95", "v96", "v97", "v98", "v99", "v100", "v101", "v102", "v103", "v104", "v105", "v106", "v107", "v108", "v109", "v110", "v111",
      "v112", "v113", "v114", "v115", "v116", "v117", "v118", "v119", "v120", "v121", "v122", "v123", "v124", "v125", "v126", "v127", "v128", "v129", "v130", "v131",
      "v132", "v133", "v134", "v135", "v136", "v137", "v138", "v139", "v140", "v141", "v142", "v143", "v144", "v145", "v146", "v147", "v148", "v149", "v150", "v151",
      "v152", "v153", "v154", "v155", "v156", "v157", "v158", "v159", "v160", "v161", "v162", "v163", "v164", "v165", "v166", "v167", "v168", "v169", "v170", "v171",
      "v172", "v173", "v174", "v175", "v176", "v177", "v178", "v179", "v180", "v181", "v182", "v183", "v184", "v185", "v186", "v187", "v188", "v189", "v190", "v191",
      "v192", "v193", "v194", "v195", "v196", "v197", "v198", "v199", "v200", "v201", "v202", "v203", "v204", "v205", "v206", "v207", "v208", "v209", "v210", "v211",
      "v212", "v213", "v214", "v215", "v216", "v217", "v218", "v219", "v220", "v221", "v222", "v223", "v224", "v225", "v226", "v227", "v228", "v229", "v230", "v231",
      "v232", "v233", "v234", "v235", "v236", "v237", "v238", "v239", "v240", "v241", "v242", "v243", "v244", "v245", "v246", "v247", "v248", "v249", "v250", "v251",
      "v252", "v253", "v254", "v255", "a0", "a1", "a2", "a3", "a4", "a5", "a6", "a7", "a8", "a9", "a10", "a11", "a12", "a13", "a14", "a15", "a16", "a17", "a18",
      "a19", "a20", "a21", "a22", "a23", "a24", "a25", "a26", "a27", "a28", "a29", "a30", "a31", "a32", "a33", "a34", "a35", "a36", "a37", "a38", "a39", "a40",
      "a41", "a42", "a43", "a44", "a45", "a46", "a47", "a48", "a49", "a50", "a51", "a52", "a53", "a54", "a55", "a56", "a57", "a58", "a59", "a60", "a61", "a62",
      "a63", "a64", "a65", "a66", "a67", "a68", "a69", "a70", "a71", "a72", "a73", "a74", "a75", "a76", "a77", "a78", "a79", "a80", "a81", "a82", "a83", "a84",
      "a85", "a86", "a87", "a88", "a89", "a90", "a91", "a92", "a93", "a94", "a95", "a96", "a97", "a98", "a99", "a100", "a101", "a102", "a103", "a104", "a105",
      "a106", "a107", "a108", "a109", "a110", "a111", "a112", "a113", "a114", "a115", "a116", "a117", "a118", "a119", "a120", "a121", "a122", "a123", "a124", "a125",
      "a126", "a127", "a128", "a129", "a130", "a131", "a132", "a133", "a134", "a135", "a136", "a137", "a138", "a139", "a140", "a141", "a142", "a143", "a144", "a145",
      "a146", "a147", "a148", "a149", "a150", "a151", "a152", "a153", "a154", "a155", "a156", "a157", "a158", "a159", "a160", "a161", "a162", "a163", "a164", "a165",
      "a166", "a167", "a168", "a169", "a170", "a171", "a172", "a173", "a174", "a175", "a176", "a177", "a178", "a179", "a180", "a181", "a182", "a183", "a184", "a185",
      "a186", "a187", "a188", "a189", "a190", "a191", "a192", "a193", "a194", "a195", "a196", "a197", "a198", "a199", "a200", "a201", "a202", "a203", "a204", "a205",
      "a206", "a207", "a208", "a209", "a210", "a211", "a212", "a213", "a214", "a215", "a216", "a217", "a218", "a219", "a220", "a221", "a222", "a223", "a224", "a225",
      "a226", "a227", "a228", "a229", "a230", "a231", "a232", "a233", "a234", "a235", "a236", "a237", "a238", "a239", "a240", "a241", "a242", "a243", "a244", "a245",
      "a246", "a247", "a248", "a249", "a250", "a251", "a252", "a253", "a254", "a255", "s4", "s5", "s6", "s7", "s8", "s9", "s10", "s11", "s12", "s13", "s14", "s15",
      "s16", "s17", "s18", "s19", "s20", "s21", "s22", "s23", "s24", "s25", "s26", "s27", "s28", "s29", "s30", "s31", "s33", "s34", "s35", "s36", "s37", "s38",
      "s39", "s40", "s41", "s42", "s43", "s44", "s45", "s46", "s47", "s48", "s49", "s50", "s51", "s52", "s53", "s54", "s55", "s56", "s57", "s58", "s59", "s60",
      "s61", "s62", "s63", "s64", "s65", "s66", "s67", "s68", "s69", "s70", "s71", "s72", "s73", "s74", "s75", "s76", "s77", "s78", "s79", "s80", "s81", "s82",
      "s83", "s84", "s85", "s86", "s87", "s88", "s89", "s90", "s91", "s92", "s93", "s94", "s95", "s96", "s97", "s98", "s99" , "vcc", "scc", "memory");
}

extern "C" void kernel_launch(void* const* d_in, const int* in_sizes, int n_in, void* d_out, int out_size, void* d_ws, size_t ws_size, hipStream_t stream) {
  fused_block<<<256, 256, 0, stream>>>((const char*)d_in[0], (const char*)d_in[1], (const char*)d_in[2], (const char*)d_in[3], (const char*)d_in[4],
                                       (const char*)d_in[6], (const char*)d_in[7], (const char*)d_in[8], (char*)d_ws, (char*)d_out);
}
```
